```python
import torch

def _softmax(x, axis=-1):
    return torch.softmax(x, dim=axis)

def _gelu(x):
    return torch.nn.functional.gelu(x, approximate='tanh')

def _silu(x):
    return torch.nn.functional.silu(x)

def _sigmoid(x):
    return torch.sigmoid(x)

def _layernorm(x, axis=-1, eps=1e-05):
    m = x.mean(dim=axis, keepdim=True)
    v = x.var(dim=axis, keepdim=True, unbiased=False)
    return (x - m) / torch.sqrt(v + eps)

def _scatter_add(tgt, idx, src, axis=0):
    return tgt.clone().index_add_(axis, idx, src)

def _segment_sum(src, seg, n):
    out = torch.zeros((n,) + src.shape[1:], dtype=src.dtype)
    return out.index_add_(0, seg, src)

def _pad(x, pw):
    flat = tuple((v for pair in reversed(pw) for v in pair))
    return torch.nn.functional.pad(x, flat)
kernel_name = 'standalone__tree_speculative_verify_tree_attention_over_candidate_token_tree__sampling'

def setup_inputs():

    _w = lambda a: (lambda t: t.to(torch.bfloat16) if t.is_floating_point() else t)(torch.as_tensor(a))
    B, Hq, Hkv, T, C, D = (8, 16, 8, 16, 8192, 256)
    tree_mask = torch.zeros((B, T, T), dtype=float)
    for b in range(B):
        parent = torch.zeros(T, dtype=torch.int64)
        for i in range(1, T):
            parent[i] = int(torch.randint(0, i, (1,)).item())
        for i in range(T):
            j = i
            tree_mask[b, i, j] = 1.0
            while j > 0:
                j = parent[j]
                tree_mask[b, i, j] = 1.0
    return {'q': _w((lambda *s: torch.randn(*s, dtype=torch.bfloat16) * 0.8)(B, Hq, T, D)), 'k_cache': _w((lambda *s: torch.randn(*s, dtype=torch.bfloat16) * 0.8)(B, Hkv, C, D)), 'v_cache': _w((lambda *s: torch.randn(*s, dtype=torch.bfloat16) * 0.05)(B, Hkv, C, D)), 'k_tree': _w((lambda *s: torch.randn(*s, dtype=torch.bfloat16) * 0.8)(B, Hkv, T, D)), 'v_tree': _w((lambda *s: torch.randn(*s, dtype=torch.bfloat16) * 0.05)(B, Hkv, T, D)), 'tree_mask': _w(tree_mask)}

def reference(q, k_cache, v_cache, k_tree, v_tree, tree_mask):
    B, Hq, T, D = q.shape
    Hkv, C = (k_cache.shape[1], k_cache.shape[2])
    rep = Hq // Hkv
    kc = (lambda x, n, axis: torch.repeat_interleave(x, n, dim=axis))(k_cache, rep, axis=1)
    vc = (lambda x, n, axis: torch.repeat_interleave(x, n, dim=axis))(v_cache, rep, axis=1)
    kt = (lambda x, n, axis: torch.repeat_interleave(x, n, dim=axis))(k_tree, rep, axis=1)
    vt = (lambda x, n, axis: torch.repeat_interleave(x, n, dim=axis))(v_tree, rep, axis=1)
    k = torch.concatenate([kc, kt], axis=2)
    v = torch.concatenate([vc, vt], axis=2)
    scale = D ** (-0.5)
    scores = torch.einsum('bhqd,bhkd->bhqk', q, k) * scale
    ctx_mask = torch.ones((B, 1, T, C), dtype=scores.dtype)
    tm = tree_mask[:, None, :, :].clone()
    full_mask = torch.concatenate([ctx_mask, tm], axis=-1)
    scores = scores + (1.0 - full_mask) * -1000000000.0
    p = _softmax(scores, axis=-1)
    return torch.einsum('bhqk,bhkd->bhqd', p, v)
```

```cpp
#include <hip/hip_runtime.h>
#include <hip/hip_bf16.h>

#define TA_W4 2
#define TA_RUN 4

namespace ta {

constexpr int NB = 8, HKV = 8, CTX = 8192, HD = 256;
constexpr int NPAIR = NB * HKV, NSPLIT = 4, NW = 4;
constexpr int ROWB = HD * 2;
constexpr int WG_BYTES = CTX / NSPLIT * ROWB;
constexpr int TILES = CTX / NSPLIT / (32 * NW);
constexpr int KP = 1040, VP = 1088;
constexpr int KSLOT = 16 * KP, VSLOT = 16 * VP, WAVE_LDS = KSLOT + VSLOT;
constexpr int TREE_K = NW * WAVE_LDS, TREE_V = TREE_K + 8 * KP, TREE_M = TREE_V + 8 * VP, ML_OFF = TREE_M + 1024;
constexpr int LDS_BYTES = ML_OFF + NW * 32 * 8;
constexpr float SCALE = 0.0625f, LOG2E = 1.4426950408889634f, C2 = SCALE * LOG2E;
constexpr int W4 = TA_W4;
constexpr size_t PO_BYTES = (size_t)NPAIR * NSPLIT * 32 * HD * 4;

typedef __bf16 bf16x8 __attribute__((ext_vector_type(8)));
typedef __bf16 bf16x2 __attribute__((ext_vector_type(2)));
typedef short s16x4 __attribute__((ext_vector_type(4)));
typedef short s16x8 __attribute__((ext_vector_type(8)));
typedef float f32x16 __attribute__((ext_vector_type(16)));
typedef float f32x4 __attribute__((ext_vector_type(4)));
typedef float f32x2 __attribute__((ext_vector_type(2)));
typedef unsigned u32x4 __attribute__((ext_vector_type(4)));
typedef unsigned u32x2 __attribute__((ext_vector_type(2)));
#define LAS __attribute__((address_space(3)))
typedef LAS const char* lds_cptr;
typedef __amdgpu_buffer_rsrc_t srd_t;

#define MFMA32(a, b, c) __builtin_amdgcn_mfma_f32_32x32x16_bf16((a), (b), (c), 0, 0, 0)

__device__ __forceinline__ srd_t make_srd(const void* p, int bytes) {
  return __builtin_amdgcn_make_buffer_rsrc((void*)p, (short)0, bytes, 0x00020000);
}

template <int PAD, bool NT>
__device__ __forceinline__ void dma4(srd_t srd, unsigned voff, unsigned soff, unsigned ldsb) {
  unsigned keep;
  if constexpr (NT) {
    asm volatile(
        "s_mov_b32 %0, m0\n\ts_mov_b32 m0, %4\n\ts_nop 3\n\t"
        "buffer_load_dwordx4 %1, %2, %3 offen nt lds\n\t"
        "s_add_u32 m0, %4, %5\n\ts_nop 0\n\t"
        "buffer_load_dwordx4 %1, %2, %3 offen offset:1024 nt lds\n\t"
        "s_add_u32 m0, %4, %6\n\ts_nop 0\n\t"
        "buffer_load_dwordx4 %1, %2, %3 offen offset:2048 nt lds\n\t"
        "s_add_u32 m0, %4, %7\n\ts_nop 0\n\t"
        "buffer_load_dwordx4 %1, %2, %3 offen offset:3072 nt lds\n\t"
        "s_mov_b32 m0, %0"
        : "=&s"(keep)
        : "v"(voff), "s"(srd), "s"(soff), "s"(ldsb), "n"(PAD), "n"(2 * PAD), "n"(3 * PAD)
        : "memory", "scc");
  } else {
    asm volatile(
        "s_mov_b32 %0, m0\n\ts_mov_b32 m0, %4\n\ts_nop 3\n\t"
        "buffer_load_dwordx4 %1, %2, %3 offen lds\n\t"
        "s_add_u32 m0, %4, %5\n\ts_nop 0\n\t"
        "buffer_load_dwordx4 %1, %2, %3 offen offset:1024 lds\n\t"
        "s_add_u32 m0, %4, %6\n\ts_nop 0\n\t"
        "buffer_load_dwordx4 %1, %2, %3 offen offset:2048 lds\n\t"
        "s_add_u32 m0, %4, %7\n\ts_nop 0\n\t"
        "buffer_load_dwordx4 %1, %2, %3 offen offset:3072 lds\n\t"
        "s_mov_b32 m0, %0"
        : "=&s"(keep)
        : "v"(voff), "s"(srd), "s"(soff), "s"(ldsb), "n"(PAD), "n"(2 * PAD), "n"(3 * PAD)
        : "memory", "scc");
  }
}
__device__ __forceinline__ void dma1(srd_t srd, unsigned voff, unsigned soff, unsigned ldsb) {
  unsigned keep;
  asm volatile("s_mov_b32 %0, m0\n\ts_mov_b32 m0, %4\n\ts_nop 3\n\t"
               "buffer_load_dwordx4 %1, %2, %3 offen lds\n\t"
               "s_mov_b32 m0, %0"
               : "=&s"(keep) : "v"(voff), "s"(srd), "s"(soff), "s"(ldsb) : "memory");
}
template <int N> __device__ __forceinline__ void wait_vm() { asm volatile("s_waitcnt vmcnt(%0)" ::"n"(N) : "memory"); }
template <int N> __device__ __forceinline__ void wait_vm_lgkm() { asm volatile("s_waitcnt vmcnt(%0) lgkmcnt(0)" ::"n"(N) : "memory"); }

__device__ __forceinline__ bf16x8 ld_frag(lds_cptr p) { return *(const LAS bf16x8*)p; }
__device__ __forceinline__ s16x4 ld_tr(lds_cptr p) { return __builtin_amdgcn_ds_read_tr16_b64_v4i16((LAS s16x4*)p); }
__device__ __forceinline__ bf16x8 join(s16x4 lo, s16x4 hi) {
  return __builtin_bit_cast(bf16x8, __builtin_shufflevector(lo, hi, 0, 1, 2, 3, 4, 5, 6, 7));
}
__device__ __forceinline__ float xhalf_max(float x) {
  auto r = __builtin_amdgcn_permlane32_swap(__float_as_uint(x), __float_as_uint(x), false, false);
  return fmaxf(__uint_as_float(r[0]), __uint_as_float(r[1]));
}
__device__ __forceinline__ float xhalf_sum(float x) {
  auto r = __builtin_amdgcn_permlane32_swap(__float_as_uint(x), __float_as_uint(x), false, false);
  return __uint_as_float(r[0]) + __uint_as_float(r[1]);
}
__device__ __forceinline__ bf16x8 pack8(const f32x16& p, int o) {
  bf16x8 r;
#pragma unroll
  for (int j = 0; j < 8; ++j) r[j] = (__bf16)p[o + j];
  return r;
}
__device__ __forceinline__ unsigned grp_off(int t, int g, int w) {
  if constexpr (TA_RUN == 4) return t * 65536 + (g * 4 + w) * 4096;
  else if constexpr (TA_RUN == 8) return t * 65536 + ((g >> 1) * 4 + w) * 8192 + (g & 1) * 4096;
  else return t * 65536 + w * 16384 + g * 4096;
}

__global__ __launch_bounds__(NW * 64, 1) void tree_attn_split(
    const __hip_bfloat16* __restrict__ q, const __hip_bfloat16* __restrict__ kc, const __hip_bfloat16* __restrict__ vc,
    const __hip_bfloat16* __restrict__ kt, const __hip_bfloat16* __restrict__ vt, const __hip_bfloat16* __restrict__ mask,
    float* __restrict__ po, float* __restrict__ pml) {
  extern __shared__ __attribute__((aligned(16))) char lds[];
  const int lane = threadIdx.x & 63, w = __builtin_amdgcn_readfirstlane(threadIdx.x >> 6);
  const int r = lane & 31, h = lane >> 5;
  const int pair = blockIdx.x >> 2, split = blockIdx.x & 3;
  const srd_t sK = make_srd((const char*)kc + (size_t)blockIdx.x * WG_BYTES, WG_BYTES);
  const srd_t sV = make_srd((const char*)vc + (size_t)blockIdx.x * WG_BYTES, WG_BYTES);
  const unsigned lds0 = (unsigned)(uintptr_t)lds;
  const unsigned slotK = lds0 + w * WAVE_LDS, slotV = slotK + KSLOT;
  const unsigned voff = lane * 16;
  const bool tree_wave = (split == NSPLIT - 1) && (w == NW - 1);

  {
    const srd_t sQ = make_srd((const char*)q + (size_t)pair * 32 * ROWB, 32 * ROWB);
#pragma unroll
    for (int g = 0; g < 4; ++g) dma4<KP - 1024, false>(sQ, voff, g * 4096, slotV + g * 4 * KP);
  }
  if (tree_wave) {
    const srd_t sKt = make_srd((const char*)kt + (size_t)pair * 16 * ROWB, 16 * ROWB);
    const srd_t sVt = make_srd((const char*)vt + (size_t)pair * 16 * ROWB, 16 * ROWB);
    const srd_t sM = make_srd((const char*)mask + (size_t)(pair >> 3) * 512, 512);
#pragma unroll
    for (int g = 0; g < 2; ++g) dma4<KP - 1024, false>(sKt, voff, g * 4096, lds0 + TREE_K + g * 4 * KP);
#pragma unroll
    for (int g = 0; g < 2; ++g) dma4<VP - 1024, false>(sVt, voff, g * 4096, lds0 + TREE_V + g * 4 * VP);
    dma1(sM, voff, 0, lds0 + TREE_M);
  }
#pragma unroll
  for (int g = 0; g < W4; ++g) dma4<KP - 1024, true>(sK, voff, grp_off(0, g, w), slotK + g * 4 * KP);

  const lds_cptr kfrag = (lds_cptr)lds + w * WAVE_LDS + (r & 15) * KP + (r >> 4) * 512 + h * 16;
  const lds_cptr vfrag = (lds_cptr)lds + w * WAVE_LDS + KSLOT + (4 * h + ((lane & 15) >> 2)) * VP + ((lane >> 4) & 1) * 32 + (lane & 3) * 8;

  wait_vm<4 * W4>();
  bf16x8 qf[16];
#pragma unroll
  for (int kk = 0; kk < 16; ++kk) qf[kk] = ld_frag(kfrag + KSLOT + kk * 32);

  float m = -INFINITY, l = 0.f;
  f32x16 o[8];
#pragma unroll
  for (int c = 0; c < 8; ++c) o[c] = f32x16{};

  if (tree_wave) {
    const lds_cptr tk = (lds_cptr)lds + TREE_K + (r & 7) * KP + ((r >> 3) & 1) * 512 + h * 16;
    const lds_cptr tv = (lds_cptr)lds + TREE_V + (4 * h + ((lane & 15) >> 2)) * VP + ((lane >> 4) & 1) * 32 + (lane & 3) * 8;
    f32x16 s = f32x16{};
#pragma unroll
    for (int kk = 0; kk < 16; ++kk) s = MFMA32(ld_frag(tk + kk * 32), qf[kk], s);
    const int tq = (2 * (r & 15) + (r >> 4)) & 15;
    const LAS unsigned short* mrow = (const LAS unsigned short*)((lds_cptr)lds + TREE_M) + tq * 16;
    float tmax = -INFINITY;
#pragma unroll
    for (int i = 0; i < 8; ++i) {
      const int key = 2 * ((i & 3) + 4 * h) + (i >> 2);
      const float mk = __uint_as_float((unsigned)mrow[key] << 16);
      s[i] = fmaf(s[i], SCALE, (1.0f - mk) * -1000000000.0f) * LOG2E;
      tmax = fmaxf(tmax, s[i]);
    }
    m = xhalf_max(tmax);
#pragma unroll
    for (int i = 0; i < 8; ++i) { s[i] = __builtin_amdgcn_exp2f(s[i] - m); l += s[i]; }
    const bf16x8 p0 = pack8(s, 0);
#pragma unroll
    for (int c = 0; c < 8; ++c) o[c] = MFMA32(join(ld_tr(tv + c * 64), ld_tr(tv + 512 + c * 64)), p0, o[c]);
  }

#pragma unroll
  for (int g = W4; g < 4; ++g) { wait_vm<4 * (W4 - 1)>(); dma4<KP - 1024, true>(sK, voff, grp_off(0, g, w), slotK + g * 4 * KP); }
#pragma unroll
  for (int g = 0; g < W4; ++g) { wait_vm_lgkm<4 * (W4 - 1)>(); dma4<VP - 1024, true>(sV, voff, grp_off(0, g, w), slotV + g * 4 * VP); }

  for (int t = 0; t < TILES; ++t) {
    const unsigned voffn = t + 1 < TILES ? voff : voff + WG_BYTES;
#define ISSUE_V(g) do { wait_vm<4 * (W4 - 1)>(); dma4<VP - 1024, true>(sV, voff, grp_off(t, (g), w), slotV + (g) * 4 * VP); } while (0)
#define ISSUE_KN(g) do { wait_vm_lgkm<4 * (W4 - 1)>(); dma4<KP - 1024, true>(sK, voffn, grp_off(t + 1, (g), w) & (WG_BYTES - 1), slotK + (g) * 4 * KP); } while (0)
#define ISSUE_VN(g) do { wait_vm_lgkm<4 * (W4 - 1)>(); dma4<VP - 1024, true>(sV, voffn, grp_off(t + 1, (g), w) & (WG_BYTES - 1), slotV + (g) * 4 * VP); } while (0)
    wait_vm<4 * W4>();
    f32x16 s = f32x16{};
#pragma unroll
    for (int kk = 0; kk < 8; ++kk) s = MFMA32(ld_frag(kfrag + kk * 32), qf[kk], s);
    if constexpr (W4 < 4) ISSUE_V(W4);
#pragma unroll
    for (int kk = 8; kk < 16; ++kk) s = MFMA32(ld_frag(kfrag + kk * 32), qf[kk], s);
#pragma unroll
    for (int g = W4 + 1; g < 4; ++g) ISSUE_V(g);

    float tmax = fmaxf(s[0], s[1]);
#pragma unroll
    for (int i = 2; i < 16; ++i) tmax = fmaxf(tmax, s[i]);
    const float mn = fmaxf(m, xhalf_max(tmax) * C2);
    const float alpha = __builtin_amdgcn_exp2f(m - mn);
    m = mn;
    float ps = 0.f;
#pragma unroll
    for (int i = 0; i < 16; ++i) { s[i] = __builtin_amdgcn_exp2f(fmaf(s[i], C2, -mn)); ps += s[i]; }
    l = fmaf(l, alpha, ps);
    const bf16x8 p0 = pack8(s, 0), p1 = pack8(s, 8);

    ISSUE_KN(0);
    if (__any(alpha != 1.0f)) {
#pragma unroll
      for (int c = 0; c < 8; ++c) o[c] *= alpha;
    }
#pragma unroll
    for (int g = 1; g < W4; ++g) ISSUE_KN(g);

    wait_vm<4 * W4>();
#pragma unroll
    for (int c = 0; c < 4; ++c) {
      o[c] = MFMA32(join(ld_tr(vfrag + c * 64), ld_tr(vfrag + 8 * VP + c * 64)), p0, o[c]);
      o[c] = MFMA32(join(ld_tr(vfrag + 512 + c * 64), ld_tr(vfrag + 8 * VP + 512 + c * 64)), p1, o[c]);
    }
    if constexpr (W4 < 4) ISSUE_KN(W4);
#pragma unroll
    for (int c = 4; c < 8; ++c) {
      o[c] = MFMA32(join(ld_tr(vfrag + c * 64), ld_tr(vfrag + 8 * VP + c * 64)), p0, o[c]);
      o[c] = MFMA32(join(ld_tr(vfrag + 512 + c * 64), ld_tr(vfrag + 8 * VP + 512 + c * 64)), p1, o[c]);
    }
#pragma unroll
    for (int g = W4 + 1; g < 4; ++g) ISSUE_KN(g);
#pragma unroll
    for (int g = 0; g < W4; ++g) ISSUE_VN(g);
#undef ISSUE_V
#undef ISSUE_KN
#undef ISSUE_VN
  }

  l = xhalf_sum(l);
  wait_vm_lgkm<0>();
  {
    LAS f32x4* mine = (LAS f32x4*)((LAS char*)lds + w * WAVE_LDS) + lane;
#pragma unroll
    for (int c = 0; c < 8; ++c)
#pragma unroll
      for (int g = 0; g < 4; ++g) mine[(c * 4 + g) * 64] = f32x4{o[c][4 * g], o[c][4 * g + 1], o[c][4 * g + 2], o[c][4 * g + 3]};
    if (h == 0) ((LAS f32x2*)((LAS char*)lds + ML_OFF))[w * 32 + r] = f32x2{m, l};
  }
  __syncthreads();
  float wt[NW], M = -INFINITY, L = 0.f;
  {
    f32x2 ml[NW];
#pragma unroll
    for (int i = 0; i < NW; ++i) { ml[i] = ((const LAS f32x2*)((lds_cptr)lds + ML_OFF))[i * 32 + r]; M = fmaxf(M, ml[i][0]); }
#pragma unroll
    for (int i = 0; i < NW; ++i) { wt[i] = __builtin_amdgcn_exp2f(ml[i][0] - M); L = fmaf(wt[i], ml[i][1], L); }
  }
  f32x4* dst = (f32x4*)po + ((size_t)blockIdx.x * NW + w) * 8 * 64 + lane;
#pragma unroll
  for (int j = 0; j < 8; ++j) {
    f32x4 a = f32x4{};
#pragma unroll
    for (int i = 0; i < NW; ++i) a += wt[i] * ((const LAS f32x4*)((lds_cptr)lds + i * WAVE_LDS))[(8 * w + j) * 64 + lane];
    dst[j * 64] = a;
  }
  if (w == 0 && h == 0) ((f32x2*)pml)[blockIdx.x * 32 + r] = f32x2{M, L};
}

__global__ __launch_bounds__(64) void tree_attn_combine(const float* __restrict__ po, const float* __restrict__ pml,
                                                        __hip_bfloat16* __restrict__ out) {
  const int lane = threadIdx.x, r = lane & 31, h = lane >> 5;
  const int pair = blockIdx.x >> 3, c = blockIdx.x & 7;
  f32x2 ml[NSPLIT];
  f32x4 a[NSPLIT][4];
#pragma unroll
  for (int s = 0; s < NSPLIT; ++s) {
    ml[s] = ((const f32x2*)pml)[(pair * NSPLIT + s) * 32 + r];
#pragma unroll
    for (int j = 0; j < 4; ++j) a[s][j] = ((const f32x4*)po)[((size_t)(pair * NSPLIT + s) * 32 + c * 4 + j) * 64 + lane];
  }
  float M = -INFINITY, L = 0.f, wt[NSPLIT];
#pragma unroll
  for (int s = 0; s < NSPLIT; ++s) M = fmaxf(M, ml[s][0]);
#pragma unroll
  for (int s = 0; s < NSPLIT; ++s) { wt[s] = __builtin_amdgcn_exp2f(ml[s][0] - M); L = fmaf(wt[s], ml[s][1], L); }
  const float inv = 1.0f / L;
  u32x2 pk[4];
#pragma unroll
  for (int j = 0; j < 4; ++j) {
    f32x4 v = f32x4{};
#pragma unroll
    for (int s = 0; s < NSPLIT; ++s) v += wt[s] * a[s][j];
    v *= inv;
    const bf16x2 lo = {(__bf16)v[0], (__bf16)v[1]}, hi = {(__bf16)v[2], (__bf16)v[3]};
    pk[j] = u32x2{__builtin_bit_cast(unsigned, lo), __builtin_bit_cast(unsigned, hi)};
  }
  char* row = (char*)out + ((size_t)pair * 32 + 2 * (r & 15) + (r >> 4)) * ROWB + c * 64 + h * 16;
#pragma unroll
  for (int j = 0; j < 4; j += 2) {
    auto x = __builtin_amdgcn_permlane32_swap(pk[j][0], pk[j + 1][0], false, false);
    auto y = __builtin_amdgcn_permlane32_swap(pk[j][1], pk[j + 1][1], false, false);
    *(u32x4*)(row + j * 16) = u32x4{x[0], y[0], x[1], y[1]};
  }
}

}

extern "C" void kernel_launch(void* const* d_in, const int* in_sizes, int n_in, void* d_out, int out_size, void* d_ws,
                              size_t ws_size, hipStream_t stream) {
  using namespace ta;
  (void)in_sizes; (void)n_in; (void)out_size; (void)ws_size;
  float* po = (float*)d_ws;
  float* pml = (float*)((char*)d_ws + PO_BYTES);
  (void)hipFuncSetAttribute((const void*)tree_attn_split, hipFuncAttributeMaxDynamicSharedMemorySize, LDS_BYTES);
  hipLaunchKernelGGL(tree_attn_split, dim3(NPAIR * NSPLIT), dim3(NW * 64), LDS_BYTES, stream,
                     (const __hip_bfloat16*)d_in[0], (const __hip_bfloat16*)d_in[1], (const __hip_bfloat16*)d_in[2],
                     (const __hip_bfloat16*)d_in[3], (const __hip_bfloat16*)d_in[4], (const __hip_bfloat16*)d_in[5], po, pml);
  hipLaunchKernelGGL(tree_attn_combine, dim3(NPAIR * 8), dim3(64), 0, stream, (const float*)po, (const float*)pml,
                     (__hip_bfloat16*)d_out);
}
```

```cpp
#include <hip/hip_runtime.h>
#include <hip/hip_bf16.h>

#define TA_WIN 20
#define TA_SLEEP 1

#define STAMP(k) do {} while (0)

namespace ta {

constexpr int NB = 8, HKV = 8, CTX = 8192, HD = 256;
constexpr int NPAIR = NB * HKV, NSPLIT = 4, NW = 4, NPART = NSPLIT;
constexpr int ROWB = HD * 2;
constexpr int WG_BYTES = CTX / NSPLIT * ROWB;
constexpr int TILES = CTX / NSPLIT / (32 * NW);
constexpr int RT = 8;
constexpr int KP = 1040, VP = 1088;
constexpr int SLOT = 16 * VP, NSLOT = 5;
constexpr int P_OFF = NSLOT * SLOT, P_WAVE = (TILES - RT) * 2048;
constexpr int A_OFF = P_OFF + NW * P_WAVE, A_WAVE = TILES * 128;
constexpr int TV_OFF = A_OFF + NW * A_WAVE;
constexpr int FLAG_OFF = TV_OFF + NW * ROWB;
constexpr int LDS_BYTES = FLAG_OFF + 32;
constexpr int Q_OFF = P_OFF, TK_OFF = Q_OFF + 16 * KP;
constexpr int NCHUNK = TILES * 2 * NW;
constexpr float SCALE = 0.0625f, LOG2E = 1.4426950408889634f, C2 = SCALE * LOG2E;
constexpr int WIN = TA_WIN;
constexpr size_t PO_BYTES = (size_t)NPAIR * NPART * 32 * HD * 4;
static_assert(LDS_BYTES <= 160 * 1024 && P_WAVE >= 16384 && TK_OFF + NW * ROWB <= A_OFF && A_WAVE >= 256, "LDS map");

constexpr int lag_after(int g) { return (WIN - 4 - 4 * g + 15) / 16; }
constexpr int pub_group() { int g = 3; while (g > 0 && lag_after(g - 1) == lag_after(3)) --g; return g; }
static_assert(WIN >= 17 && WIN <= 48, "publish / drain ladder assumes 1..3 chunks in flight");

typedef __bf16 bf16x8 __attribute__((ext_vector_type(8)));
typedef __bf16 bf16x2 __attribute__((ext_vector_type(2)));
typedef short s16x4 __attribute__((ext_vector_type(4)));
typedef float f32x16 __attribute__((ext_vector_type(16)));
typedef float f32x4 __attribute__((ext_vector_type(4)));
typedef float f32x2 __attribute__((ext_vector_type(2)));
typedef unsigned u32x4 __attribute__((ext_vector_type(4)));
typedef unsigned u32x2 __attribute__((ext_vector_type(2)));
#define LAS __attribute__((address_space(3)))
typedef LAS const char* lds_cptr;
typedef LAS char* lds_ptr;
typedef __amdgpu_buffer_rsrc_t srd_t;

#define SBAR() __builtin_amdgcn_sched_barrier(0)
#define PIN(x) asm volatile("" : "+v"(x))
#define MFMA32(a, b, c) __builtin_amdgcn_mfma_f32_32x32x16_bf16((a), (b), (c), 0, 0, 0)

__device__ __forceinline__ srd_t make_srd(const void* p, int bytes) {
  return __builtin_amdgcn_make_buffer_rsrc((void*)p, (short)0, bytes, 0x00020000);
}

template <int PAD>
__device__ __forceinline__ void dma4(srd_t srd, unsigned voff, unsigned soff, unsigned ldsb) {
  unsigned keep;
  asm volatile(
      "s_mov_b32 %0, m0\n\ts_mov_b32 m0, %4\n\ts_nop 3\n\t"
      "buffer_load_dwordx4 %1, %2, %3 offen lds\n\t"
      "s_add_u32 m0, %4, %5\n\ts_nop 0\n\t"
      "buffer_load_dwordx4 %1, %2, %3 offen offset:1024 lds\n\t"
      "s_add_u32 m0, %4, %6\n\ts_nop 0\n\t"
      "buffer_load_dwordx4 %1, %2, %3 offen offset:2048 lds\n\t"
      "s_add_u32 m0, %4, %7\n\ts_nop 0\n\t"
      "buffer_load_dwordx4 %1, %2, %3 offen offset:3072 lds\n\t"
      "s_mov_b32 m0, %0"
      : "=&s"(keep)
      : "v"(voff), "s"(srd), "s"(soff), "s"(ldsb), "n"(PAD), "n"(2 * PAD), "n"(3 * PAD)
      : "memory", "scc");
}
template <int PAD, int N>
__device__ __forceinline__ void dma4_paced(srd_t srd, unsigned voff, unsigned soff, unsigned ldsb) {
  unsigned keep;
  asm volatile(
      "s_mov_b32 %0, m0\n\ts_mov_b32 m0, %4\n\ts_nop 2\n\ts_waitcnt vmcnt(%8)\n\t"
      "buffer_load_dwordx4 %1, %2, %3 offen nt lds\n\t"
      "s_add_u32 m0, %4, %5\n\ts_waitcnt vmcnt(%8)\n\t"
      "buffer_load_dwordx4 %1, %2, %3 offen offset:1024 nt lds\n\t"
      "s_add_u32 m0, %4, %6\n\ts_waitcnt vmcnt(%8)\n\t"
      "buffer_load_dwordx4 %1, %2, %3 offen offset:2048 nt lds\n\t"
      "s_add_u32 m0, %4, %7\n\ts_waitcnt vmcnt(%8)\n\t"
      "buffer_load_dwordx4 %1, %2, %3 offen offset:3072 nt lds\n\t"
      "s_mov_b32 m0, %0"
      : "=&s"(keep)
      : "v"(voff), "s"(srd), "s"(soff), "s"(ldsb), "n"(PAD), "n"(2 * PAD), "n"(3 * PAD), "n"(N)
      : "memory", "scc");
}
__device__ __forceinline__ void dma1(srd_t srd, unsigned voff, unsigned soff, unsigned ldsb) {
  unsigned keep;
  asm volatile("s_mov_b32 %0, m0\n\ts_mov_b32 m0, %4\n\ts_nop 3\n\t"
               "buffer_load_dwordx4 %1, %2, %3 offen lds\n\t"
               "s_mov_b32 m0, %0"
               : "=&s"(keep) : "v"(voff), "s"(srd), "s"(soff), "s"(ldsb) : "memory");
}
template <int N> __device__ __forceinline__ void wait_vm() { asm volatile("s_waitcnt vmcnt(%0)" ::"n"(N) : "memory"); }

__device__ __forceinline__ bf16x8 ld_frag(lds_cptr p) { return *(const LAS bf16x8*)p; }
__device__ __forceinline__ s16x4 ld_tr(lds_cptr p) { return __builtin_amdgcn_ds_read_tr16_b64_v4i16((LAS s16x4*)p); }
__device__ __forceinline__ bf16x8 join(s16x4 lo, s16x4 hi) {
  return __builtin_bit_cast(bf16x8, __builtin_shufflevector(lo, hi, 0, 1, 2, 3, 4, 5, 6, 7));
}
__device__ __forceinline__ float xhalf_max(float x) {
  auto r = __builtin_amdgcn_permlane32_swap(__float_as_uint(x), __float_as_uint(x), false, false);
  return fmaxf(__uint_as_float(r[0]), __uint_as_float(r[1]));
}
__device__ __forceinline__ float xhalf_sum(float x) {
  auto r = __builtin_amdgcn_permlane32_swap(__float_as_uint(x), __float_as_uint(x), false, false);
  return __uint_as_float(r[0]) + __uint_as_float(r[1]);
}
__device__ __forceinline__ bf16x8 pack8(const f32x16& p, int o) {
  bf16x8 r;
#pragma unroll
  for (int j = 0; j < 8; ++j) r[j] = (__bf16)p[o + j];
  return r;
}
__device__ __forceinline__ void flag_set(unsigned addr, unsigned v) {
  asm volatile("s_waitcnt lgkmcnt(0)\n\tds_write_b32 %0, %1" ::"v"(addr), "v"(v) : "memory");
}
__device__ __forceinline__ void flag_wait(unsigned addr, int need) {
  for (int spin = 0; spin < (1 << 16); ++spin) {
    unsigned v;
    asm volatile("ds_read_b32 %0, %1\n\ts_waitcnt lgkmcnt(0)" : "=v"(v) : "v"(addr) : "memory");
    if ((int)__builtin_amdgcn_readfirstlane(v) >= need) break;
    __builtin_amdgcn_s_sleep(TA_SLEEP);
  }
}

__global__ __launch_bounds__((NW + 1) * 64, 2) void tree_attn_split(
    const __hip_bfloat16* __restrict__ q, const __hip_bfloat16* __restrict__ kc, const __hip_bfloat16* __restrict__ vc,
    const __hip_bfloat16* __restrict__ kt, const __hip_bfloat16* __restrict__ vt, const __hip_bfloat16* __restrict__ mask,
    float* __restrict__ po, float* __restrict__ pml) {
  extern __shared__ __attribute__((aligned(16))) char lds[];
  const int lane = threadIdx.x & 63, wid = __builtin_amdgcn_readfirstlane(threadIdx.x >> 6);
  STAMP(0);
  const unsigned lds0 = (unsigned)(uintptr_t)lds;
  const unsigned voff = lane * 16;
  const unsigned flags = lds0 + FLAG_OFF;

  if (wid == 0) {
    __builtin_amdgcn_s_setprio(3);
    const srd_t sK = make_srd((const char*)kc + (size_t)blockIdx.x * WG_BYTES, WG_BYTES);
    const srd_t sV = make_srd((const char*)vc + (size_t)blockIdx.x * WG_BYTES, WG_BYTES);
    constexpr int PUBG = pub_group(), LAG = lag_after(PUBG);
    unsigned dst = lds0;
    for (int c = 0; c < NCHUNK; ++c) {
      const unsigned soff = (c & (NCHUNK / 2 - 1)) * 16384;
      if (c == 1) {
        if (lane < 8) flag_set(flags + lane * 4, 0);
        asm volatile("s_waitcnt lgkmcnt(0)\n\ts_barrier" ::: "memory");
      }
      if (c >= NSLOT) flag_wait(flags + 4 + ((c - NSLOT) & 3) * 4, ((c - NSLOT) >> 2) + 1);
      if (c >= NCHUNK / 2) {
#pragma unroll
        for (int g = 0; g < 4; ++g) {
          dma4_paced<VP - 1024, WIN - 1>(sV, voff, soff + g * 4096, dst + g * 4 * VP);
          if (g == PUBG) flag_set(flags, c - LAG);
        }
      } else {
#pragma unroll
        for (int g = 0; g < 4; ++g) {
          dma4_paced<KP - 1024, WIN - 1>(sK, voff, soff + g * 4096, dst + g * 4 * KP);
          if (g == PUBG && c >= 1) flag_set(flags, c - LAG);
        }
      }
      dst = dst == lds0 + (NSLOT - 1) * SLOT ? lds0 : dst + SLOT;
    }
    if constexpr (LAG > 1) { wait_vm<32>(); flag_set(flags, NCHUNK - 2); }
    wait_vm<16>(); flag_set(flags, NCHUNK - 1);
    wait_vm<0>(); flag_set(flags, NCHUNK);
    return;
  }

  const int w = wid - 1;
  const int r = lane & 31, h = lane >> 5;
  const int pair = blockIdx.x >> 2, split = blockIdx.x & 3;
  const int tkey = 4 * split + w;
  dma4<KP - 1024>(make_srd((const char*)q + (size_t)pair * 32 * ROWB, 32 * ROWB), voff, w * 4096, lds0 + Q_OFF + w * 4 * KP);
  if (lane < 32) {
    dma1(make_srd((const char*)kt + (size_t)pair * 16 * ROWB, 16 * ROWB), voff, tkey * ROWB, lds0 + TK_OFF + w * ROWB);
    dma1(make_srd((const char*)vt + (size_t)pair * 16 * ROWB, 16 * ROWB), voff, tkey * ROWB, lds0 + TV_OFF + w * ROWB);
  }
  const int qrow = 2 * (r & 15) + (r >> 4);
  const unsigned mbits = ((const unsigned short*)mask)[((pair >> 3) * 16 + (qrow & 15)) * 16 + tkey];
  const int koff = (r & 15) * KP + (r >> 4) * 512 + h * 16;
  const int vofs = (4 * h + ((lane & 15) >> 2)) * VP + ((lane >> 4) & 1) * 32 + (lane & 3) * 8;
  asm volatile("s_barrier" ::: "memory");
  wait_vm<0>();
  if (lane == 0) asm volatile("ds_add_u32 %0, %1" ::"v"(flags + 20), "v"(1u) : "memory");
  flag_wait(flags + 20, NW);
  STAMP(1);
  bf16x8 qf[16];
#pragma unroll
  for (int kk = 0; kk < 16; ++kk) qf[kk] = ld_frag((lds_cptr)lds + Q_OFF + koff + kk * 32);

  STAMP(2);
  float m, l = h == 0 ? 1.f : 0.f;
  {
    const lds_cptr tk = (lds_cptr)lds + TK_OFF + w * ROWB + h * 16;
    f32x16 s = f32x16{};
#pragma unroll
    for (int kk = 0; kk < 16; ++kk) s = MFMA32(ld_frag(tk + kk * 32), qf[kk], s);
    m = fmaf(s[0], SCALE, (1.0f - __uint_as_float(mbits << 16)) * -1000000000.0f) * LOG2E;
    PIN(m);
  }
  unsigned resc_mask = 0;
  int slot_off = w * SLOT;
  const lds_ptr pbuf = (lds_ptr)lds + P_OFF + w * P_WAVE + lane * 16;
  const lds_ptr abuf = (lds_ptr)lds + A_OFF + w * A_WAVE + r * 4;
  bf16x8 preg[RT][2];

  auto k_tile = [&](int t, bf16x8& p0, bf16x8& p1) {
    flag_wait(flags, 4 * t + w + 1);
    const lds_cptr kfrag = (lds_cptr)lds + slot_off + koff;
    f32x16 s = f32x16{};
    bf16x8 kf[8];
#pragma unroll
    for (int kk = 0; kk < 8; ++kk) kf[kk] = ld_frag(kfrag + kk * 32);
    SBAR();
#pragma unroll
    for (int kk = 0; kk < 8; ++kk) { s = MFMA32(kf[kk], qf[kk], s); kf[kk] = ld_frag(kfrag + (kk + 8) * 32); SBAR(); }
    flag_set(flags + 4 + w * 4, t + 1);
    slot_off = slot_off == 0 ? (NSLOT - 1) * SLOT : slot_off - SLOT;
    SBAR();
#pragma unroll
    for (int kk = 0; kk < 8; ++kk) s = MFMA32(kf[kk], qf[kk + 8], s);
    float tmax = fmaxf(s[0], s[1]);
#pragma unroll
    for (int i = 2; i < 16; ++i) tmax = fmaxf(tmax, s[i]);
    const float mn = fmaxf(m, xhalf_max(tmax) * C2);
    const float alpha = __builtin_amdgcn_exp2f(m - mn);
    m = mn;
    float ps = 0.f;
#pragma unroll
    for (int i = 0; i < 16; ++i) { s[i] = __builtin_amdgcn_exp2f(fmaf(s[i], C2, -mn)); ps += s[i]; }
    l = fmaf(l, alpha, ps);
    p0 = pack8(s, 0); p1 = pack8(s, 8);
    PIN(l); PIN(m);
    *(LAS float*)(abuf + t * 128) = alpha;
    if (__any(alpha != 1.0f)) resc_mask |= 1u << t;
  };
#pragma unroll
  for (int t = 0; t < RT; ++t) { k_tile(t, preg[t][0], preg[t][1]); PIN(preg[t][0]); PIN(preg[t][1]); }
#pragma unroll 1
  for (int t = RT; t < TILES; ++t) {
    bf16x8 p0, p1;
    k_tile(t, p0, p1);
    *(LAS bf16x8*)(pbuf + (t - RT) * 2048) = p0;
    *(LAS bf16x8*)(pbuf + (t - RT) * 2048 + 1024) = p1;
  }

  f32x16 o[8];
  {
    const lds_cptr tv = (lds_cptr)lds + TV_OFF + w * ROWB + ((lane >> 4) & 1) * 32 + (lane & 3) * 8;
    const bf16x8 pt = __builtin_bit_cast(bf16x8, u32x4{h == 0 ? 0x3f80u : 0u, 0u, 0u, 0u});
#pragma unroll
    for (int c = 0; c < 8; ++c) { const s16x4 v4 = ld_tr(tv + c * 64); o[c] = MFMA32(join(v4, v4), pt, f32x16{}); }
  }
  int last_slot = 0;
  auto v_tile = [&](int t, const bf16x8& p0, const bf16x8& p1) {
    if ((resc_mask >> t) & 1) {
      const float alpha = *(const LAS float*)(abuf + t * 128);
#pragma unroll
      for (int c = 0; c < 8; ++c) o[c] *= alpha;
    }
    flag_wait(flags, 4 * (TILES + t) + w + 1);
    const lds_cptr vfrag = (lds_cptr)lds + slot_off + vofs;
    bf16x8 vf[8];
#define VLOAD(i) join(ld_tr(vfrag + ((i) & 1) * 512 + ((i) >> 1) * 64), ld_tr(vfrag + 8 * VP + ((i) & 1) * 512 + ((i) >> 1) * 64))
#pragma unroll
    for (int i = 0; i < 8; ++i) vf[i] = VLOAD(i);
    SBAR();
#pragma unroll
    for (int i = 0; i < 8; ++i) { o[i >> 1] = MFMA32(vf[i], (i & 1) ? p1 : p0, o[i >> 1]); vf[i] = VLOAD(i + 8); SBAR(); }
    flag_set(flags + 4 + w * 4, TILES + t + 1);
    last_slot = slot_off;
    slot_off = slot_off == 0 ? (NSLOT - 1) * SLOT : slot_off - SLOT;
    SBAR();
#pragma unroll
    for (int i = 0; i < 8; ++i) o[4 + (i >> 1)] = MFMA32(vf[i], (i & 1) ? p1 : p0, o[4 + (i >> 1)]);
    SBAR();
#undef VLOAD
  };
#pragma unroll
  for (int t = 0; t < RT; ++t) v_tile(t, preg[t][0], preg[t][1]);
#pragma unroll 1
  for (int t = RT; t < TILES; ++t) {
    const bf16x8 p0 = ld_frag(pbuf + (t - RT) * 2048), p1 = ld_frag(pbuf + (t - RT) * 2048 + 1024);
    v_tile(t, p0, p1);
  }

  STAMP(3);
  l = xhalf_sum(l);
  {
    LAS f32x4* lo = (LAS f32x4*)pbuf;
    LAS f32x4* hi = (LAS f32x4*)((lds_ptr)lds + last_slot) + lane;
#pragma unroll
    for (int c = 0; c < 8; ++c)
#pragma unroll
      for (int g = 0; g < 4; ++g)
        (c < 4 ? lo : hi)[((c & 3) * 4 + g) * 64] = f32x4{o[c][4 * g], o[c][4 * g + 1], o[c][4 * g + 2], o[c][4 * g + 3]};
    if (h == 0) ((LAS f32x2*)((lds_ptr)lds + A_OFF + w * A_WAVE))[r] = f32x2{m, l};
  }
  STAMP(4);
  __syncthreads();
  STAMP(5);
  float wt[NW], M = -INFINITY, L = 0.f;
  {
    f32x2 ml[NW];
#pragma unroll
    for (int i = 0; i < NW; ++i) { ml[i] = ((const LAS f32x2*)((lds_cptr)lds + A_OFF + i * A_WAVE))[r]; M = fmaxf(M, ml[i][0]); }
#pragma unroll
    for (int i = 0; i < NW; ++i) { wt[i] = __builtin_amdgcn_exp2f(ml[i][0] - M); L = fmaf(wt[i], ml[i][1], L); }
  }
  f32x4* dst = (f32x4*)po + ((size_t)(pair * NPART + split) * NW + w) * 8 * 64 + lane;
  const LAS f32x4* src[NW];
#pragma unroll
  for (int i = 0; i < NW; ++i) {
    const int base = w < 2 ? P_OFF + i * P_WAVE : ((NCHUNK - NW + i) % NSLOT) * SLOT;
    src[i] = (const LAS f32x4*)((lds_cptr)lds + base) + (w & 1) * 8 * 64 + lane;
  }
#pragma unroll
  for (int j = 0; j < 8; ++j) {
    f32x4 a = f32x4{};
#pragma unroll
    for (int i = 0; i < NW; ++i) a += wt[i] * src[i][j * 64];
    dst[j * 64] = a;
  }
  if (w == 0 && h == 0) ((f32x2*)pml)[(pair * NPART + split) * 32 + r] = f32x2{M, L};
}

__global__ __launch_bounds__(64) void tree_attn_combine(const float* __restrict__ po, const float* __restrict__ pml,
                                                        __hip_bfloat16* __restrict__ out) {
  const int lane = threadIdx.x, r = lane & 31, h = lane >> 5;
  const int pair = blockIdx.x >> 3, c = blockIdx.x & 7;
  f32x2 ml[NPART];
  f32x4 a[NPART][4];
#pragma unroll
  for (int s = 0; s < NPART; ++s) {
    ml[s] = ((const f32x2*)pml)[(pair * NPART + s) * 32 + r];
#pragma unroll
    for (int j = 0; j < 4; ++j) a[s][j] = ((const f32x4*)po)[((size_t)(pair * NPART + s) * 32 + c * 4 + j) * 64 + lane];
  }
  float M = -INFINITY, L = 0.f, wt[NPART];
#pragma unroll
  for (int s = 0; s < NPART; ++s) M = fmaxf(M, ml[s][0]);
#pragma unroll
  for (int s = 0; s < NPART; ++s) { wt[s] = __builtin_amdgcn_exp2f(ml[s][0] - M); L = fmaf(wt[s], ml[s][1], L); }
  const float inv = 1.0f / L;
  u32x2 pk[4];
#pragma unroll
  for (int j = 0; j < 4; ++j) {
    f32x4 v = f32x4{};
#pragma unroll
    for (int s = 0; s < NPART; ++s) v += wt[s] * a[s][j];
    v *= inv;
    const bf16x2 lo = {(__bf16)v[0], (__bf16)v[1]}, hi = {(__bf16)v[2], (__bf16)v[3]};
    pk[j] = u32x2{__builtin_bit_cast(unsigned, lo), __builtin_bit_cast(unsigned, hi)};
  }
  char* row = (char*)out + ((size_t)pair * 32 + 2 * (r & 15) + (r >> 4)) * ROWB + c * 64 + h * 16;
#pragma unroll
  for (int j = 0; j < 4; j += 2) {
    auto x = __builtin_amdgcn_permlane32_swap(pk[j][0], pk[j + 1][0], false, false);
    auto y = __builtin_amdgcn_permlane32_swap(pk[j][1], pk[j + 1][1], false, false);
    *(u32x4*)(row + j * 16) = u32x4{x[0], y[0], x[1], y[1]};
  }
}

}

extern "C" void kernel_launch(void* const* d_in, const int* in_sizes, int n_in, void* d_out, int out_size, void* d_ws,
                              size_t ws_size, hipStream_t stream) {
  using namespace ta;
  (void)in_sizes; (void)n_in; (void)out_size; (void)ws_size;
  float* po = (float*)d_ws;
  float* pml = (float*)((char*)d_ws + PO_BYTES);
  (void)hipFuncSetAttribute((const void*)tree_attn_split, hipFuncAttributeMaxDynamicSharedMemorySize, LDS_BYTES);
  hipLaunchKernelGGL(tree_attn_split, dim3(NPAIR * NSPLIT), dim3((NW + 1) * 64), LDS_BYTES, stream,
                     (const __hip_bfloat16*)d_in[0], (const __hip_bfloat16*)d_in[1], (const __hip_bfloat16*)d_in[2],
                     (const __hip_bfloat16*)d_in[3], (const __hip_bfloat16*)d_in[4], (const __hip_bfloat16*)d_in[5], po, pml);
  hipLaunchKernelGGL(tree_attn_combine, dim3(NPAIR * 8), dim3(64), 0, stream, (const float*)po, (const float*)pml,
                     (__hip_bfloat16*)d_out);
}
```

```cpp
#include <hip/hip_runtime.h>
#include <hip/hip_bf16.h>

#define TA_WIN 20
#define TA_KPOL 1
#define TA_VPOL 1
#define TA_SLEEP 1

#define STAMP(k) do {} while (0)

namespace ta {

constexpr int NB = 8, HKV = 8, CTX = 8192, HD = 256;
constexpr int NPAIR = NB * HKV, NSPLIT = 4, NW = 4, NPART = NSPLIT;
constexpr int ROWB = HD * 2;
constexpr int WG_BYTES = CTX / NSPLIT * ROWB;
constexpr int TILES = CTX / NSPLIT / (32 * NW);
constexpr int RT = 8;
constexpr int KP = 1040, VP = 1088;
constexpr int SLOT = 16 * VP, NSLOT = 5;
constexpr int P_OFF = NSLOT * SLOT, P_WAVE = (TILES - RT) * 2048;
constexpr int A_OFF = P_OFF + NW * P_WAVE, A_WAVE = TILES * 128;
constexpr int TV_OFF = A_OFF + NW * A_WAVE;
constexpr int FLAG_OFF = TV_OFF + NW * ROWB;
constexpr int LDS_BYTES = FLAG_OFF + 32;
constexpr int Q_OFF = P_OFF, TK_OFF = Q_OFF + 16 * KP;
constexpr int NCHUNK = TILES * 2 * NW;
constexpr float SCALE = 0.0625f, LOG2E = 1.4426950408889634f, C2 = SCALE * LOG2E;
constexpr int WIN = TA_WIN;
constexpr size_t PO_BYTES = (size_t)NPAIR * NPART * 32 * HD * 4;
static_assert(LDS_BYTES <= 160 * 1024 && P_WAVE >= 16384 && TK_OFF + NW * ROWB <= A_OFF && A_WAVE >= 256, "LDS map");

constexpr int lag_after(int g) { return (WIN - 4 - 4 * g + 15) / 16; }
constexpr int pub_group() { int g = 3; while (g > 0 && lag_after(g - 1) == lag_after(3)) --g; return g; }
static_assert(WIN >= 17 && WIN <= 48, "publish / drain ladder assumes 1..3 chunks in flight");

typedef __bf16 bf16x8 __attribute__((ext_vector_type(8)));
typedef __bf16 bf16x2 __attribute__((ext_vector_type(2)));
typedef short s16x4 __attribute__((ext_vector_type(4)));
typedef float f32x16 __attribute__((ext_vector_type(16)));
typedef float f32x4 __attribute__((ext_vector_type(4)));
typedef float f32x2 __attribute__((ext_vector_type(2)));
typedef unsigned u32x4 __attribute__((ext_vector_type(4)));
typedef unsigned u32x2 __attribute__((ext_vector_type(2)));
#define LAS __attribute__((address_space(3)))
typedef LAS const char* lds_cptr;
typedef LAS char* lds_ptr;
typedef __amdgpu_buffer_rsrc_t srd_t;

#define SBAR() __builtin_amdgcn_sched_barrier(0)
#define PIN(x) asm volatile("" : "+v"(x))
#define MFMA32(a, b, c) __builtin_amdgcn_mfma_f32_32x32x16_bf16((a), (b), (c), 0, 0, 0)

__device__ __forceinline__ srd_t make_srd(const void* p, int bytes) {
  return __builtin_amdgcn_make_buffer_rsrc((void*)p, (short)0, bytes, 0x00020000);
}

template <int PAD>
__device__ __forceinline__ void dma4(srd_t srd, unsigned voff, unsigned soff, unsigned ldsb) {
  unsigned keep;
  asm volatile(
      "s_mov_b32 %0, m0\n\ts_mov_b32 m0, %4\n\ts_nop 3\n\t"
      "buffer_load_dwordx4 %1, %2, %3 offen lds\n\t"
      "s_add_u32 m0, %4, %5\n\ts_nop 0\n\t"
      "buffer_load_dwordx4 %1, %2, %3 offen offset:1024 lds\n\t"
      "s_add_u32 m0, %4, %6\n\ts_nop 0\n\t"
      "buffer_load_dwordx4 %1, %2, %3 offen offset:2048 lds\n\t"
      "s_add_u32 m0, %4, %7\n\ts_nop 0\n\t"
      "buffer_load_dwordx4 %1, %2, %3 offen offset:3072 lds\n\t"
      "s_mov_b32 m0, %0"
      : "=&s"(keep)
      : "v"(voff), "s"(srd), "s"(soff), "s"(ldsb), "n"(PAD), "n"(2 * PAD), "n"(3 * PAD)
      : "memory", "scc");
}
#define TA_DMA4P(POL) \
  asm volatile( \
      "s_mov_b32 %0, m0\n\ts_mov_b32 m0, %4\n\ts_nop 2\n\ts_waitcnt vmcnt(%8)\n\t" \
      "buffer_load_dwordx4 %1, %2, %3 offen " POL " lds\n\t" \
      "s_add_u32 m0, %4, %5\n\ts_waitcnt vmcnt(%8)\n\t" \
      "buffer_load_dwordx4 %1, %2, %3 offen offset:1024 " POL " lds\n\t" \
      "s_add_u32 m0, %4, %6\n\ts_waitcnt vmcnt(%8)\n\t" \
      "buffer_load_dwordx4 %1, %2, %3 offen offset:2048 " POL " lds\n\t" \
      "s_add_u32 m0, %4, %7\n\ts_waitcnt vmcnt(%8)\n\t" \
      "buffer_load_dwordx4 %1, %2, %3 offen offset:3072 " POL " lds\n\t" \
      "s_mov_b32 m0, %0" \
      : "=&s"(keep) \
      : "v"(voff), "s"(srd), "s"(soff), "s"(ldsb), "n"(PAD), "n"(2 * PAD), "n"(3 * PAD), "n"(N) \
      : "memory", "scc")
template <int PAD, int N, int POL>
__device__ __forceinline__ void dma4_paced(srd_t srd, unsigned voff, unsigned soff, unsigned ldsb) {
  unsigned keep;
  if constexpr (POL == 0) TA_DMA4P("");
  else if constexpr (POL == 1) TA_DMA4P("nt");
  else if constexpr (POL == 2) TA_DMA4P("sc1 nt");
  else if constexpr (POL == 3) TA_DMA4P("sc0 sc1 nt");
  else if constexpr (POL == 4) TA_DMA4P("sc0 nt");
  else if constexpr (POL == 5) TA_DMA4P("sc1");
  else TA_DMA4P("sc0 sc1");
}
__device__ __forceinline__ void dma1(srd_t srd, unsigned voff, unsigned soff, unsigned ldsb) {
  unsigned keep;
  asm volatile("s_mov_b32 %0, m0\n\ts_mov_b32 m0, %4\n\ts_nop 3\n\t"
               "buffer_load_dwordx4 %1, %2, %3 offen lds\n\t"
               "s_mov_b32 m0, %0"
               : "=&s"(keep) : "v"(voff), "s"(srd), "s"(soff), "s"(ldsb) : "memory");
}
template <int N> __device__ __forceinline__ void wait_vm() { asm volatile("s_waitcnt vmcnt(%0)" ::"n"(N) : "memory"); }

__device__ __forceinline__ bf16x8 ld_frag(lds_cptr p) { return *(const LAS bf16x8*)p; }
__device__ __forceinline__ s16x4 ld_tr(lds_cptr p) { return __builtin_amdgcn_ds_read_tr16_b64_v4i16((LAS s16x4*)p); }
__device__ __forceinline__ bf16x8 join(s16x4 lo, s16x4 hi) {
  return __builtin_bit_cast(bf16x8, __builtin_shufflevector(lo, hi, 0, 1, 2, 3, 4, 5, 6, 7));
}
__device__ __forceinline__ float xhalf_max(float x) {
  auto r = __builtin_amdgcn_permlane32_swap(__float_as_uint(x), __float_as_uint(x), false, false);
  return fmaxf(__uint_as_float(r[0]), __uint_as_float(r[1]));
}
__device__ __forceinline__ float xhalf_sum(float x) {
  auto r = __builtin_amdgcn_permlane32_swap(__float_as_uint(x), __float_as_uint(x), false, false);
  return __uint_as_float(r[0]) + __uint_as_float(r[1]);
}
__device__ __forceinline__ bf16x8 pack8(const f32x16& p, int o) {
  bf16x8 r;
#pragma unroll
  for (int j = 0; j < 8; ++j) r[j] = (__bf16)p[o + j];
  return r;
}
__device__ __forceinline__ void flag_set(unsigned addr, unsigned v) {
  asm volatile("s_waitcnt lgkmcnt(0)\n\tds_write_b32 %0, %1" ::"v"(addr), "v"(v) : "memory");
}
__device__ __forceinline__ void flag_wait(unsigned addr, int need) {
  for (int spin = 0; spin < (1 << 16); ++spin) {
    unsigned v;
    asm volatile("ds_read_b32 %0, %1\n\ts_waitcnt lgkmcnt(0)" : "=v"(v) : "v"(addr) : "memory");
    if ((int)__builtin_amdgcn_readfirstlane(v) >= need) break;
    __builtin_amdgcn_s_sleep(TA_SLEEP);
  }
}

__global__ __launch_bounds__((NW + 1) * 64, 2) void tree_attn_split(
    const __hip_bfloat16* __restrict__ q, const __hip_bfloat16* __restrict__ kc, const __hip_bfloat16* __restrict__ vc,
    const __hip_bfloat16* __restrict__ kt, const __hip_bfloat16* __restrict__ vt, const __hip_bfloat16* __restrict__ mask,
    float* __restrict__ po, float* __restrict__ pml) {
  extern __shared__ __attribute__((aligned(16))) char lds[];
  const int lane = threadIdx.x & 63, wid = __builtin_amdgcn_readfirstlane(threadIdx.x >> 6);
  STAMP(0);
  const unsigned lds0 = (unsigned)(uintptr_t)lds;
  const unsigned voff = lane * 16;
  const unsigned flags = lds0 + FLAG_OFF;

  if (wid == 0) {
    __builtin_amdgcn_s_setprio(3);
    const srd_t sK = make_srd((const char*)kc + (size_t)blockIdx.x * WG_BYTES, WG_BYTES);
    const srd_t sV = make_srd((const char*)vc + (size_t)blockIdx.x * WG_BYTES, WG_BYTES);
    constexpr int PUBG = pub_group(), LAG = lag_after(PUBG);
    unsigned dst = lds0;
    for (int c = 0; c < NCHUNK; ++c) {
      const unsigned soff = (c & (NCHUNK / 2 - 1)) * 16384;
      if (c == 1) {
        if (lane < 8) flag_set(flags + lane * 4, 0);
        asm volatile("s_waitcnt lgkmcnt(0)\n\ts_barrier" ::: "memory");
      }
      if (c >= NSLOT) flag_wait(flags + 4 + ((c - NSLOT) & 3) * 4, ((c - NSLOT) >> 2) + 1);
      if (c >= NCHUNK / 2) {
#pragma unroll
        for (int g = 0; g < 4; ++g) {
          dma4_paced<VP - 1024, WIN - 1, TA_VPOL>(sV, voff, soff + g * 4096, dst + g * 4 * VP);
          if (g == PUBG) flag_set(flags, c - LAG);
        }
      } else {
#pragma unroll
        for (int g = 0; g < 4; ++g) {
          dma4_paced<KP - 1024, WIN - 1, TA_KPOL>(sK, voff, soff + g * 4096, dst + g * 4 * KP);
          if (g == PUBG && c >= 1) flag_set(flags, c - LAG);
        }
      }
      dst = dst == lds0 + (NSLOT - 1) * SLOT ? lds0 : dst + SLOT;
    }
    if constexpr (LAG > 1) { wait_vm<32>(); flag_set(flags, NCHUNK - 2); }
    wait_vm<16>(); flag_set(flags, NCHUNK - 1);
    wait_vm<0>(); flag_set(flags, NCHUNK);
    return;
  }

  const int w = wid - 1;
  const int r = lane & 31, h = lane >> 5;
  const int pair = blockIdx.x >> 2, split = blockIdx.x & 3;
  const int tkey = 4 * split + w;
  dma4<KP - 1024>(make_srd((const char*)q + (size_t)pair * 32 * ROWB, 32 * ROWB), voff, w * 4096, lds0 + Q_OFF + w * 4 * KP);
  if (lane < 32) {
    dma1(make_srd((const char*)kt + (size_t)pair * 16 * ROWB, 16 * ROWB), voff, tkey * ROWB, lds0 + TK_OFF + w * ROWB);
    dma1(make_srd((const char*)vt + (size_t)pair * 16 * ROWB, 16 * ROWB), voff, tkey * ROWB, lds0 + TV_OFF + w * ROWB);
  }
  const int qrow = 2 * (r & 15) + (r >> 4);
  const unsigned mbits = ((const unsigned short*)mask)[((pair >> 3) * 16 + (qrow & 15)) * 16 + tkey];
  const int koff = (r & 15) * KP + (r >> 4) * 512 + h * 16;
  const int vofs = (4 * h + ((lane & 15) >> 2)) * VP + ((lane >> 4) & 1) * 32 + (lane & 3) * 8;
  asm volatile("s_barrier" ::: "memory");
  wait_vm<0>();
  if (lane == 0) asm volatile("ds_add_u32 %0, %1" ::"v"(flags + 20), "v"(1u) : "memory");
  flag_wait(flags + 20, NW);
  STAMP(1);
  bf16x8 qf[16];
#pragma unroll
  for (int kk = 0; kk < 16; ++kk) qf[kk] = ld_frag((lds_cptr)lds + Q_OFF + koff + kk * 32);

  STAMP(2);
  float m, l = h == 0 ? 1.f : 0.f;
  {
    const lds_cptr tk = (lds_cptr)lds + TK_OFF + w * ROWB + h * 16;
    f32x16 s = f32x16{};
#pragma unroll
    for (int kk = 0; kk < 16; ++kk) s = MFMA32(ld_frag(tk + kk * 32), qf[kk], s);
    m = fmaf(s[0], SCALE, (1.0f - __uint_as_float(mbits << 16)) * -1000000000.0f) * LOG2E;
    PIN(m);
  }
  unsigned resc_mask = 0;
  int slot_off = w * SLOT;
  const lds_ptr pbuf = (lds_ptr)lds + P_OFF + w * P_WAVE + lane * 16;
  const lds_ptr abuf = (lds_ptr)lds + A_OFF + w * A_WAVE + r * 4;
  bf16x8 preg[RT][2];

  auto k_tile = [&](int t, bf16x8& p0, bf16x8& p1) {
    flag_wait(flags, 4 * t + w + 1);
    const lds_cptr kfrag = (lds_cptr)lds + slot_off + koff;
    f32x16 s = f32x16{};
    bf16x8 kf[8];
#pragma unroll
    for (int kk = 0; kk < 8; ++kk) kf[kk] = ld_frag(kfrag + kk * 32);
    SBAR();
#pragma unroll
    for (int kk = 0; kk < 8; ++kk) { s = MFMA32(kf[kk], qf[kk], s); kf[kk] = ld_frag(kfrag + (kk + 8) * 32); SBAR(); }
    flag_set(flags + 4 + w * 4, t + 1);
    slot_off = slot_off == 0 ? (NSLOT - 1) * SLOT : slot_off - SLOT;
    SBAR();
#pragma unroll
    for (int kk = 0; kk < 8; ++kk) s = MFMA32(kf[kk], qf[kk + 8], s);
    float tmax = fmaxf(s[0], s[1]);
#pragma unroll
    for (int i = 2; i < 16; ++i) tmax = fmaxf(tmax, s[i]);
    const float mn = fmaxf(m, xhalf_max(tmax) * C2);
    const float alpha = __builtin_amdgcn_exp2f(m - mn);
    m = mn;
    float ps = 0.f;
#pragma unroll
    for (int i = 0; i < 16; ++i) { s[i] = __builtin_amdgcn_exp2f(fmaf(s[i], C2, -mn)); ps += s[i]; }
    l = fmaf(l, alpha, ps);
    p0 = pack8(s, 0); p1 = pack8(s, 8);
    PIN(l); PIN(m);
    *(LAS float*)(abuf + t * 128) = alpha;
    if (__any(alpha != 1.0f)) resc_mask |= 1u << t;
  };
#pragma unroll
  for (int t = 0; t < RT; ++t) { k_tile(t, preg[t][0], preg[t][1]); PIN(preg[t][0]); PIN(preg[t][1]); }
#pragma unroll 1
  for (int t = RT; t < TILES; ++t) {
    bf16x8 p0, p1;
    k_tile(t, p0, p1);
    *(LAS bf16x8*)(pbuf + (t - RT) * 2048) = p0;
    *(LAS bf16x8*)(pbuf + (t - RT) * 2048 + 1024) = p1;
  }

  f32x16 o[8];
  {
    const lds_cptr tv = (lds_cptr)lds + TV_OFF + w * ROWB + ((lane >> 4) & 1) * 32 + (lane & 3) * 8;
    const bf16x8 pt = __builtin_bit_cast(bf16x8, u32x4{h == 0 ? 0x3f80u : 0u, 0u, 0u, 0u});
#pragma unroll
    for (int c = 0; c < 8; ++c) { const s16x4 v4 = ld_tr(tv + c * 64); o[c] = MFMA32(join(v4, v4), pt, f32x16{}); }
  }
  int last_slot = 0;
  auto v_tile = [&](int t, const bf16x8& p0, const bf16x8& p1) {
    if ((resc_mask >> t) & 1) {
      const float alpha = *(const LAS float*)(abuf + t * 128);
#pragma unroll
      for (int c = 0; c < 8; ++c) o[c] *= alpha;
    }
    flag_wait(flags, 4 * (TILES + t) + w + 1);
    const lds_cptr vfrag = (lds_cptr)lds + slot_off + vofs;
    bf16x8 vf[8];
#define VLOAD(i) join(ld_tr(vfrag + ((i) & 1) * 512 + ((i) >> 1) * 64), ld_tr(vfrag + 8 * VP + ((i) & 1) * 512 + ((i) >> 1) * 64))
#pragma unroll
    for (int i = 0; i < 8; ++i) vf[i] = VLOAD(i);
    SBAR();
#pragma unroll
    for (int i = 0; i < 8; ++i) { o[i >> 1] = MFMA32(vf[i], (i & 1) ? p1 : p0, o[i >> 1]); vf[i] = VLOAD(i + 8); SBAR(); }
    flag_set(flags + 4 + w * 4, TILES + t + 1);
    last_slot = slot_off;
    slot_off = slot_off == 0 ? (NSLOT - 1) * SLOT : slot_off - SLOT;
    SBAR();
#pragma unroll
    for (int i = 0; i < 8; ++i) o[4 + (i >> 1)] = MFMA32(vf[i], (i & 1) ? p1 : p0, o[4 + (i >> 1)]);
    SBAR();
#undef VLOAD
  };
#pragma unroll
  for (int t = 0; t < RT; ++t) v_tile(t, preg[t][0], preg[t][1]);
#pragma unroll 1
  for (int t = RT; t < TILES; ++t) {
    const bf16x8 p0 = ld_frag(pbuf + (t - RT) * 2048), p1 = ld_frag(pbuf + (t - RT) * 2048 + 1024);
    v_tile(t, p0, p1);
  }

  STAMP(3);
  l = xhalf_sum(l);
  {
    LAS f32x4* lo = (LAS f32x4*)pbuf;
    LAS f32x4* hi = (LAS f32x4*)((lds_ptr)lds + last_slot) + lane;
#pragma unroll
    for (int c = 0; c < 8; ++c)
#pragma unroll
      for (int g = 0; g < 4; ++g)
        (c < 4 ? lo : hi)[((c & 3) * 4 + g) * 64] = f32x4{o[c][4 * g], o[c][4 * g + 1], o[c][4 * g + 2], o[c][4 * g + 3]};
    if (h == 0) ((LAS f32x2*)((lds_ptr)lds + A_OFF + w * A_WAVE))[r] = f32x2{m, l};
  }
  STAMP(4);
  __syncthreads();
  STAMP(5);
  float wt[NW], M = -INFINITY, L = 0.f;
  {
    f32x2 ml[NW];
#pragma unroll
    for (int i = 0; i < NW; ++i) { ml[i] = ((const LAS f32x2*)((lds_cptr)lds + A_OFF + i * A_WAVE))[r]; M = fmaxf(M, ml[i][0]); }
#pragma unroll
    for (int i = 0; i < NW; ++i) { wt[i] = __builtin_amdgcn_exp2f(ml[i][0] - M); L = fmaf(wt[i], ml[i][1], L); }
  }
  f32x4* dst = (f32x4*)po + ((size_t)(pair * NPART + split) * NW + w) * 8 * 64 + lane;
  const LAS f32x4* src[NW];
#pragma unroll
  for (int i = 0; i < NW; ++i) {
    const int base = w < 2 ? P_OFF + i * P_WAVE : ((NCHUNK - NW + i) % NSLOT) * SLOT;
    src[i] = (const LAS f32x4*)((lds_cptr)lds + base) + (w & 1) * 8 * 64 + lane;
  }
#pragma unroll
  for (int j = 0; j < 8; ++j) {
    f32x4 a = f32x4{};
#pragma unroll
    for (int i = 0; i < NW; ++i) a += wt[i] * src[i][j * 64];
    dst[j * 64] = a;
  }
  if (w == 0 && h == 0) ((f32x2*)pml)[(pair * NPART + split) * 32 + r] = f32x2{M, L};
}

__global__ __launch_bounds__(64) void tree_attn_combine(const float* __restrict__ po, const float* __restrict__ pml,
                                                        __hip_bfloat16* __restrict__ out) {
  const int lane = threadIdx.x, r = lane & 31, h = lane >> 5;
  const int pair = blockIdx.x >> 3, c = blockIdx.x & 7;
  f32x2 ml[NPART];
  f32x4 a[NPART][4];
#pragma unroll
  for (int s = 0; s < NPART; ++s) {
    ml[s] = ((const f32x2*)pml)[(pair * NPART + s) * 32 + r];
#pragma unroll
    for (int j = 0; j < 4; ++j) a[s][j] = ((const f32x4*)po)[((size_t)(pair * NPART + s) * 32 + c * 4 + j) * 64 + lane];
  }
  float M = -INFINITY, L = 0.f, wt[NPART];
#pragma unroll
  for (int s = 0; s < NPART; ++s) M = fmaxf(M, ml[s][0]);
#pragma unroll
  for (int s = 0; s < NPART; ++s) { wt[s] = __builtin_amdgcn_exp2f(ml[s][0] - M); L = fmaf(wt[s], ml[s][1], L); }
  const float inv = 1.0f / L;
  u32x2 pk[4];
#pragma unroll
  for (int j = 0; j < 4; ++j) {
    f32x4 v = f32x4{};
#pragma unroll
    for (int s = 0; s < NPART; ++s) v += wt[s] * a[s][j];
    v *= inv;
    const bf16x2 lo = {(__bf16)v[0], (__bf16)v[1]}, hi = {(__bf16)v[2], (__bf16)v[3]};
    pk[j] = u32x2{__builtin_bit_cast(unsigned, lo), __builtin_bit_cast(unsigned, hi)};
  }
  char* row = (char*)out + ((size_t)pair * 32 + 2 * (r & 15) + (r >> 4)) * ROWB + c * 64 + h * 16;
#pragma unroll
  for (int j = 0; j < 4; j += 2) {
    auto x = __builtin_amdgcn_permlane32_swap(pk[j][0], pk[j + 1][0], false, false);
    auto y = __builtin_amdgcn_permlane32_swap(pk[j][1], pk[j + 1][1], false, false);
    *(u32x4*)(row + j * 16) = u32x4{x[0], y[0], x[1], y[1]};
  }
}

}

extern "C" void kernel_launch(void* const* d_in, const int* in_sizes, int n_in, void* d_out, int out_size, void* d_ws,
                              size_t ws_size, hipStream_t stream) {
  using namespace ta;
  (void)in_sizes; (void)n_in; (void)out_size; (void)ws_size;
  float* po = (float*)d_ws;
  float* pml = (float*)((char*)d_ws + PO_BYTES);
  (void)hipFuncSetAttribute((const void*)tree_attn_split, hipFuncAttributeMaxDynamicSharedMemorySize, LDS_BYTES);
  hipLaunchKernelGGL(tree_attn_split, dim3(NPAIR * NSPLIT), dim3((NW + 1) * 64), LDS_BYTES, stream,
                     (const __hip_bfloat16*)d_in[0], (const __hip_bfloat16*)d_in[1], (const __hip_bfloat16*)d_in[2],
                     (const __hip_bfloat16*)d_in[3], (const __hip_bfloat16*)d_in[4], (const __hip_bfloat16*)d_in[5], po, pml);
  hipLaunchKernelGGL(tree_attn_combine, dim3(NPAIR * 8), dim3(64), 0, stream, (const float*)po, (const float*)pml,
                     (__hip_bfloat16*)d_out);
}
```

```cpp
#include <hip/hip_runtime.h>
#include <hip/hip_bf16.h>

#define TA_WIN 20

#define STAMP(k) do {} while (0)

#define LSTALL(x) x

namespace ta {

constexpr int NB = 8, HKV = 8, CTX = 8192, HD = 256;
constexpr int NPAIR = NB * HKV, NSPLIT = 4, NW = 4;
constexpr int ROWB = HD * 2;
constexpr int WG_BYTES = CTX / NSPLIT * ROWB;
constexpr int NT = CTX / NSPLIT / 32;
constexpr int KP = 1040, VP = 1088;
constexpr int SLOT = 16 * VP, NSLOT = 5;
constexpr int P_OFF = NSLOT * SLOT;
constexpr int REF_OFF = P_OFF + NT / 2 * 2048;
constexpr int TV_OFF = REF_OFF + (NT + 1) * 128;
constexpr int PT_OFF = TV_OFF + 4 * ROWB;
constexpr int L_OFF = PT_OFF + 256;
constexpr int FLAG_OFF = L_OFF + NW * 128;
constexpr int F_DONE = 0, F_PRDY = 16, F_LAND = 32, F_REF = 36, F_Q = 40, F_NONCE = 48;
constexpr int LDS_BYTES = FLAG_OFF + 64;
constexpr int TK_OFF = REF_OFF - 4 * ROWB, Q_OFF = TK_OFF - 16 * KP;
constexpr float SCALE = 0.0625f, LOG2E = 1.4426950408889634f, C2 = SCALE * LOG2E;
constexpr int WIN = TA_WIN;
constexpr int ZONE = 8192 + 256;
constexpr int CTRL_BYTES = NPAIR * 16 * 256;
static_assert(LDS_BYTES <= 160 * 1024 && Q_OFF >= P_OFF + 16 * 2048 && FLAG_OFF % 16 == 0, "LDS map");

constexpr int lag_after(int g) { return (WIN - 4 - 4 * g + 15) / 16; }
constexpr int pub_group() { int g = 3; while (g > 0 && lag_after(g - 1) == lag_after(3)) --g; return g; }
static_assert(WIN >= 17 && WIN <= 48, "publish / drain ladder assumes 1..3 chunks in flight");

typedef __bf16 bf16x8 __attribute__((ext_vector_type(8)));
typedef __bf16 bf16x2 __attribute__((ext_vector_type(2)));
typedef short s16x4 __attribute__((ext_vector_type(4)));
typedef float f32x16 __attribute__((ext_vector_type(16)));
typedef float f32x4 __attribute__((ext_vector_type(4)));
typedef float f32x2 __attribute__((ext_vector_type(2)));
typedef unsigned u32x4 __attribute__((ext_vector_type(4)));
typedef unsigned u32x2 __attribute__((ext_vector_type(2)));
typedef unsigned long long u64;
#define LAS __attribute__((address_space(3)))
#define GAS __attribute__((address_space(1)))
typedef LAS const char* lds_cptr;
typedef LAS char* lds_ptr;
typedef __amdgpu_buffer_rsrc_t srd_t;

#define SBAR() __builtin_amdgcn_sched_barrier(0)
#define PIN(x) asm volatile("" : "+v"(x))
#define MFMA32(a, b, c) __builtin_amdgcn_mfma_f32_32x32x16_bf16((a), (b), (c), 0, 0, 0)
#define RLX_AGENT __ATOMIC_RELAXED, __HIP_MEMORY_SCOPE_AGENT
#define RFL(x) __builtin_amdgcn_readfirstlane(x)

__device__ __forceinline__ srd_t make_srd(const void* p, int bytes) {
  return __builtin_amdgcn_make_buffer_rsrc((void*)p, (short)0, bytes, 0x00020000);
}

template <int PAD>
__device__ __forceinline__ void dma4(srd_t srd, unsigned voff, unsigned soff, unsigned ldsb) {
  unsigned keep;
  asm volatile(
      "s_mov_b32 %0, m0\n\ts_mov_b32 m0, %4\n\ts_nop 3\n\t"
      "buffer_load_dwordx4 %1, %2, %3 offen lds\n\t"
      "s_add_u32 m0, %4, %5\n\ts_nop 0\n\t"
      "buffer_load_dwordx4 %1, %2, %3 offen offset:1024 lds\n\t"
      "s_add_u32 m0, %4, %6\n\ts_nop 0\n\t"
      "buffer_load_dwordx4 %1, %2, %3 offen offset:2048 lds\n\t"
      "s_add_u32 m0, %4, %7\n\ts_nop 0\n\t"
      "buffer_load_dwordx4 %1, %2, %3 offen offset:3072 lds\n\t"
      "s_mov_b32 m0, %0"
      : "=&s"(keep)
      : "v"(voff), "s"(srd), "s"(soff), "s"(ldsb), "n"(PAD), "n"(2 * PAD), "n"(3 * PAD)
      : "memory", "scc");
}
template <int PAD, int N>
__device__ __forceinline__ void dma4_paced(srd_t srd, unsigned voff, unsigned soff, unsigned ldsb) {
  unsigned keep;
  asm volatile(
      "s_mov_b32 %0, m0\n\ts_mov_b32 m0, %4\n\ts_nop 2\n\ts_waitcnt vmcnt(%8)\n\t"
      "buffer_load_dwordx4 %1, %2, %3 offen nt lds\n\t"
      "s_add_u32 m0, %4, %5\n\ts_waitcnt vmcnt(%8)\n\t"
      "buffer_load_dwordx4 %1, %2, %3 offen offset:1024 nt lds\n\t"
      "s_add_u32 m0, %4, %6\n\ts_waitcnt vmcnt(%8)\n\t"
      "buffer_load_dwordx4 %1, %2, %3 offen offset:2048 nt lds\n\t"
      "s_add_u32 m0, %4, %7\n\ts_waitcnt vmcnt(%8)\n\t"
      "buffer_load_dwordx4 %1, %2, %3 offen offset:3072 nt lds\n\t"
      "s_mov_b32 m0, %0"
      : "=&s"(keep)
      : "v"(voff), "s"(srd), "s"(soff), "s"(ldsb), "n"(PAD), "n"(2 * PAD), "n"(3 * PAD), "n"(N)
      : "memory", "scc");
}
__device__ __forceinline__ void dma2(srd_t srd, unsigned voff, unsigned soff, unsigned ldsb) {
  unsigned keep;
  asm volatile("s_mov_b32 %0, m0\n\ts_mov_b32 m0, %4\n\ts_nop 3\n\t"
               "buffer_load_dwordx4 %1, %2, %3 offen lds\n\t"
               "buffer_load_dwordx4 %1, %2, %3 offen offset:1024 lds\n\t"
               "s_mov_b32 m0, %0"
               : "=&s"(keep) : "v"(voff), "s"(srd), "s"(soff), "s"(ldsb) : "memory");
}
template <int N> __device__ __forceinline__ void wait_vm() { asm volatile("s_waitcnt vmcnt(%0)" ::"n"(N) : "memory"); }

__device__ __forceinline__ bf16x8 ld_frag(lds_cptr p) { return *(const LAS bf16x8*)p; }
__device__ __forceinline__ s16x4 ld_tr(lds_cptr p) { return __builtin_amdgcn_ds_read_tr16_b64_v4i16((LAS s16x4*)p); }
__device__ __forceinline__ bf16x8 join(s16x4 lo, s16x4 hi) {
  return __builtin_bit_cast(bf16x8, __builtin_shufflevector(lo, hi, 0, 1, 2, 3, 4, 5, 6, 7));
}
__device__ __forceinline__ float xhalf_max(float x) {
  auto r = __builtin_amdgcn_permlane32_swap(__float_as_uint(x), __float_as_uint(x), false, false);
  return fmaxf(__uint_as_float(r[0]), __uint_as_float(r[1]));
}
__device__ __forceinline__ float xhalf_sum(float x) {
  auto r = __builtin_amdgcn_permlane32_swap(__float_as_uint(x), __float_as_uint(x), false, false);
  return __uint_as_float(r[0]) + __uint_as_float(r[1]);
}
__device__ __forceinline__ unsigned pack2(float a, float b) {
  const bf16x2 v = {(__bf16)a, (__bf16)b};
  return __builtin_bit_cast(unsigned, v);
}
__device__ __forceinline__ bf16x8 pack8(const f32x16& p, int o) {
  bf16x8 r;
#pragma unroll
  for (int j = 0; j < 8; ++j) r[j] = (__bf16)p[o + j];
  return r;
}
__device__ __forceinline__ void flag_set(unsigned addr, unsigned v) {
  asm volatile("s_waitcnt lgkmcnt(0)\n\tds_write_b32 %0, %1" ::"v"(addr), "v"(v) : "memory");
}
__device__ __forceinline__ void flag_wait(unsigned addr, int need) {
#pragma unroll 1
  for (int spin = 0; spin < (1 << 16); ++spin) {
    unsigned v;
    asm volatile("ds_read_b32 %0, %1\n\ts_waitcnt lgkmcnt(0)" : "=v"(v) : "v"(addr) : "memory");
    if ((int)RFL(v) >= need) break;
    __builtin_amdgcn_s_sleep(1);
  }
}
__device__ __forceinline__ void flag_wait4(unsigned addr, int need) {
#pragma unroll 1
  for (int spin = 0; spin < (1 << 16); ++spin) {
    u32x4 v;
    asm volatile("ds_read_b128 %0, %1\n\ts_waitcnt lgkmcnt(0)" : "=v"(v) : "v"(addr) : "memory");
    if ((int)RFL(min(min(v[0], v[1]), min(v[2], v[3]))) >= need) break;
    __builtin_amdgcn_s_sleep(1);
  }
}

__global__ __launch_bounds__((NW + 1) * 64, 2) void tree_attn(
    const __hip_bfloat16* __restrict__ q, const __hip_bfloat16* __restrict__ kc, const __hip_bfloat16* __restrict__ vc,
    const __hip_bfloat16* __restrict__ kt, const __hip_bfloat16* __restrict__ vt, const __hip_bfloat16* __restrict__ mask,
    char* ws, __hip_bfloat16* __restrict__ out) {
  extern __shared__ __attribute__((aligned(16))) char lds[];
  const int lane = threadIdx.x & 63, wid = RFL(threadIdx.x >> 6);
  STAMP(0);
  const unsigned lds0 = (unsigned)(uintptr_t)lds;
  const unsigned voff = lane * 16;
  const unsigned flags = lds0 + FLAG_OFF;

  if (__builtin_expect(wid == 0, 1)) {
    __builtin_amdgcn_s_setprio(3);
    const srd_t sK = make_srd((const char*)kc + (size_t)blockIdx.x * WG_BYTES, WG_BYTES);
    const srd_t sV = make_srd((const char*)vc + (size_t)blockIdx.x * WG_BYTES, WG_BYTES);
    constexpr int PUBG = pub_group(), LAG = lag_after(PUBG);
    unsigned dst = lds0;
#pragma unroll 1
    for (int c = 0; c < NT; ++c) {
      if (c == 1) {
        if (lane < 16) flag_set(flags + lane * 4, lane < 4 ? lane : 0);
        asm volatile("s_waitcnt lgkmcnt(0)\n\ts_barrier" ::: "memory");
      }
      LSTALL(if (c >= NSLOT) flag_wait4(flags + F_DONE, c - NSLOT + 1));
#pragma unroll
      for (int g = 0; g < 4; ++g) {
        dma4_paced<KP - 1024, WIN - 1>(sK, voff, c * 16384 + g * 4096, dst + g * 4 * KP);
        if (g == PUBG && c >= 1) flag_set(flags + F_LAND, c - LAG);
      }
      dst = dst == lds0 + (NSLOT - 1) * SLOT ? lds0 : dst + SLOT;
    }
#pragma unroll 1
    for (int c = NT; c < 2 * NT; ++c) {
      LSTALL(flag_wait4(flags + F_DONE, c - NSLOT + 1));
#pragma unroll
      for (int g = 0; g < 4; ++g) {
        dma4_paced<VP - 1024, WIN - 1>(sV, voff, (c - NT) * 16384 + g * 4096, dst + g * 4 * VP);
        if (g == PUBG) flag_set(flags + F_LAND, c - LAG);
      }
      dst = dst == lds0 + (NSLOT - 1) * SLOT ? lds0 : dst + SLOT;
    }
    if constexpr (LAG > 1) { wait_vm<32>(); flag_set(flags + F_LAND, 2 * NT - 2); }
    wait_vm<16>(); flag_set(flags + F_LAND, 2 * NT - 1);
    wait_vm<0>(); flag_set(flags + F_LAND, 2 * NT);
    return;
  }

  const int w = wid - 1;
  const int r = lane & 31, h = lane >> 5;
  const int pair = blockIdx.x >> 2, split = blockIdx.x & 3;
  dma4<KP - 1024>(make_srd((const char*)q + (size_t)pair * 32 * ROWB, 32 * ROWB), voff, w * 4096, lds0 + Q_OFF + w * 4 * KP);
  if (w == 3) dma2(make_srd((const char*)kt + (size_t)pair * 16 * ROWB, 16 * ROWB), voff, split * 4 * ROWB, lds0 + TK_OFF);
  if (w == 2) dma2(make_srd((const char*)vt + (size_t)pair * 16 * ROWB, 16 * ROWB), voff, split * 4 * ROWB, lds0 + TV_OFF);
  const int qrow = 2 * (r & 15) + (r >> 4);
  const u32x2 mbits = *(const u32x2*)((const char*)mask + (((pair >> 3) * 16 + (qrow & 15)) * 16 + 4 * split) * 2);

  GAS u64* hs_out = (GAS u64*)(ws + ((pair * 4 + split) * 4 + w) * 256);
  GAS u64* hs_in = (GAS u64*)(ws + ((pair * 4 + w) * 4 + split) * 256);
  u64 hs_word = __hip_atomic_load(hs_in, RLX_AGENT);
  const unsigned rdy0 = __hip_atomic_load((GAS unsigned*)(hs_in + 16), RLX_AGENT);

  const int koff = (r & 15) * KP + (r >> 4) * 512 + h * 16;
  const int trofs = ((lane >> 4) & 1) * 32 + (lane & 3) * 8 + w * 128;
  const int vofs = (4 * h + ((lane & 15) >> 2)) * VP + trofs;
  asm volatile("s_barrier" ::: "memory");
  wait_vm<0>();
  if (lane == 0) asm volatile("ds_add_u32 %0, %1" ::"v"(flags + F_Q), "v"(1u) : "memory");

  unsigned nonce = RFL((unsigned)(hs_word >> 32)) + 1, peer_nonce = RFL((unsigned)hs_word);
  if (nonce == RFL(rdy0)) ++nonce;
  bool confirmed = w == split;
  if (!confirmed) {
    if (lane == 0) __hip_atomic_store(hs_out, ((u64)peer_nonce << 32) | nonce, RLX_AGENT);
    flag_set(flags + F_NONCE + w * 4, nonce);
    hs_word = __hip_atomic_load(hs_in, RLX_AGENT);
  }
  auto hs_step = [&]() {
    const unsigned x = RFL((unsigned)hs_word), y = RFL((unsigned)(hs_word >> 32));
    if (x != peer_nonce) {
      peer_nonce = x;
      if (lane == 0) __hip_atomic_store(hs_out, ((u64)peer_nonce << 32) | nonce, RLX_AGENT);
    }
    if (y == nonce) confirmed = true;
    else hs_word = __hip_atomic_load(hs_in, RLX_AGENT);
  };

  flag_wait(flags + F_Q, NW);
  STAMP(1);
  bf16x8 qf[16];
#pragma unroll
  for (int kk = 0; kk < 16; ++kk) qf[kk] = ld_frag((lds_cptr)lds + Q_OFF + koff + kk * 32);

  const lds_ptr refp = (lds_ptr)lds + REF_OFF + r * 4;
  float m = -INFINITY, l = 0.f;
  if (w == 3) {
    const lds_cptr tk = (lds_cptr)lds + TK_OFF + (r & 3) * ROWB + h * 16;
    f32x16 s = f32x16{};
#pragma unroll
    for (int kk = 0; kk < 16; ++kk) s = MFMA32(ld_frag(tk + kk * 32), qf[kk], s);
#pragma unroll
    for (int i = 0; i < 4; ++i) {
      const float mk = __uint_as_float((i & 1) ? (mbits[i >> 1] & 0xffff0000u) : (mbits[i >> 1] << 16));
      s[i] = fmaf(s[i], SCALE, (1.0f - mk) * -1000000000.0f) * LOG2E;
      m = fmaxf(m, s[i]);
    }
#pragma unroll
    for (int i = 0; i < 4; ++i) { s[i] = __builtin_amdgcn_exp2f(s[i] - m); l += s[i]; }
    if (h) l = 0.f;
    *(LAS float*)refp = m;
    *(LAS u32x2*)((lds_ptr)lds + PT_OFF + r * 8) = u32x2{pack2(s[0], s[1]), pack2(s[2], s[3])};
    flag_set(flags + F_REF, 1);
  }
  STAMP(2);

  int slot_off = w * SLOT;
  const lds_ptr pbuf = (lds_ptr)lds + P_OFF + lane * 16;
  bf16x8 preg[NT / 8][2];

  auto k_tile = [&](int c, bf16x8& p0, bf16x8& p1) {
    flag_wait(flags + F_LAND, c + 1);
    const lds_cptr kfrag = (lds_cptr)lds + slot_off + koff;
    f32x16 s = f32x16{};
    bf16x8 kf[8];
#pragma unroll
    for (int kk = 0; kk < 8; ++kk) kf[kk] = ld_frag(kfrag + kk * 32);
    SBAR();
#pragma unroll
    for (int kk = 0; kk < 8; ++kk) { s = MFMA32(kf[kk], qf[kk], s); kf[kk] = ld_frag(kfrag + (kk + 8) * 32); SBAR(); }
    flag_set(flags + F_DONE + w * 4, min(c + NW, NT));
    slot_off = slot_off == 0 ? (NSLOT - 1) * SLOT : slot_off - SLOT;
    SBAR();
#pragma unroll
    for (int kk = 0; kk < 8; ++kk) s = MFMA32(kf[kk], qf[kk + 8], s);
    float tmax = fmaxf(s[0], s[1]);
#pragma unroll
    for (int i = 2; i < 16; ++i) tmax = fmaxf(tmax, s[i]);
    tmax = xhalf_max(tmax) * C2;
    PIN(tmax);
    flag_wait(flags + F_REF, c + 1);
    const float mn = fmaxf(*(const LAS float*)(refp + c * 128), tmax);
    *(LAS float*)(refp + (c + 1) * 128) = mn;
    flag_set(flags + F_REF, c + 2);
    const float alpha = __builtin_amdgcn_exp2f(m - mn);
    m = mn;
    float ps = 0.f;
#pragma unroll
    for (int i = 0; i < 16; ++i) { s[i] = __builtin_amdgcn_exp2f(fmaf(s[i], C2, -mn)); ps += s[i]; }
    l = fmaf(l, alpha, ps);
    p0 = pack8(s, 0); p1 = pack8(s, 8);
    PIN(l); PIN(m);
  };
#pragma unroll 1
  for (int c = w; c < NT / 2; c += NW) {
    bf16x8 p0, p1;
    k_tile(c, p0, p1);
    *(LAS bf16x8*)(pbuf + c * 2048) = p0;
    *(LAS bf16x8*)(pbuf + c * 2048 + 1024) = p1;
    if (!confirmed) hs_step();
  }
#pragma unroll
  for (int t = 0; t < NT / 8; ++t) { k_tile(NT / 2 + 4 * t + w, preg[t][0], preg[t][1]); PIN(preg[t][0]); PIN(preg[t][1]); }
  flag_set(flags + F_PRDY + w * 4, 1);

  f32x16 o[2];
  float rcur = *(const LAS float*)refp;
  {
    const lds_cptr tv = (lds_cptr)lds + TV_OFF + ((lane & 15) >> 2) * ROWB + trofs;
    u32x2 pt2 = *(const LAS u32x2*)((lds_cptr)lds + PT_OFF + r * 8);
    if (h) pt2 = u32x2{0u, 0u};
    const bf16x8 pt = __builtin_bit_cast(bf16x8, u32x4{pt2[0], pt2[1], 0u, 0u});
#pragma unroll
    for (int cb = 0; cb < 2; ++cb) { const s16x4 v4 = ld_tr(tv + cb * 64); o[cb] = MFMA32(join(v4, v4), pt, f32x16{}); }
  }
  flag_wait4(flags + F_PRDY, 1);
  const float M = *(const LAS float*)(refp + NT * 128);
  *(LAS float*)((lds_ptr)lds + L_OFF + w * 128 + r * 4) = xhalf_sum(l) * __builtin_amdgcn_exp2f(m - M);
  slot_off = (NT % NSLOT) * SLOT;
#pragma unroll 1
  for (int v = 0; v < NT; ++v) {
    if (v == 20 || v == 36) {
      flag_wait4(flags + F_DONE, NT + v - 4);
      if (v == 20) {
#pragma unroll
        for (int j = 0; j < 4; ++j) { *(LAS bf16x8*)(pbuf + (4 * j + w) * 2048) = preg[j][0]; *(LAS bf16x8*)(pbuf + (4 * j + w) * 2048 + 1024) = preg[j][1]; }
      } else {
#pragma unroll
        for (int j = 4; j < 8; ++j) { *(LAS bf16x8*)(pbuf + (4 * j + w) * 2048) = preg[j][0]; *(LAS bf16x8*)(pbuf + (4 * j + w) * 2048 + 1024) = preg[j][1]; }
      }
      flag_set(flags + F_PRDY + w * 4, v == 20 ? 2 : 3);
    }
    if (v == 32 || v == 48) flag_wait4(flags + F_PRDY, v == 32 ? 2 : 3);
    const float rnew = *(const LAS float*)(refp + (v + 1) * 128);
    bf16x8 p0 = ld_frag(pbuf + (v & (NT / 2 - 1)) * 2048), p1 = ld_frag(pbuf + (v & (NT / 2 - 1)) * 2048 + 1024);
    if (__any(rnew != rcur)) {
      const float alpha = __builtin_amdgcn_exp2f(rcur - rnew);
      o[0] *= alpha; o[1] *= alpha;
      rcur = rnew;
    }
    PIN(p0); PIN(p1);
    flag_wait(flags + F_LAND, NT + v + 1);
    const lds_cptr vfrag = (lds_cptr)lds + slot_off + vofs;
    bf16x8 vf[4];
#pragma unroll
    for (int i = 0; i < 4; ++i)
      vf[i] = join(ld_tr(vfrag + (i >> 1) * 512 + (i & 1) * 64), ld_tr(vfrag + 8 * VP + (i >> 1) * 512 + (i & 1) * 64));
    SBAR();
    o[0] = MFMA32(vf[0], p0, o[0]);
    o[1] = MFMA32(vf[1], p0, o[1]);
    SBAR();
    flag_set(flags + F_DONE + w * 4, NT + v + 1);
    slot_off = slot_off == (NSLOT - 1) * SLOT ? 0 : slot_off + SLOT;
    o[0] = MFMA32(vf[2], p1, o[0]);
    o[1] = MFMA32(vf[3], p1, o[1]);
  }
  STAMP(3);
  float L = 0.f;
#pragma unroll
  for (int i = 0; i < NW; ++i) L += *(const LAS float*)((lds_cptr)lds + L_OFF + i * 128 + r * 4);

  char* zones = ws + CTRL_BYTES + (size_t)pair * 16 * ZONE;
  if (w != split) {
    const srd_t sZ = make_srd(zones + (w * 4 + split) * ZONE, ZONE);
#pragma unroll
    for (int j = 0; j < 8; ++j) {
      const f32x4 x = {o[j >> 2][4 * (j & 3)], o[j >> 2][4 * (j & 3) + 1], o[j >> 2][4 * (j & 3) + 2], o[j >> 2][4 * (j & 3) + 3]};
      __builtin_amdgcn_raw_buffer_store_b128(__builtin_bit_cast(u32x4, x), sZ, voff + j * 1024, 0, 16);
    }
    __builtin_amdgcn_raw_buffer_store_b32(__float_as_uint(h ? L : M), sZ, 8192 + lane * 4, 0, 16);
#pragma unroll 1
    for (int spin = 0; !confirmed && spin < (1 << 16); ++spin) { hs_step(); __builtin_amdgcn_s_sleep(1); }
    wait_vm<0>();
    STAMP(4);
    if (lane == 0) __hip_atomic_store((GAS unsigned*)(hs_out + 16), peer_nonce, RLX_AGENT);
  } else {
    const srd_t sZ = make_srd(zones + split * 4 * ZONE, 4 * ZONE);
    const int src = (split + 1 + (lane & 3)) & 3;
    GAS unsigned* rdy = (GAS unsigned*)(ws + ((pair * 4 + src) * 4 + split) * 256 + 128);
    const unsigned want = *(const LAS unsigned*)((lds_cptr)lds + FLAG_OFF + F_NONCE + src * 4);
    u32x4 a[3][8];
    unsigned ml[3];
    unsigned have = 0;
#pragma unroll 1
    for (int spin = 0; have != 7u && spin < (1 << 16); ++spin) {
      const unsigned got = __hip_atomic_load(rdy, RLX_AGENT);
      const unsigned fresh = (unsigned)__ballot(got == want) & 7u & ~have;
      __builtin_amdgcn_fence(__ATOMIC_ACQUIRE, "wavefront");
#pragma unroll
      for (int k = 0; k < 3; ++k)
        if (fresh & (1u << k)) {
          const unsigned zo = ((split + 1 + k) & 3) * ZONE;
#pragma unroll
          for (int j = 0; j < 8; ++j) a[k][j] = __builtin_amdgcn_raw_buffer_load_b128(sZ, voff + j * 1024, zo, 16);
          ml[k] = __builtin_amdgcn_raw_buffer_load_b32(sZ, 8192 + lane * 4, zo, 16);
        }
      have |= fresh;
      if (!fresh) __builtin_amdgcn_s_sleep(1);
    }
    STAMP(4);
    float mk[3], lk[3], G = M;
#pragma unroll
    for (int k = 0; k < 3; ++k) {
      auto x = __builtin_amdgcn_permlane32_swap(ml[k], ml[k], false, false);
      mk[k] = __uint_as_float(x[0]); lk[k] = __uint_as_float(x[1]);
      G = fmaxf(G, mk[k]);
    }
    float w0 = __builtin_amdgcn_exp2f(M - G), wk[3], den = w0 * L;
#pragma unroll
    for (int k = 0; k < 3; ++k) { wk[k] = __builtin_amdgcn_exp2f(mk[k] - G); den = fmaf(wk[k], lk[k], den); }
    const float inv = 1.0f / den;
    w0 *= inv;
#pragma unroll
    for (int k = 0; k < 3; ++k) wk[k] *= inv;
    u32x2 pk[8];
#pragma unroll
    for (int j = 0; j < 8; ++j) {
      float y[4];
#pragma unroll
      for (int e = 0; e < 4; ++e) {
        y[e] = w0 * o[j >> 2][4 * (j & 3) + e];
#pragma unroll
        for (int k = 0; k < 3; ++k) y[e] = fmaf(wk[k], __uint_as_float(a[k][j][e]), y[e]);
      }
      pk[j] = u32x2{pack2(y[0], y[1]), pack2(y[2], y[3])};
    }
    char* row = (char*)out + ((size_t)pair * 32 + qrow) * ROWB + w * 128 + h * 16;
#pragma unroll
    for (int j = 0; j < 8; j += 2) {
      auto x = __builtin_amdgcn_permlane32_swap(pk[j][0], pk[j + 1][0], false, false);
      auto y = __builtin_amdgcn_permlane32_swap(pk[j][1], pk[j + 1][1], false, false);
      *(u32x4*)(row + j * 16) = u32x4{x[0], y[0], x[1], y[1]};
    }
  }
}

}

extern "C" void kernel_launch(void* const* d_in, const int* in_sizes, int n_in, void* d_out, int out_size, void* d_ws,
                              size_t ws_size, hipStream_t stream) {
  using namespace ta;
  (void)in_sizes; (void)n_in; (void)out_size; (void)ws_size;
  (void)hipFuncSetAttribute((const void*)tree_attn, hipFuncAttributeMaxDynamicSharedMemorySize, LDS_BYTES);
  hipLaunchKernelGGL(tree_attn, dim3(NPAIR * NSPLIT), dim3((NW + 1) * 64), LDS_BYTES, stream,
                     (const __hip_bfloat16*)d_in[0], (const __hip_bfloat16*)d_in[1], (const __hip_bfloat16*)d_in[2],
                     (const __hip_bfloat16*)d_in[3], (const __hip_bfloat16*)d_in[4], (const __hip_bfloat16*)d_in[5],
                     (char*)d_ws, (__hip_bfloat16*)d_out);
}
```

```cpp
#include <hip/hip_runtime.h>
#include <hip/hip_bf16.h>

#define TA_WIN 20
#define TA_WSTEP 2
#define TA_TAU 545
#define TA_OFF 500
#define TA_LSTEP 1000
#define TA_KMAX 7

#define STAMP(k) do {} while (0)

namespace ta {

constexpr int NB = 8, HKV = 8, CTX = 8192, HD = 256;
constexpr int NPAIR = NB * HKV, NSPLIT = 4, NW = 4;
constexpr int ROWB = HD * 2;
constexpr int WG_BYTES = CTX / NSPLIT * ROWB;
constexpr int NT = CTX / NSPLIT / 32;
constexpr int KP = 1040, VP = 1088;
constexpr int SLOT = 16 * VP, NSLOT = 5;
constexpr int P_OFF = NSLOT * SLOT;
constexpr int REF_OFF = P_OFF + NT / 2 * 2048;
constexpr int TV_OFF = REF_OFF + (NT + 1) * 128;
constexpr int PT_OFF = TV_OFF + 4 * ROWB;
constexpr int L_OFF = PT_OFF + 256;
constexpr int FLAG_OFF = L_OFF + NW * 128;
constexpr int F_DONE = 0, F_PRDY = 16, F_LAND = 32, F_REF = 36, F_Q = 40, F_SPARE = 44, F_NONCE = 48;
constexpr int LDS_BYTES = FLAG_OFF + 64;
constexpr int TK_OFF = REF_OFF - 4 * ROWB, Q_OFF = TK_OFF - 16 * KP;
constexpr float SCALE = 0.0625f, LOG2E = 1.4426950408889634f, C2 = SCALE * LOG2E;
constexpr int WIN = TA_WIN;
constexpr int ZONE = 8192 + 256;
constexpr int CTRL_BYTES = NPAIR * 16 * 256;
static_assert(LDS_BYTES <= 160 * 1024 && Q_OFF >= P_OFF + 16 * 2048 && FLAG_OFF % 16 == 0, "LDS map");

static_assert(WIN >= 17 && WIN + 7 * TA_WSTEP <= 48, "publish / drain ladder assumes 1..3 chunks in flight");

typedef __bf16 bf16x8 __attribute__((ext_vector_type(8)));
typedef __bf16 bf16x2 __attribute__((ext_vector_type(2)));
typedef short s16x4 __attribute__((ext_vector_type(4)));
typedef float f32x16 __attribute__((ext_vector_type(16)));
typedef float f32x4 __attribute__((ext_vector_type(4)));
typedef float f32x2 __attribute__((ext_vector_type(2)));
typedef unsigned u32x4 __attribute__((ext_vector_type(4)));
typedef unsigned u32x2 __attribute__((ext_vector_type(2)));
typedef unsigned long long u64;
#define LAS __attribute__((address_space(3)))
#define GAS __attribute__((address_space(1)))
typedef LAS const char* lds_cptr;
typedef LAS char* lds_ptr;
typedef __amdgpu_buffer_rsrc_t srd_t;

#define SBAR() __builtin_amdgcn_sched_barrier(0)
#define PIN(x) asm volatile("" : "+v"(x))
#define MFMA32(a, b, c) __builtin_amdgcn_mfma_f32_32x32x16_bf16((a), (b), (c), 0, 0, 0)
#define RLX_AGENT __ATOMIC_RELAXED, __HIP_MEMORY_SCOPE_AGENT
#define RFL(x) __builtin_amdgcn_readfirstlane(x)

__device__ __forceinline__ srd_t make_srd(const void* p, int bytes) {
  return __builtin_amdgcn_make_buffer_rsrc((void*)p, (short)0, bytes, 0x00020000);
}

template <int PAD>
__device__ __forceinline__ void dma4(srd_t srd, unsigned voff, unsigned soff, unsigned ldsb) {
  unsigned keep;
  asm volatile(
      "s_mov_b32 %0, m0\n\ts_mov_b32 m0, %4\n\ts_nop 3\n\t"
      "buffer_load_dwordx4 %1, %2, %3 offen lds\n\t"
      "s_add_u32 m0, %4, %5\n\ts_nop 0\n\t"
      "buffer_load_dwordx4 %1, %2, %3 offen offset:1024 lds\n\t"
      "s_add_u32 m0, %4, %6\n\ts_nop 0\n\t"
      "buffer_load_dwordx4 %1, %2, %3 offen offset:2048 lds\n\t"
      "s_add_u32 m0, %4, %7\n\ts_nop 0\n\t"
      "buffer_load_dwordx4 %1, %2, %3 offen offset:3072 lds\n\t"
      "s_mov_b32 m0, %0"
      : "=&s"(keep)
      : "v"(voff), "s"(srd), "s"(soff), "s"(ldsb), "n"(PAD), "n"(2 * PAD), "n"(3 * PAD)
      : "memory", "scc");
}
#define TA_PIECE(I, OFS, STEP) \
      "s_waitcnt vmcnt(%[n])\n\t" \
      "buffer_load_dwordx4 %[vo], %[srd], %[so] offen " OFS " nt lds\n\t" \
      "s_add_u32 m0, m0, " STEP "\n\t" \
      ".if (%[n] & 15) == " #I "\n\tds_write_b32 %[la], %[lv]\n\t.endif\n\t"
#define TA_GROUP(I0, I1, I2, I3) \
      TA_PIECE(I0, "", "%[pad]") TA_PIECE(I1, "offset:1024", "%[pad]") TA_PIECE(I2, "offset:2048", "%[pad]") \
      TA_PIECE(I3, "offset:3072", "%[padg]") "s_add_u32 %[so], %[so], 0x1000\n\t"
template <int N>
__device__ __forceinline__ void dma_chunk(srd_t srd, unsigned voff, unsigned soff, unsigned ldsb, unsigned pad, unsigned land_addr,
                                          int c, unsigned done_addr, u32x4& done, u64& now) {
  unsigned keep;
  asm volatile(
      "s_mov_b32 %[keep], m0\n\ts_mov_b32 m0, %[lds]\n\ts_nop 2\n\t"
      TA_GROUP(0, 1, 2, 3) TA_GROUP(4, 5, 6, 7) TA_GROUP(8, 9, 10, 11)
      "ds_read_b128 %[done], %[da]\n\ts_memrealtime %[now]\n\t"
      TA_GROUP(12, 13, 14, 15)
      "s_mov_b32 m0, %[keep]\n\ts_waitcnt lgkmcnt(0)"
      : [keep] "=&s"(keep), [so] "+s"(soff), [done] "=&v"(done), [now] "=&s"(now)
      : [vo] "v"(voff), [srd] "s"(srd), [lds] "s"(ldsb), [pad] "s"(pad), [padg] "s"(pad + 4096), [n] "n"(N),
        [la] "v"(land_addr), [lv] "v"(c - N / 16), [da] "v"(done_addr)
      : "memory", "scc");
}
__device__ __forceinline__ void dma2(srd_t srd, unsigned voff, unsigned soff, unsigned ldsb) {
  unsigned keep;
  asm volatile("s_mov_b32 %0, m0\n\ts_mov_b32 m0, %4\n\ts_nop 3\n\t"
               "buffer_load_dwordx4 %1, %2, %3 offen lds\n\t"
               "buffer_load_dwordx4 %1, %2, %3 offen offset:1024 lds\n\t"
               "s_mov_b32 m0, %0"
               : "=&s"(keep) : "v"(voff), "s"(srd), "s"(soff), "s"(ldsb) : "memory");
}
template <int N> __device__ __forceinline__ void wait_vm() { asm volatile("s_waitcnt vmcnt(%0)" ::"n"(N) : "memory"); }

__device__ __forceinline__ bf16x8 ld_frag(lds_cptr p) { return *(const LAS bf16x8*)p; }
__device__ __forceinline__ s16x4 ld_tr(lds_cptr p) { return __builtin_amdgcn_ds_read_tr16_b64_v4i16((LAS s16x4*)p); }
__device__ __forceinline__ bf16x8 join(s16x4 lo, s16x4 hi) {
  return __builtin_bit_cast(bf16x8, __builtin_shufflevector(lo, hi, 0, 1, 2, 3, 4, 5, 6, 7));
}
__device__ __forceinline__ float xhalf_max(float x) {
  auto r = __builtin_amdgcn_permlane32_swap(__float_as_uint(x), __float_as_uint(x), false, false);
  return fmaxf(__uint_as_float(r[0]), __uint_as_float(r[1]));
}
__device__ __forceinline__ float xhalf_sum(float x) {
  auto r = __builtin_amdgcn_permlane32_swap(__float_as_uint(x), __float_as_uint(x), false, false);
  return __uint_as_float(r[0]) + __uint_as_float(r[1]);
}
__device__ __forceinline__ unsigned pack2(float a, float b) {
  const bf16x2 v = {(__bf16)a, (__bf16)b};
  return __builtin_bit_cast(unsigned, v);
}
__device__ __forceinline__ bf16x8 pack8(const f32x16& p, int o) {
  bf16x8 r;
#pragma unroll
  for (int j = 0; j < 8; ++j) r[j] = (__bf16)p[o + j];
  return r;
}
__device__ __forceinline__ void flag_set(unsigned addr, unsigned v) {
  asm volatile("s_waitcnt lgkmcnt(0)\n\tds_write_b32 %0, %1" ::"v"(addr), "v"(v) : "memory");
}
__device__ __forceinline__ void flag_wait(unsigned addr, int need) {
#pragma unroll 1
  for (int spin = 0; spin < (1 << 16); ++spin) {
    unsigned v;
    asm volatile("ds_read_b32 %0, %1\n\ts_waitcnt lgkmcnt(0)" : "=v"(v) : "v"(addr) : "memory");
    if ((int)RFL(v) >= need) break;
    __builtin_amdgcn_s_sleep(1);
  }
}
__device__ __forceinline__ void flag_wait4(unsigned addr, int need) {
#pragma unroll 1
  for (int spin = 0; spin < (1 << 16); ++spin) {
    u32x4 v;
    asm volatile("ds_read_b128 %0, %1\n\ts_waitcnt lgkmcnt(0)" : "=v"(v) : "v"(addr) : "memory");
    if ((int)RFL(min(min(v[0], v[1]), min(v[2], v[3]))) >= need) break;
    __builtin_amdgcn_s_sleep(1);
  }
}

__global__ __launch_bounds__((NW + 1) * 64, 2) void tree_attn(
    const __hip_bfloat16* __restrict__ q, const __hip_bfloat16* __restrict__ kc, const __hip_bfloat16* __restrict__ vc,
    const __hip_bfloat16* __restrict__ kt, const __hip_bfloat16* __restrict__ vt, const __hip_bfloat16* __restrict__ mask,
    char* ws, __hip_bfloat16* __restrict__ out) {
  extern __shared__ __attribute__((aligned(16))) char lds[];
  const int lane = threadIdx.x & 63, wid = RFL(threadIdx.x >> 6);
  STAMP(0);
  const unsigned lds0 = (unsigned)(uintptr_t)lds;
  const unsigned voff = lane * 16;
  const unsigned flags = lds0 + FLAG_OFF;

  if (__builtin_expect(wid == 0, 1)) {
    __builtin_amdgcn_s_setprio(3);
    const srd_t sK = make_srd((const char*)kc + (size_t)blockIdx.x * WG_BYTES, WG_BYTES);
    const srd_t sV = make_srd((const char*)vc + (size_t)blockIdx.x * WG_BYTES, WG_BYTES);
    u64 now = __builtin_amdgcn_s_memrealtime();
    const unsigned t0 = (unsigned)now + TA_OFF / 10;
    u32x4 done = {0u, 0u, 0u, 0u};
    unsigned dst = lds0;
#pragma unroll 1
    for (int c = 0; c < 2 * NT; ++c) {
      if (c >= NSLOT && (int)RFL(min(min(done[0], done[1]), min(done[2], done[3]))) < c - NSLOT + 1)
        flag_wait4(flags + F_DONE, c - NSLOT + 1);
      const int lag = (int)(RFL((unsigned)now) - t0) - (((4 * c - 1) * (TA_TAU * 64 / 10)) >> 8);
      const int k = lag <= 0 ? 0 : min((lag * (655360 / TA_LSTEP)) >> 16, TA_KMAX);
      const srd_t srd = c < NT ? sK : sV;
      const unsigned soff = (c & (NT - 1)) * 16384, pad = c < NT ? KP - 1024 : VP - 1024;
      const unsigned la = flags + (c == 0 ? F_SPARE : F_LAND);
#define TA_CH(K) dma_chunk<WIN - 1 + (K) * TA_WSTEP>(srd, voff, soff, dst, pad, la, c, flags + F_DONE, done, now)
      if (k < 4) {
        if (k < 2) { if (k == 0) TA_CH(0); else TA_CH(1); } else { if (k == 2) TA_CH(2); else TA_CH(3); }
      } else {
        if (k < 6) { if (k == 4) TA_CH(4); else TA_CH(5); } else { if (k == 6) TA_CH(6); else TA_CH(7); }
      }
#undef TA_CH
      if (c == 0) {
        if (lane < 16) flag_set(flags + lane * 4, lane < 4 ? lane : 0);
        asm volatile("s_waitcnt lgkmcnt(0)\n\ts_barrier" ::: "memory");
      }
      dst = dst == lds0 + (NSLOT - 1) * SLOT ? lds0 : dst + SLOT;
    }
    wait_vm<32>(); flag_set(flags + F_LAND, 2 * NT - 2);
    wait_vm<16>(); flag_set(flags + F_LAND, 2 * NT - 1);
    wait_vm<0>(); flag_set(flags + F_LAND, 2 * NT);
    return;
  }

  const int w = wid - 1;
  const int r = lane & 31, h = lane >> 5;
  const int pair = blockIdx.x >> 2, split = blockIdx.x & 3;
  dma4<KP - 1024>(make_srd((const char*)q + (size_t)pair * 32 * ROWB, 32 * ROWB), voff, w * 4096, lds0 + Q_OFF + w * 4 * KP);
  if (w == 3) dma2(make_srd((const char*)kt + (size_t)pair * 16 * ROWB, 16 * ROWB), voff, split * 4 * ROWB, lds0 + TK_OFF);
  if (w == 2) dma2(make_srd((const char*)vt + (size_t)pair * 16 * ROWB, 16 * ROWB), voff, split * 4 * ROWB, lds0 + TV_OFF);
  const int qrow = 2 * (r & 15) + (r >> 4);
  const u32x2 mbits = *(const u32x2*)((const char*)mask + (((pair >> 3) * 16 + (qrow & 15)) * 16 + 4 * split) * 2);

  GAS u64* hs_out = (GAS u64*)(ws + ((pair * 4 + split) * 4 + w) * 256);
  GAS u64* hs_in = (GAS u64*)(ws + ((pair * 4 + w) * 4 + split) * 256);
  u64 hs_word = __hip_atomic_load(hs_in, RLX_AGENT);
  const unsigned rdy0 = __hip_atomic_load((GAS unsigned*)(hs_in + 16), RLX_AGENT);

  const int koff = (r & 15) * KP + (r >> 4) * 512 + h * 16;
  const int trofs = ((lane >> 4) & 1) * 32 + (lane & 3) * 8 + w * 128;
  const int vofs = (4 * h + ((lane & 15) >> 2)) * VP + trofs;
  asm volatile("s_barrier" ::: "memory");
  wait_vm<0>();
  if (lane == 0) asm volatile("ds_add_u32 %0, %1" ::"v"(flags + F_Q), "v"(1u) : "memory");

  unsigned nonce = RFL((unsigned)(hs_word >> 32)) + 1, peer_nonce = RFL((unsigned)hs_word);
  if (nonce == RFL(rdy0)) ++nonce;
  bool confirmed = w == split;
  if (!confirmed) {
    if (lane == 0) __hip_atomic_store(hs_out, ((u64)peer_nonce << 32) | nonce, RLX_AGENT);
    flag_set(flags + F_NONCE + w * 4, nonce);
    hs_word = __hip_atomic_load(hs_in, RLX_AGENT);
  }
  auto hs_step = [&]() {
    const unsigned x = RFL((unsigned)hs_word), y = RFL((unsigned)(hs_word >> 32));
    if (x != peer_nonce) {
      peer_nonce = x;
      if (lane == 0) __hip_atomic_store(hs_out, ((u64)peer_nonce << 32) | nonce, RLX_AGENT);
    }
    if (y == nonce) confirmed = true;
    else hs_word = __hip_atomic_load(hs_in, RLX_AGENT);
  };

  flag_wait(flags + F_Q, NW);
  STAMP(1);
  bf16x8 qf[16];
#pragma unroll
  for (int kk = 0; kk < 16; ++kk) qf[kk] = ld_frag((lds_cptr)lds + Q_OFF + koff + kk * 32);

  const lds_ptr refp = (lds_ptr)lds + REF_OFF + r * 4;
  float m = -INFINITY, l = 0.f;
  if (w == 3) {
    const lds_cptr tk = (lds_cptr)lds + TK_OFF + (r & 3) * ROWB + h * 16;
    f32x16 s = f32x16{};
#pragma unroll
    for (int kk = 0; kk < 16; ++kk) s = MFMA32(ld_frag(tk + kk * 32), qf[kk], s);
#pragma unroll
    for (int i = 0; i < 4; ++i) {
      const float mk = __uint_as_float((i & 1) ? (mbits[i >> 1] & 0xffff0000u) : (mbits[i >> 1] << 16));
      s[i] = fmaf(s[i], SCALE, (1.0f - mk) * -1000000000.0f) * LOG2E;
      m = fmaxf(m, s[i]);
    }
#pragma unroll
    for (int i = 0; i < 4; ++i) { s[i] = __builtin_amdgcn_exp2f(s[i] - m); l += s[i]; }
    if (h) l = 0.f;
    *(LAS float*)refp = m;
    *(LAS u32x2*)((lds_ptr)lds + PT_OFF + r * 8) = u32x2{pack2(s[0], s[1]), pack2(s[2], s[3])};
    flag_set(flags + F_REF, 1);
  }
  STAMP(2);

  int slot_off = w * SLOT;
  const lds_ptr pbuf = (lds_ptr)lds + P_OFF + lane * 16;
  bf16x8 preg[NT / 8][2];

  auto k_tile = [&](int c, bf16x8& p0, bf16x8& p1) {
    flag_wait(flags + F_LAND, c + 1);
    const lds_cptr kfrag = (lds_cptr)lds + slot_off + koff;
    f32x16 s = f32x16{};
    bf16x8 kf[8];
#pragma unroll
    for (int kk = 0; kk < 8; ++kk) kf[kk] = ld_frag(kfrag + kk * 32);
    SBAR();
#pragma unroll
    for (int kk = 0; kk < 8; ++kk) { s = MFMA32(kf[kk], qf[kk], s); kf[kk] = ld_frag(kfrag + (kk + 8) * 32); SBAR(); }
    flag_set(flags + F_DONE + w * 4, min(c + NW, NT));
    slot_off = slot_off == 0 ? (NSLOT - 1) * SLOT : slot_off - SLOT;
    SBAR();
#pragma unroll
    for (int kk = 0; kk < 8; ++kk) s = MFMA32(kf[kk], qf[kk + 8], s);
    float tmax = fmaxf(s[0], s[1]);
#pragma unroll
    for (int i = 2; i < 16; ++i) tmax = fmaxf(tmax, s[i]);
    tmax = xhalf_max(tmax) * C2;
    PIN(tmax);
    flag_wait(flags + F_REF, c + 1);
    const float mn = fmaxf(*(const LAS float*)(refp + c * 128), tmax);
    *(LAS float*)(refp + (c + 1) * 128) = mn;
    flag_set(flags + F_REF, c + 2);
    const float alpha = __builtin_amdgcn_exp2f(m - mn);
    m = mn;
    float ps = 0.f;
#pragma unroll
    for (int i = 0; i < 16; ++i) { s[i] = __builtin_amdgcn_exp2f(fmaf(s[i], C2, -mn)); ps += s[i]; }
    l = fmaf(l, alpha, ps);
    p0 = pack8(s, 0); p1 = pack8(s, 8);
    PIN(l); PIN(m);
  };
#pragma unroll 1
  for (int c = w; c < NT / 2; c += NW) {
    bf16x8 p0, p1;
    k_tile(c, p0, p1);
    *(LAS bf16x8*)(pbuf + c * 2048) = p0;
    *(LAS bf16x8*)(pbuf + c * 2048 + 1024) = p1;
    if (!confirmed) hs_step();
  }
#pragma unroll
  for (int t = 0; t < NT / 8; ++t) { k_tile(NT / 2 + 4 * t + w, preg[t][0], preg[t][1]); PIN(preg[t][0]); PIN(preg[t][1]); }
  flag_set(flags + F_PRDY + w * 4, 1);

  f32x16 o[2];
  float rcur = *(const LAS float*)refp;
  {
    const lds_cptr tv = (lds_cptr)lds + TV_OFF + ((lane & 15) >> 2) * ROWB + trofs;
    u32x2 pt2 = *(const LAS u32x2*)((lds_cptr)lds + PT_OFF + r * 8);
    if (h) pt2 = u32x2{0u, 0u};
    const bf16x8 pt = __builtin_bit_cast(bf16x8, u32x4{pt2[0], pt2[1], 0u, 0u});
#pragma unroll
    for (int cb = 0; cb < 2; ++cb) { const s16x4 v4 = ld_tr(tv + cb * 64); o[cb] = MFMA32(join(v4, v4), pt, f32x16{}); }
  }
  flag_wait4(flags + F_PRDY, 1);
  const float M = *(const LAS float*)(refp + NT * 128);
  *(LAS float*)((lds_ptr)lds + L_OFF + w * 128 + r * 4) = xhalf_sum(l) * __builtin_amdgcn_exp2f(m - M);
  slot_off = (NT % NSLOT) * SLOT;
#pragma unroll 1
  for (int v = 0; v < NT; ++v) {
    if (v == 20 || v == 36) {
      flag_wait4(flags + F_DONE, NT + v - 4);
      if (v == 20) {
#pragma unroll
        for (int j = 0; j < 4; ++j) { *(LAS bf16x8*)(pbuf + (4 * j + w) * 2048) = preg[j][0]; *(LAS bf16x8*)(pbuf + (4 * j + w) * 2048 + 1024) = preg[j][1]; }
      } else {
#pragma unroll
        for (int j = 4; j < 8; ++j) { *(LAS bf16x8*)(pbuf + (4 * j + w) * 2048) = preg[j][0]; *(LAS bf16x8*)(pbuf + (4 * j + w) * 2048 + 1024) = preg[j][1]; }
      }
      flag_set(flags + F_PRDY + w * 4, v == 20 ? 2 : 3);
    }
    if (v == 32 || v == 48) flag_wait4(flags + F_PRDY, v == 32 ? 2 : 3);
    const float rnew = *(const LAS float*)(refp + (v + 1) * 128);
    bf16x8 p0 = ld_frag(pbuf + (v & (NT / 2 - 1)) * 2048), p1 = ld_frag(pbuf + (v & (NT / 2 - 1)) * 2048 + 1024);
    if (__any(rnew != rcur)) {
      const float alpha = __builtin_amdgcn_exp2f(rcur - rnew);
      o[0] *= alpha; o[1] *= alpha;
      rcur = rnew;
    }
    PIN(p0); PIN(p1);
    flag_wait(flags + F_LAND, NT + v + 1);
    const lds_cptr vfrag = (lds_cptr)lds + slot_off + vofs;
    bf16x8 vf[4];
#pragma unroll
    for (int i = 0; i < 4; ++i)
      vf[i] = join(ld_tr(vfrag + (i >> 1) * 512 + (i & 1) * 64), ld_tr(vfrag + 8 * VP + (i >> 1) * 512 + (i & 1) * 64));
    SBAR();
    o[0] = MFMA32(vf[0], p0, o[0]);
    o[1] = MFMA32(vf[1], p0, o[1]);
    SBAR();
    flag_set(flags + F_DONE + w * 4, NT + v + 1);
    slot_off = slot_off == (NSLOT - 1) * SLOT ? 0 : slot_off + SLOT;
    o[0] = MFMA32(vf[2], p1, o[0]);
    o[1] = MFMA32(vf[3], p1, o[1]);
  }
  STAMP(3);
  float L = 0.f;
#pragma unroll
  for (int i = 0; i < NW; ++i) L += *(const LAS float*)((lds_cptr)lds + L_OFF + i * 128 + r * 4);

  char* zones = ws + CTRL_BYTES + (size_t)pair * 16 * ZONE;
  if (w != split) {
    const srd_t sZ = make_srd(zones + (w * 4 + split) * ZONE, ZONE);
#pragma unroll
    for (int j = 0; j < 8; ++j) {
      const f32x4 x = {o[j >> 2][4 * (j & 3)], o[j >> 2][4 * (j & 3) + 1], o[j >> 2][4 * (j & 3) + 2], o[j >> 2][4 * (j & 3) + 3]};
      __builtin_amdgcn_raw_buffer_store_b128(__builtin_bit_cast(u32x4, x), sZ, voff + j * 1024, 0, 16);
    }
    __builtin_amdgcn_raw_buffer_store_b32(__float_as_uint(h ? L : M), sZ, 8192 + lane * 4, 0, 16);
#pragma unroll 1
    for (int spin = 0; !confirmed && spin < (1 << 16); ++spin) { hs_step(); __builtin_amdgcn_s_sleep(1); }
    wait_vm<0>();
    STAMP(4);
    if (lane == 0) __hip_atomic_store((GAS unsigned*)(hs_out + 16), peer_nonce, RLX_AGENT);
  } else {
    const srd_t sZ = make_srd(zones + split * 4 * ZONE, 4 * ZONE);
    const int src = (split + 1 + (lane & 3)) & 3;
    GAS unsigned* rdy = (GAS unsigned*)(ws + ((pair * 4 + src) * 4 + split) * 256 + 128);
    const unsigned want = *(const LAS unsigned*)((lds_cptr)lds + FLAG_OFF + F_NONCE + src * 4);
    u32x4 a[3][8];
    unsigned ml[3];
    unsigned have = 0;
#pragma unroll 1
    for (int spin = 0; have != 7u && spin < (1 << 16); ++spin) {
      const unsigned got = __hip_atomic_load(rdy, RLX_AGENT);
      const unsigned fresh = (unsigned)__ballot(got == want) & 7u & ~have;
      __builtin_amdgcn_fence(__ATOMIC_ACQUIRE, "wavefront");
#pragma unroll
      for (int k = 0; k < 3; ++k)
        if (fresh & (1u << k)) {
          const unsigned zo = ((split + 1 + k) & 3) * ZONE;
#pragma unroll
          for (int j = 0; j < 8; ++j) a[k][j] = __builtin_amdgcn_raw_buffer_load_b128(sZ, voff + j * 1024, zo, 16);
          ml[k] = __builtin_amdgcn_raw_buffer_load_b32(sZ, 8192 + lane * 4, zo, 16);
        }
      have |= fresh;
      if (!fresh) __builtin_amdgcn_s_sleep(1);
    }
    STAMP(4);
    float mk[3], lk[3], G = M;
#pragma unroll
    for (int k = 0; k < 3; ++k) {
      auto x = __builtin_amdgcn_permlane32_swap(ml[k], ml[k], false, false);
      mk[k] = __uint_as_float(x[0]); lk[k] = __uint_as_float(x[1]);
      G = fmaxf(G, mk[k]);
    }
    float w0 = __builtin_amdgcn_exp2f(M - G), wk[3], den = w0 * L;
#pragma unroll
    for (int k = 0; k < 3; ++k) { wk[k] = __builtin_amdgcn_exp2f(mk[k] - G); den = fmaf(wk[k], lk[k], den); }
    const float inv = 1.0f / den;
    w0 *= inv;
#pragma unroll
    for (int k = 0; k < 3; ++k) wk[k] *= inv;
    u32x2 pk[8];
#pragma unroll
    for (int j = 0; j < 8; ++j) {
      float y[4];
#pragma unroll
      for (int e = 0; e < 4; ++e) {
        y[e] = w0 * o[j >> 2][4 * (j & 3) + e];
#pragma unroll
        for (int k = 0; k < 3; ++k) y[e] = fmaf(wk[k], __uint_as_float(a[k][j][e]), y[e]);
      }
      pk[j] = u32x2{pack2(y[0], y[1]), pack2(y[2], y[3])};
    }
    char* row = (char*)out + ((size_t)pair * 32 + qrow) * ROWB + w * 128 + h * 16;
#pragma unroll
    for (int j = 0; j < 8; j += 2) {
      auto x = __builtin_amdgcn_permlane32_swap(pk[j][0], pk[j + 1][0], false, false);
      auto y = __builtin_amdgcn_permlane32_swap(pk[j][1], pk[j + 1][1], false, false);
      *(u32x4*)(row + j * 16) = u32x4{x[0], y[0], x[1], y[1]};
    }
  }
}

}

extern "C" void kernel_launch(void* const* d_in, const int* in_sizes, int n_in, void* d_out, int out_size, void* d_ws,
                              size_t ws_size, hipStream_t stream) {
  using namespace ta;
  (void)in_sizes; (void)n_in; (void)out_size; (void)ws_size;
  (void)hipFuncSetAttribute((const void*)tree_attn, hipFuncAttributeMaxDynamicSharedMemorySize, LDS_BYTES);
  hipLaunchKernelGGL(tree_attn, dim3(NPAIR * NSPLIT), dim3((NW + 1) * 64), LDS_BYTES, stream,
                     (const __hip_bfloat16*)d_in[0], (const __hip_bfloat16*)d_in[1], (const __hip_bfloat16*)d_in[2],
                     (const __hip_bfloat16*)d_in[3], (const __hip_bfloat16*)d_in[4], (const __hip_bfloat16*)d_in[5],
                     (char*)d_ws, (__hip_bfloat16*)d_out);
}
```

```cpp
#include <hip/hip_runtime.h>
#include <hip/hip_bf16.h>

#define TA_WIN 20
#define TA_WSTEP 2
#define TA_TAU 545
#define TA_OFF 500
#define TA_LSTEP 700
#define TA_KMAX 7

#define STAMP(k) do {} while (0)

namespace ta {

constexpr int NB = 8, HKV = 8, CTX = 8192, HD = 256;
constexpr int NPAIR = NB * HKV, NSPLIT = 4, NW = 4;
constexpr int ROWB = HD * 2;
constexpr int WG_BYTES = CTX / NSPLIT * ROWB;
constexpr int NT = CTX / NSPLIT / 32;
constexpr int KP = 1040, VP = 1088;
constexpr int SLOT = 16 * VP, NSLOT = 5;
constexpr int P_OFF = NSLOT * SLOT;
constexpr int REF_OFF = P_OFF + NT / 2 * 2048;
constexpr int TV_OFF = REF_OFF + (NT + 1) * 128;
constexpr int PT_OFF = TV_OFF + 4 * ROWB;
constexpr int L_OFF = PT_OFF + 256;
constexpr int FLAG_OFF = L_OFF + NW * 128;
constexpr int F_DONE = 0, F_PRDY = 16, F_LAND = 32, F_REF = 36, F_Q = 40, F_SPARE = 44, F_NONCE = 48;
constexpr int LDS_BYTES = FLAG_OFF + 64;
constexpr int TK_OFF = REF_OFF - 4 * ROWB, Q_OFF = TK_OFF - 16 * KP;
constexpr float SCALE = 0.0625f, LOG2E = 1.4426950408889634f, C2 = SCALE * LOG2E;
constexpr int WIN = TA_WIN;
constexpr int ZONE = 8192 + 256;
constexpr int CTRL_BYTES = NPAIR * 16 * 256;
static_assert(LDS_BYTES <= 160 * 1024 && Q_OFF >= P_OFF + 16 * 2048 && FLAG_OFF % 16 == 0, "LDS map");

static_assert(WIN >= 17 && WIN + 7 * TA_WSTEP <= 48, "publish / drain ladder assumes 1..3 chunks in flight");

typedef __bf16 bf16x8 __attribute__((ext_vector_type(8)));
typedef __bf16 bf16x2 __attribute__((ext_vector_type(2)));
typedef short s16x4 __attribute__((ext_vector_type(4)));
typedef float f32x16 __attribute__((ext_vector_type(16)));
typedef float f32x4 __attribute__((ext_vector_type(4)));
typedef float f32x2 __attribute__((ext_vector_type(2)));
typedef unsigned u32x4 __attribute__((ext_vector_type(4)));
typedef unsigned u32x2 __attribute__((ext_vector_type(2)));
typedef unsigned long long u64;
#define LAS __attribute__((address_space(3)))
#define GAS __attribute__((address_space(1)))
typedef LAS const char* lds_cptr;
typedef LAS char* lds_ptr;
typedef __amdgpu_buffer_rsrc_t srd_t;

#define SBAR() __builtin_amdgcn_sched_barrier(0)
#define PIN(x) asm volatile("" : "+v"(x))
#define MFMA32(a, b, c) __builtin_amdgcn_mfma_f32_32x32x16_bf16((a), (b), (c), 0, 0, 0)
#define RLX_AGENT __ATOMIC_RELAXED, __HIP_MEMORY_SCOPE_AGENT
#define RFL(x) __builtin_amdgcn_readfirstlane(x)

__device__ __forceinline__ srd_t make_srd(const void* p, int bytes) {
  return __builtin_amdgcn_make_buffer_rsrc((void*)p, (short)0, bytes, 0x00020000);
}

template <int PAD>
__device__ __forceinline__ void dma4(srd_t srd, unsigned voff, unsigned soff, unsigned ldsb) {
  unsigned keep;
  asm volatile(
      "s_mov_b32 %0, m0\n\ts_mov_b32 m0, %4\n\ts_nop 3\n\t"
      "buffer_load_dwordx4 %1, %2, %3 offen lds\n\t"
      "s_add_u32 m0, %4, %5\n\ts_nop 0\n\t"
      "buffer_load_dwordx4 %1, %2, %3 offen offset:1024 lds\n\t"
      "s_add_u32 m0, %4, %6\n\ts_nop 0\n\t"
      "buffer_load_dwordx4 %1, %2, %3 offen offset:2048 lds\n\t"
      "s_add_u32 m0, %4, %7\n\ts_nop 0\n\t"
      "buffer_load_dwordx4 %1, %2, %3 offen offset:3072 lds\n\t"
      "s_mov_b32 m0, %0"
      : "=&s"(keep)
      : "v"(voff), "s"(srd), "s"(soff), "s"(ldsb), "n"(PAD), "n"(2 * PAD), "n"(3 * PAD)
      : "memory", "scc");
}
#define TA_PIECE(I, OFS, STEP) \
      "s_waitcnt vmcnt(%[n])\n\t" \
      "buffer_load_dwordx4 %[vo], %[srd], %[so] offen " OFS " nt lds\n\t" \
      "s_add_u32 m0, m0, " STEP "\n\t" \
      ".if (%[n] & 15) == " #I "\n\tds_write_b32 %[la], %[lv]\n\t.endif\n\t"
#define TA_GROUP(I0, I1, I2, I3) \
      TA_PIECE(I0, "", "%[pad]") TA_PIECE(I1, "offset:1024", "%[pad]") TA_PIECE(I2, "offset:2048", "%[pad]") \
      TA_PIECE(I3, "offset:3072", "%[padg]") "s_add_u32 %[so], %[so], 0x1000\n\t"
template <int N>
__device__ __forceinline__ void dma_chunk(srd_t srd, unsigned voff, unsigned soff, unsigned ldsb, unsigned pad, unsigned land_addr,
                                          int c, unsigned done_addr, u32x4& done, u64& now) {
  unsigned keep;
  asm volatile(
      "s_mov_b32 %[keep], m0\n\ts_mov_b32 m0, %[lds]\n\ts_nop 2\n\t"
      TA_GROUP(0, 1, 2, 3) TA_GROUP(4, 5, 6, 7) TA_GROUP(8, 9, 10, 11)
      "ds_read_b128 %[done], %[da]\n\ts_memrealtime %[now]\n\t"
      TA_GROUP(12, 13, 14, 15)
      "s_mov_b32 m0, %[keep]\n\ts_waitcnt lgkmcnt(0)"
      : [keep] "=&s"(keep), [so] "+s"(soff), [done] "=&v"(done), [now] "=&s"(now)
      : [vo] "v"(voff), [srd] "s"(srd), [lds] "s"(ldsb), [pad] "s"(pad), [padg] "s"(pad + 4096), [n] "n"(N),
        [la] "v"(land_addr), [lv] "v"(c - N / 16), [da] "v"(done_addr)
      : "memory", "scc");
}
__device__ __forceinline__ void dma2(srd_t srd, unsigned voff, unsigned soff, unsigned ldsb) {
  unsigned keep;
  asm volatile("s_mov_b32 %0, m0\n\ts_mov_b32 m0, %4\n\ts_nop 3\n\t"
               "buffer_load_dwordx4 %1, %2, %3 offen lds\n\t"
               "buffer_load_dwordx4 %1, %2, %3 offen offset:1024 lds\n\t"
               "s_mov_b32 m0, %0"
               : "=&s"(keep) : "v"(voff), "s"(srd), "s"(soff), "s"(ldsb) : "memory");
}
template <int N> __device__ __forceinline__ void wait_vm() { asm volatile("s_waitcnt vmcnt(%0)" ::"n"(N) : "memory"); }

__device__ __forceinline__ bf16x8 ld_frag(lds_cptr p) { return *(const LAS bf16x8*)p; }
__device__ __forceinline__ s16x4 ld_tr(lds_cptr p) { return __builtin_amdgcn_ds_read_tr16_b64_v4i16((LAS s16x4*)p); }
__device__ __forceinline__ bf16x8 join(s16x4 lo, s16x4 hi) {
  return __builtin_bit_cast(bf16x8, __builtin_shufflevector(lo, hi, 0, 1, 2, 3, 4, 5, 6, 7));
}
__device__ __forceinline__ float xhalf_max(float x) {
  auto r = __builtin_amdgcn_permlane32_swap(__float_as_uint(x), __float_as_uint(x), false, false);
  return fmaxf(__uint_as_float(r[0]), __uint_as_float(r[1]));
}
__device__ __forceinline__ float xhalf_sum(float x) {
  auto r = __builtin_amdgcn_permlane32_swap(__float_as_uint(x), __float_as_uint(x), false, false);
  return __uint_as_float(r[0]) + __uint_as_float(r[1]);
}
__device__ __forceinline__ unsigned pack2(float a, float b) {
  const bf16x2 v = {(__bf16)a, (__bf16)b};
  return __builtin_bit_cast(unsigned, v);
}
__device__ __forceinline__ bf16x8 pack8(const f32x16& p, int o) {
  bf16x8 r;
#pragma unroll
  for (int j = 0; j < 8; ++j) r[j] = (__bf16)p[o + j];
  return r;
}
__device__ __forceinline__ void flag_set(unsigned addr, unsigned v) {
  asm volatile("s_waitcnt lgkmcnt(0)\n\tds_write_b32 %0, %1" ::"v"(addr), "v"(v) : "memory");
}
__device__ __forceinline__ void flag_wait(unsigned addr, int need) {
#pragma unroll 1
  for (int spin = 0; spin < (1 << 16); ++spin) {
    unsigned v;
    asm volatile("ds_read_b32 %0, %1\n\ts_waitcnt lgkmcnt(0)" : "=v"(v) : "v"(addr) : "memory");
    if ((int)RFL(v) >= need) break;
    __builtin_amdgcn_s_sleep(1);
  }
}
__device__ __forceinline__ void flag_wait4(unsigned addr, int need) {
#pragma unroll 1
  for (int spin = 0; spin < (1 << 16); ++spin) {
    u32x4 v;
    asm volatile("ds_read_b128 %0, %1\n\ts_waitcnt lgkmcnt(0)" : "=v"(v) : "v"(addr) : "memory");
    if ((int)RFL(min(min(v[0], v[1]), min(v[2], v[3]))) >= need) break;
    __builtin_amdgcn_s_sleep(1);
  }
}

__global__ __launch_bounds__((NW + 1) * 64, 2) void tree_attn(
    const __hip_bfloat16* __restrict__ q, const __hip_bfloat16* __restrict__ kc, const __hip_bfloat16* __restrict__ vc,
    const __hip_bfloat16* __restrict__ kt, const __hip_bfloat16* __restrict__ vt, const __hip_bfloat16* __restrict__ mask,
    char* ws, __hip_bfloat16* __restrict__ out) {
  extern __shared__ __attribute__((aligned(16))) char lds[];
  const int lane = threadIdx.x & 63, wid = RFL(threadIdx.x >> 6);
  STAMP(0);
#define PROBE(k) do {} while (0)
  PROBE(0);
  const unsigned lds0 = (unsigned)(uintptr_t)lds;
  const unsigned voff = lane * 16;
  const unsigned flags = lds0 + FLAG_OFF;

  if (__builtin_expect(wid == 0, 1)) {
    __builtin_amdgcn_s_setprio(3);
    const srd_t sK = make_srd((const char*)kc + (size_t)blockIdx.x * WG_BYTES, WG_BYTES);
    const srd_t sV = make_srd((const char*)vc + (size_t)blockIdx.x * WG_BYTES, WG_BYTES);
    u64 now = __builtin_amdgcn_s_memrealtime();
    const unsigned t0 = (unsigned)now + TA_OFF / 10;
    u32x4 done = {0u, 0u, 0u, 0u};
    unsigned dst = lds0;
#pragma unroll 1
    for (int c = 0; c < 2 * NT; ++c) {
      if (c >= NSLOT && (int)RFL(min(min(done[0], done[1]), min(done[2], done[3]))) < c - NSLOT + 1)
        flag_wait4(flags + F_DONE, c - NSLOT + 1);
      const int lag = (int)(RFL((unsigned)now) - t0) - (((4 * c - 1) * (TA_TAU * 64 / 10)) >> 8);
      const int k = lag <= 0 ? 0 : min((lag * (655360 / TA_LSTEP)) >> 16, TA_KMAX);
      const srd_t srd = c < NT ? sK : sV;
      const unsigned soff = (c & (NT - 1)) * 16384, pad = c < NT ? KP - 1024 : VP - 1024;
      const unsigned la = flags + (c == 0 ? F_SPARE : F_LAND);
#define TA_CH(K) dma_chunk<WIN - 1 + (K) * TA_WSTEP>(srd, voff, soff, dst, pad, la, c, flags + F_DONE, done, now)
      if (k < 4) {
        if (k < 2) { if (k == 0) TA_CH(0); else TA_CH(1); } else { if (k == 2) TA_CH(2); else TA_CH(3); }
      } else {
        if (k < 6) { if (k == 4) TA_CH(4); else TA_CH(5); } else { if (k == 6) TA_CH(6); else TA_CH(7); }
      }
#undef TA_CH
      if (c == 0) {
        if (lane < 16) flag_set(flags + lane * 4, lane < 4 ? lane : 0);
        asm volatile("s_waitcnt lgkmcnt(0)\n\ts_barrier" ::: "memory");
      }
      dst = dst == lds0 + (NSLOT - 1) * SLOT ? lds0 : dst + SLOT;
    }
    wait_vm<32>(); flag_set(flags + F_LAND, 2 * NT - 2);
    wait_vm<16>(); flag_set(flags + F_LAND, 2 * NT - 1);
    wait_vm<0>(); flag_set(flags + F_LAND, 2 * NT);
    return;
  }

  const int w = wid - 1;
  const int r = lane & 31, h = lane >> 5;
  const int pair = blockIdx.x >> 2, split = blockIdx.x & 3;
  dma4<KP - 1024>(make_srd((const char*)q + (size_t)pair * 32 * ROWB, 32 * ROWB), voff, w * 4096, lds0 + Q_OFF + w * 4 * KP);
  if (w == 3) dma2(make_srd((const char*)kt + (size_t)pair * 16 * ROWB, 16 * ROWB), voff, split * 4 * ROWB, lds0 + TK_OFF);
  if (w == 2) dma2(make_srd((const char*)vt + (size_t)pair * 16 * ROWB, 16 * ROWB), voff, split * 4 * ROWB, lds0 + TV_OFF);
  const int qrow = 2 * (r & 15) + (r >> 4);
  const u32x2 mbits = *(const u32x2*)((const char*)mask + (((pair >> 3) * 16 + (qrow & 15)) * 16 + 4 * split) * 2);

  GAS u64* hs_out = (GAS u64*)(ws + ((pair * 4 + split) * 4 + w) * 256);
  GAS u64* hs_in = (GAS u64*)(ws + ((pair * 4 + w) * 4 + split) * 256);
  u64 hs_word = __hip_atomic_load(hs_in, RLX_AGENT);
  const unsigned rdy0 = __hip_atomic_load((GAS unsigned*)(hs_in + 16), RLX_AGENT);

  const int koff = (r & 15) * KP + (r >> 4) * 512 + h * 16;
  const int trofs = ((lane >> 4) & 1) * 32 + (lane & 3) * 8 + w * 128;
  const int vofs = (4 * h + ((lane & 15) >> 2)) * VP + trofs;
  asm volatile("s_barrier" ::: "memory");
  wait_vm<0>();
  if (lane == 0) asm volatile("ds_add_u32 %0, %1" ::"v"(flags + F_Q), "v"(1u) : "memory");

  unsigned nonce = RFL((unsigned)(hs_word >> 32)) + 1, peer_nonce = RFL((unsigned)hs_word);
  if (nonce == RFL(rdy0)) ++nonce;
  bool confirmed = w == split;
  if (!confirmed) {
    if (lane == 0) __hip_atomic_store(hs_out, ((u64)peer_nonce << 32) | nonce, RLX_AGENT);
    flag_set(flags + F_NONCE + w * 4, nonce);
    hs_word = __hip_atomic_load(hs_in, RLX_AGENT);
  }
  auto hs_step = [&]() {
    const unsigned x = RFL((unsigned)hs_word), y = RFL((unsigned)(hs_word >> 32));
    if (x != peer_nonce) {
      peer_nonce = x;
      if (lane == 0) __hip_atomic_store(hs_out, ((u64)peer_nonce << 32) | nonce, RLX_AGENT);
    }
    if (y == nonce) confirmed = true;
    else hs_word = __hip_atomic_load(hs_in, RLX_AGENT);
  };

  flag_wait(flags + F_Q, NW);
  STAMP(1);
  bf16x8 qf[16];
#pragma unroll
  for (int kk = 0; kk < 16; ++kk) qf[kk] = ld_frag((lds_cptr)lds + Q_OFF + koff + kk * 32);

  const lds_ptr refp = (lds_ptr)lds + REF_OFF + r * 4;
  float m = -INFINITY, l = 0.f;
  if (w == 3) {
    const lds_cptr tk = (lds_cptr)lds + TK_OFF + (r & 3) * ROWB + h * 16;
    f32x16 s = f32x16{};
#pragma unroll
    for (int kk = 0; kk < 16; ++kk) s = MFMA32(ld_frag(tk + kk * 32), qf[kk], s);
#pragma unroll
    for (int i = 0; i < 4; ++i) {
      const float mk = __uint_as_float((i & 1) ? (mbits[i >> 1] & 0xffff0000u) : (mbits[i >> 1] << 16));
      s[i] = fmaf(s[i], SCALE, (1.0f - mk) * -1000000000.0f) * LOG2E;
      m = fmaxf(m, s[i]);
    }
#pragma unroll
    for (int i = 0; i < 4; ++i) { s[i] = __builtin_amdgcn_exp2f(s[i] - m); l += s[i]; }
    if (h) l = 0.f;
    *(LAS float*)refp = m;
    *(LAS u32x2*)((lds_ptr)lds + PT_OFF + r * 8) = u32x2{pack2(s[0], s[1]), pack2(s[2], s[3])};
    flag_set(flags + F_REF, 1);
  }
  STAMP(2);

  int slot_off = w * SLOT;
  const lds_ptr pbuf = (lds_ptr)lds + P_OFF + lane * 16;
  bf16x8 preg[NT / 8][2];

  auto k_tile = [&](int c, bf16x8& p0, bf16x8& p1) {
    flag_wait(flags + F_LAND, c + 1);
    const lds_cptr kfrag = (lds_cptr)lds + slot_off + koff;
    f32x16 s = f32x16{};
    bf16x8 kf[8];
#pragma unroll
    for (int kk = 0; kk < 8; ++kk) kf[kk] = ld_frag(kfrag + kk * 32);
    SBAR();
#pragma unroll
    for (int kk = 0; kk < 8; ++kk) { s = MFMA32(kf[kk], qf[kk], s); kf[kk] = ld_frag(kfrag + (kk + 8) * 32); SBAR(); }
    flag_set(flags + F_DONE + w * 4, min(c + NW, NT));
    slot_off = slot_off == 0 ? (NSLOT - 1) * SLOT : slot_off - SLOT;
    SBAR();
#pragma unroll
    for (int kk = 0; kk < 8; ++kk) s = MFMA32(kf[kk], qf[kk + 8], s);
    float tmax = fmaxf(s[0], s[1]);
#pragma unroll
    for (int i = 2; i < 16; ++i) tmax = fmaxf(tmax, s[i]);
    tmax = xhalf_max(tmax) * C2;
    PIN(tmax);
    flag_wait(flags + F_REF, c + 1);
    const float mn = fmaxf(*(const LAS float*)(refp + c * 128), tmax);
    *(LAS float*)(refp + (c + 1) * 128) = mn;
    flag_set(flags + F_REF, c + 2);
    const float alpha = __builtin_amdgcn_exp2f(m - mn);
    m = mn;
    float ps = 0.f;
#pragma unroll
    for (int i = 0; i < 16; ++i) { s[i] = __builtin_amdgcn_exp2f(fmaf(s[i], C2, -mn)); ps += s[i]; }
    l = fmaf(l, alpha, ps);
    p0 = pack8(s, 0); p1 = pack8(s, 8);
    PIN(l); PIN(m);
  };
#pragma unroll 1
  for (int c = w; c < NT / 2; c += NW) {
    bf16x8 p0, p1;
    k_tile(c, p0, p1);
    *(LAS bf16x8*)(pbuf + c * 2048) = p0;
    *(LAS bf16x8*)(pbuf + c * 2048 + 1024) = p1;
    if (!confirmed) hs_step();
  }
#pragma unroll
  for (int t = 0; t < NT / 8; ++t) { k_tile(NT / 2 + 4 * t + w, preg[t][0], preg[t][1]); PIN(preg[t][0]); PIN(preg[t][1]); }
  flag_set(flags + F_PRDY + w * 4, 1);

  f32x16 o[2];
  float rcur = *(const LAS float*)refp;
  {
    const lds_cptr tv = (lds_cptr)lds + TV_OFF + ((lane & 15) >> 2) * ROWB + trofs;
    u32x2 pt2 = *(const LAS u32x2*)((lds_cptr)lds + PT_OFF + r * 8);
    if (h) pt2 = u32x2{0u, 0u};
    const bf16x8 pt = __builtin_bit_cast(bf16x8, u32x4{pt2[0], pt2[1], 0u, 0u});
#pragma unroll
    for (int cb = 0; cb < 2; ++cb) { const s16x4 v4 = ld_tr(tv + cb * 64); o[cb] = MFMA32(join(v4, v4), pt, f32x16{}); }
  }
  flag_wait4(flags + F_PRDY, 1);
  PROBE(2);
  const float M = *(const LAS float*)(refp + NT * 128);
  *(LAS float*)((lds_ptr)lds + L_OFF + w * 128 + r * 4) = xhalf_sum(l) * __builtin_amdgcn_exp2f(m - M);
  slot_off = (NT % NSLOT) * SLOT;
#pragma unroll 1
  for (int v = 0; v < NT; ++v) {
    if (v == 20 || v == 36) {
      flag_wait4(flags + F_DONE, NT + v - 4);
      if (v == 20) {
#pragma unroll
        for (int j = 0; j < 4; ++j) { *(LAS bf16x8*)(pbuf + (4 * j + w) * 2048) = preg[j][0]; *(LAS bf16x8*)(pbuf + (4 * j + w) * 2048 + 1024) = preg[j][1]; }
      } else {
#pragma unroll
        for (int j = 4; j < 8; ++j) { *(LAS bf16x8*)(pbuf + (4 * j + w) * 2048) = preg[j][0]; *(LAS bf16x8*)(pbuf + (4 * j + w) * 2048 + 1024) = preg[j][1]; }
      }
      flag_set(flags + F_PRDY + w * 4, v == 20 ? 2 : 3);
    }
    if (v == 32 || v == 48) flag_wait4(flags + F_PRDY, v == 32 ? 2 : 3);
    const float rnew = *(const LAS float*)(refp + (v + 1) * 128);
    bf16x8 p0 = ld_frag(pbuf + (v & (NT / 2 - 1)) * 2048), p1 = ld_frag(pbuf + (v & (NT / 2 - 1)) * 2048 + 1024);
    if (__any(rnew != rcur)) {
      const float alpha = __builtin_amdgcn_exp2f(rcur - rnew);
      o[0] *= alpha; o[1] *= alpha;
      rcur = rnew;
    }
    PIN(p0); PIN(p1);
    flag_wait(flags + F_LAND, NT + v + 1);
    const lds_cptr vfrag = (lds_cptr)lds + slot_off + vofs;
    bf16x8 vf[4];
#pragma unroll
    for (int i = 0; i < 4; ++i)
      vf[i] = join(ld_tr(vfrag + (i >> 1) * 512 + (i & 1) * 64), ld_tr(vfrag + 8 * VP + (i >> 1) * 512 + (i & 1) * 64));
    SBAR();
    o[0] = MFMA32(vf[0], p0, o[0]);
    o[1] = MFMA32(vf[1], p0, o[1]);
    SBAR();
    flag_set(flags + F_DONE + w * 4, NT + v + 1);
    slot_off = slot_off == (NSLOT - 1) * SLOT ? 0 : slot_off + SLOT;
    o[0] = MFMA32(vf[2], p1, o[0]);
    o[1] = MFMA32(vf[3], p1, o[1]);
  }
  STAMP(3);
  PROBE(3);
  float L = 0.f;
#pragma unroll
  for (int i = 0; i < NW; ++i) L += *(const LAS float*)((lds_cptr)lds + L_OFF + i * 128 + r * 4);

  char* zones = ws + CTRL_BYTES + (size_t)pair * 16 * ZONE;
  if (w != split) {
    const srd_t sZ = make_srd(zones + (w * 4 + split) * ZONE, ZONE);
#pragma unroll
    for (int j = 0; j < 8; ++j) {
      const f32x4 x = {o[j >> 2][4 * (j & 3)], o[j >> 2][4 * (j & 3) + 1], o[j >> 2][4 * (j & 3) + 2], o[j >> 2][4 * (j & 3) + 3]};
      __builtin_amdgcn_raw_buffer_store_b128(__builtin_bit_cast(u32x4, x), sZ, voff + j * 1024, 0, 16);
    }
    __builtin_amdgcn_raw_buffer_store_b32(__float_as_uint(h ? L : M), sZ, 8192 + lane * 4, 0, 16);
#pragma unroll 1
    for (int spin = 0; !confirmed && spin < (1 << 16); ++spin) { hs_step(); __builtin_amdgcn_s_sleep(1); }
    wait_vm<0>();
    STAMP(4);
    if (lane == 0) __hip_atomic_store((GAS unsigned*)(hs_out + 16), peer_nonce, RLX_AGENT);
  } else {
    const srd_t sZ = make_srd(zones + split * 4 * ZONE, 4 * ZONE);
    const int src = (split + 1 + (lane & 3)) & 3;
    GAS unsigned* rdy = (GAS unsigned*)(ws + ((pair * 4 + src) * 4 + split) * 256 + 128);
    const unsigned want = *(const LAS unsigned*)((lds_cptr)lds + FLAG_OFF + F_NONCE + src * 4);
    u32x4 a[3][8];
    unsigned ml[3];
    unsigned have = 0;
#pragma unroll 1
    for (int spin = 0; have != 7u && spin < (1 << 16); ++spin) {
      const unsigned got = __hip_atomic_load(rdy, RLX_AGENT);
      const unsigned fresh = (unsigned)__ballot(got == want) & 7u & ~have;
      __builtin_amdgcn_fence(__ATOMIC_ACQUIRE, "wavefront");
#pragma unroll
      for (int k = 0; k < 3; ++k)
        if (fresh & (1u << k)) {
          const unsigned zo = ((split + 1 + k) & 3) * ZONE;
#pragma unroll
          for (int j = 0; j < 8; ++j) a[k][j] = __builtin_amdgcn_raw_buffer_load_b128(sZ, voff + j * 1024, zo, 16);
          ml[k] = __builtin_amdgcn_raw_buffer_load_b32(sZ, 8192 + lane * 4, zo, 16);
        }
      have |= fresh;
      if (!fresh) __builtin_amdgcn_s_sleep(1);
    }
    STAMP(4);
    float mk[3], lk[3], G = M;
#pragma unroll
    for (int k = 0; k < 3; ++k) {
      auto x = __builtin_amdgcn_permlane32_swap(ml[k], ml[k], false, false);
      mk[k] = __uint_as_float(x[0]); lk[k] = __uint_as_float(x[1]);
      G = fmaxf(G, mk[k]);
    }
    float w0 = __builtin_amdgcn_exp2f(M - G), wk[3], den = w0 * L;
#pragma unroll
    for (int k = 0; k < 3; ++k) { wk[k] = __builtin_amdgcn_exp2f(mk[k] - G); den = fmaf(wk[k], lk[k], den); }
    const float inv = 1.0f / den;
    w0 *= inv;
#pragma unroll
    for (int k = 0; k < 3; ++k) wk[k] *= inv;
    u32x2 pk[8];
#pragma unroll
    for (int j = 0; j < 8; ++j) {
      float y[4];
#pragma unroll
      for (int e = 0; e < 4; ++e) {
        y[e] = w0 * o[j >> 2][4 * (j & 3) + e];
#pragma unroll
        for (int k = 0; k < 3; ++k) y[e] = fmaf(wk[k], __uint_as_float(a[k][j][e]), y[e]);
      }
      pk[j] = u32x2{pack2(y[0], y[1]), pack2(y[2], y[3])};
    }
    char* row = (char*)out + ((size_t)pair * 32 + qrow) * ROWB + w * 128 + h * 16;
#pragma unroll
    for (int j = 0; j < 8; j += 2) {
      auto x = __builtin_amdgcn_permlane32_swap(pk[j][0], pk[j + 1][0], false, false);
      auto y = __builtin_amdgcn_permlane32_swap(pk[j][1], pk[j + 1][1], false, false);
      *(u32x4*)(row + j * 16) = u32x4{x[0], y[0], x[1], y[1]};
    }
  }
}

}

extern "C" void kernel_launch(void* const* d_in, const int* in_sizes, int n_in, void* d_out, int out_size, void* d_ws,
                              size_t ws_size, hipStream_t stream) {
  using namespace ta;
  (void)in_sizes; (void)n_in; (void)out_size; (void)ws_size;
  (void)hipFuncSetAttribute((const void*)tree_attn, hipFuncAttributeMaxDynamicSharedMemorySize, LDS_BYTES);
  hipLaunchKernelGGL(tree_attn, dim3(NPAIR * NSPLIT), dim3((NW + 1) * 64), LDS_BYTES, stream,
                     (const __hip_bfloat16*)d_in[0], (const __hip_bfloat16*)d_in[1], (const __hip_bfloat16*)d_in[2],
                     (const __hip_bfloat16*)d_in[3], (const __hip_bfloat16*)d_in[4], (const __hip_bfloat16*)d_in[5],
                     (char*)d_ws, (__hip_bfloat16*)d_out);
}
```

```cpp
#include <hip/hip_runtime.h>
#include <hip/hip_bf16.h>

#define TA_PROBE 0
#define TA_HOLD 10000u
#define TA_PSEL (blockIdx.x == 0)
#define TA_WIN 20
#define TA_WSTEP 2
#define TA_TAU 545
#define TA_OFF 500
#define TA_LSTEP 700
#define TA_KMAX 7

#define STAMP(k) do {} while (0)

namespace ta {

constexpr int NB = 8, HKV = 8, CTX = 8192, HD = 256;
constexpr int NPAIR = NB * HKV, NSPLIT = 4, NW = 4;
constexpr int ROWB = HD * 2;
constexpr int WG_BYTES = CTX / NSPLIT * ROWB;
constexpr int NT = CTX / NSPLIT / 32;
constexpr int KP = 1040, VP = 1088;
constexpr int SLOT = 16 * VP, NSLOT = 5;
constexpr int P_OFF = NSLOT * SLOT;
constexpr int REF_OFF = P_OFF + NT / 2 * 2048;
constexpr int TV_OFF = REF_OFF + (NT + 1) * 128;
constexpr int PT_OFF = TV_OFF + 4 * ROWB;
constexpr int L_OFF = PT_OFF + 256;
constexpr int FLAG_OFF = L_OFF + NW * 128;
constexpr int F_DONE = 0, F_PRDY = 16, F_LAND = 32, F_REF = 36, F_Q = 40, F_SPARE = 44, F_NONCE = 48;
constexpr int LDS_BYTES = FLAG_OFF + 64;
constexpr int TK_OFF = REF_OFF - 4 * ROWB, Q_OFF = TK_OFF - 16 * KP;
constexpr float SCALE = 0.0625f, LOG2E = 1.4426950408889634f, C2 = SCALE * LOG2E;
constexpr int WIN = TA_WIN;
constexpr int ZONE = 8192 + 256;
constexpr int CTRL_BYTES = NPAIR * 16 * 256;
static_assert(LDS_BYTES <= 160 * 1024 && Q_OFF >= P_OFF + 16 * 2048 && FLAG_OFF % 16 == 0, "LDS map");

static_assert(WIN >= 17 && WIN + 7 * TA_WSTEP <= 48, "publish / drain ladder assumes 1..3 chunks in flight");

typedef __bf16 bf16x8 __attribute__((ext_vector_type(8)));
typedef __bf16 bf16x2 __attribute__((ext_vector_type(2)));
typedef short s16x4 __attribute__((ext_vector_type(4)));
typedef float f32x16 __attribute__((ext_vector_type(16)));
typedef float f32x4 __attribute__((ext_vector_type(4)));
typedef float f32x2 __attribute__((ext_vector_type(2)));
typedef unsigned u32x4 __attribute__((ext_vector_type(4)));
typedef unsigned u32x2 __attribute__((ext_vector_type(2)));
typedef unsigned long long u64;
#define LAS __attribute__((address_space(3)))
#define GAS __attribute__((address_space(1)))
typedef LAS const char* lds_cptr;
typedef LAS char* lds_ptr;
typedef __amdgpu_buffer_rsrc_t srd_t;

#define SBAR() __builtin_amdgcn_sched_barrier(0)
#define PIN(x) asm volatile("" : "+v"(x))
#define MFMA32(a, b, c) __builtin_amdgcn_mfma_f32_32x32x16_bf16((a), (b), (c), 0, 0, 0)
#define RLX_AGENT __ATOMIC_RELAXED, __HIP_MEMORY_SCOPE_AGENT
#define RFL(x) __builtin_amdgcn_readfirstlane(x)

__device__ __forceinline__ srd_t make_srd(const void* p, int bytes) {
  return __builtin_amdgcn_make_buffer_rsrc((void*)p, (short)0, bytes, 0x00020000);
}

template <int PAD>
__device__ __forceinline__ void dma4(srd_t srd, unsigned voff, unsigned soff, unsigned ldsb) {
  unsigned keep;
  asm volatile(
      "s_mov_b32 %0, m0\n\ts_mov_b32 m0, %4\n\ts_nop 3\n\t"
      "buffer_load_dwordx4 %1, %2, %3 offen lds\n\t"
      "s_add_u32 m0, %4, %5\n\ts_nop 0\n\t"
      "buffer_load_dwordx4 %1, %2, %3 offen offset:1024 lds\n\t"
      "s_add_u32 m0, %4, %6\n\ts_nop 0\n\t"
      "buffer_load_dwordx4 %1, %2, %3 offen offset:2048 lds\n\t"
      "s_add_u32 m0, %4, %7\n\ts_nop 0\n\t"
      "buffer_load_dwordx4 %1, %2, %3 offen offset:3072 lds\n\t"
      "s_mov_b32 m0, %0"
      : "=&s"(keep)
      : "v"(voff), "s"(srd), "s"(soff), "s"(ldsb), "n"(PAD), "n"(2 * PAD), "n"(3 * PAD)
      : "memory", "scc");
}
#define TA_PIECE(I, OFS, STEP) \
      "s_waitcnt vmcnt(%[n])\n\t" \
      "buffer_load_dwordx4 %[vo], %[srd], %[so] offen " OFS " nt lds\n\t" \
      "s_add_u32 m0, m0, " STEP "\n\t" \
      ".if (%[n] & 15) == " #I "\n\tds_write_b32 %[la], %[lv]\n\t.endif\n\t"
#define TA_GROUP(I0, I1, I2, I3) \
      TA_PIECE(I0, "", "%[pad]") TA_PIECE(I1, "offset:1024", "%[pad]") TA_PIECE(I2, "offset:2048", "%[pad]") \
      TA_PIECE(I3, "offset:3072", "%[padg]") "s_add_u32 %[so], %[so], 0x1000\n\t"
template <int N>
__device__ __forceinline__ void dma_chunk(srd_t srd, unsigned voff, unsigned soff, unsigned ldsb, unsigned pad, unsigned land_addr,
                                          int c, unsigned done_addr, u32x4& done, u64& now) {
  unsigned keep;
  asm volatile(
      "s_mov_b32 %[keep], m0\n\ts_mov_b32 m0, %[lds]\n\ts_nop 2\n\t"
      TA_GROUP(0, 1, 2, 3) TA_GROUP(4, 5, 6, 7) TA_GROUP(8, 9, 10, 11)
      "ds_read_b128 %[done], %[da]\n\ts_memrealtime %[now]\n\t"
      TA_GROUP(12, 13, 14, 15)
      "s_mov_b32 m0, %[keep]\n\ts_waitcnt lgkmcnt(0)"
      : [keep] "=&s"(keep), [so] "+s"(soff), [done] "=&v"(done), [now] "=&s"(now)
      : [vo] "v"(voff), [srd] "s"(srd), [lds] "s"(ldsb), [pad] "s"(pad), [padg] "s"(pad + 4096), [n] "n"(N),
        [la] "v"(land_addr), [lv] "v"(c - N / 16), [da] "v"(done_addr)
      : "memory", "scc");
}
__device__ __forceinline__ void dma2(srd_t srd, unsigned voff, unsigned soff, unsigned ldsb) {
  unsigned keep;
  asm volatile("s_mov_b32 %0, m0\n\ts_mov_b32 m0, %4\n\ts_nop 3\n\t"
               "buffer_load_dwordx4 %1, %2, %3 offen lds\n\t"
               "buffer_load_dwordx4 %1, %2, %3 offen offset:1024 lds\n\t"
               "s_mov_b32 m0, %0"
               : "=&s"(keep) : "v"(voff), "s"(srd), "s"(soff), "s"(ldsb) : "memory");
}
template <int N> __device__ __forceinline__ void wait_vm() { asm volatile("s_waitcnt vmcnt(%0)" ::"n"(N) : "memory"); }

__device__ __forceinline__ bf16x8 ld_frag(lds_cptr p) { return *(const LAS bf16x8*)p; }
__device__ __forceinline__ s16x4 ld_tr(lds_cptr p) { return __builtin_amdgcn_ds_read_tr16_b64_v4i16((LAS s16x4*)p); }
__device__ __forceinline__ bf16x8 join(s16x4 lo, s16x4 hi) {
  return __builtin_bit_cast(bf16x8, __builtin_shufflevector(lo, hi, 0, 1, 2, 3, 4, 5, 6, 7));
}
__device__ __forceinline__ float xhalf_max(float x) {
  auto r = __builtin_amdgcn_permlane32_swap(__float_as_uint(x), __float_as_uint(x), false, false);
  return fmaxf(__uint_as_float(r[0]), __uint_as_float(r[1]));
}
__device__ __forceinline__ float xhalf_sum(float x) {
  auto r = __builtin_amdgcn_permlane32_swap(__float_as_uint(x), __float_as_uint(x), false, false);
  return __uint_as_float(r[0]) + __uint_as_float(r[1]);
}
__device__ __forceinline__ unsigned pack2(float a, float b) {
  const bf16x2 v = {(__bf16)a, (__bf16)b};
  return __builtin_bit_cast(unsigned, v);
}
__device__ __forceinline__ bf16x8 pack8(const f32x16& p, int o) {
  bf16x8 r;
#pragma unroll
  for (int j = 0; j < 8; ++j) r[j] = (__bf16)p[o + j];
  return r;
}
__device__ __forceinline__ void flag_set(unsigned addr, unsigned v) {
  asm volatile("s_waitcnt lgkmcnt(0)\n\tds_write_b32 %0, %1" ::"v"(addr), "v"(v) : "memory");
}
__device__ __forceinline__ void flag_wait(unsigned addr, int need) {
#pragma unroll 1
  for (int spin = 0; spin < (1 << 16); ++spin) {
    unsigned v;
    asm volatile("ds_read_b32 %0, %1\n\ts_waitcnt lgkmcnt(0)" : "=v"(v) : "v"(addr) : "memory");
    if ((int)RFL(v) >= need) break;
    __builtin_amdgcn_s_sleep(1);
  }
}
__device__ __forceinline__ void flag_wait4(unsigned addr, int need) {
#pragma unroll 1
  for (int spin = 0; spin < (1 << 16); ++spin) {
    u32x4 v;
    asm volatile("ds_read_b128 %0, %1\n\ts_waitcnt lgkmcnt(0)" : "=v"(v) : "v"(addr) : "memory");
    if ((int)RFL(min(min(v[0], v[1]), min(v[2], v[3]))) >= need) break;
    __builtin_amdgcn_s_sleep(1);
  }
}

__global__ __launch_bounds__((NW + 1) * 64, 2) void tree_attn(
    const __hip_bfloat16* __restrict__ q, const __hip_bfloat16* __restrict__ kc, const __hip_bfloat16* __restrict__ vc,
    const __hip_bfloat16* __restrict__ kt, const __hip_bfloat16* __restrict__ vt, const __hip_bfloat16* __restrict__ mask,
    char* ws, __hip_bfloat16* __restrict__ out) {
  extern __shared__ __attribute__((aligned(16))) char lds[];
  const int lane = threadIdx.x & 63, wid = RFL(threadIdx.x >> 6);
  STAMP(0);
  unsigned probe_t = 0;
#define PROBE(k) do { if (TA_PROBE == (k)) probe_t = (unsigned)__builtin_amdgcn_s_memrealtime(); } while (0)
  PROBE(0);
  const unsigned lds0 = (unsigned)(uintptr_t)lds;
  const unsigned voff = lane * 16;
  const unsigned flags = lds0 + FLAG_OFF;

  if (__builtin_expect(wid == 0, 1)) {
    __builtin_amdgcn_s_setprio(3);
    const srd_t sK = make_srd((const char*)kc + (size_t)blockIdx.x * WG_BYTES, WG_BYTES);
    const srd_t sV = make_srd((const char*)vc + (size_t)blockIdx.x * WG_BYTES, WG_BYTES);
    u64 now = __builtin_amdgcn_s_memrealtime();
    const unsigned t0 = (unsigned)now + TA_OFF / 10;
    u32x4 done = {0u, 0u, 0u, 0u};
    unsigned dst = lds0;
#pragma unroll 1
    for (int c = 0; c < 2 * NT; ++c) {
      if (c >= NSLOT && (int)RFL(min(min(done[0], done[1]), min(done[2], done[3]))) < c - NSLOT + 1)
        flag_wait4(flags + F_DONE, c - NSLOT + 1);
      const int lag = (int)(RFL((unsigned)now) - t0) - (((4 * c - 1) * (TA_TAU * 64 / 10)) >> 8);
      const int k = lag <= 0 ? 0 : min((lag * (655360 / TA_LSTEP)) >> 16, TA_KMAX);
      const srd_t srd = c < NT ? sK : sV;
      const unsigned soff = (c & (NT - 1)) * 16384, pad = c < NT ? KP - 1024 : VP - 1024;
      const unsigned la = flags + (c == 0 ? F_SPARE : F_LAND);
#define TA_CH(K) dma_chunk<WIN - 1 + (K) * TA_WSTEP>(srd, voff, soff, dst, pad, la, c, flags + F_DONE, done, now)
      if (k < 4) {
        if (k < 2) { if (k == 0) TA_CH(0); else TA_CH(1); } else { if (k == 2) TA_CH(2); else TA_CH(3); }
      } else {
        if (k < 6) { if (k == 4) TA_CH(4); else TA_CH(5); } else { if (k == 6) TA_CH(6); else TA_CH(7); }
      }
#undef TA_CH
      if (c == 0) {
        if (lane < 16) flag_set(flags + lane * 4, lane < 4 ? lane : 0);
        asm volatile("s_waitcnt lgkmcnt(0)\n\ts_barrier" ::: "memory");
      }
      dst = dst == lds0 + (NSLOT - 1) * SLOT ? lds0 : dst + SLOT;
    }
    wait_vm<32>(); flag_set(flags + F_LAND, 2 * NT - 2);
    wait_vm<16>(); flag_set(flags + F_LAND, 2 * NT - 1);
    wait_vm<0>(); flag_set(flags + F_LAND, 2 * NT);
    return;
  }

  const int w = wid - 1;
  const int r = lane & 31, h = lane >> 5;
  const int pair = blockIdx.x >> 2, split = blockIdx.x & 3;
  dma4<KP - 1024>(make_srd((const char*)q + (size_t)pair * 32 * ROWB, 32 * ROWB), voff, w * 4096, lds0 + Q_OFF + w * 4 * KP);
  if (w == 3) dma2(make_srd((const char*)kt + (size_t)pair * 16 * ROWB, 16 * ROWB), voff, split * 4 * ROWB, lds0 + TK_OFF);
  if (w == 2) dma2(make_srd((const char*)vt + (size_t)pair * 16 * ROWB, 16 * ROWB), voff, split * 4 * ROWB, lds0 + TV_OFF);
  const int qrow = 2 * (r & 15) + (r >> 4);
  const u32x2 mbits = *(const u32x2*)((const char*)mask + (((pair >> 3) * 16 + (qrow & 15)) * 16 + 4 * split) * 2);

  GAS u64* hs_out = (GAS u64*)(ws + ((pair * 4 + split) * 4 + w) * 256);
  GAS u64* hs_in = (GAS u64*)(ws + ((pair * 4 + w) * 4 + split) * 256);
  u64 hs_word = __hip_atomic_load(hs_in, RLX_AGENT);
  const unsigned rdy0 = __hip_atomic_load((GAS unsigned*)(hs_in + 16), RLX_AGENT);

  const int koff = (r & 15) * KP + (r >> 4) * 512 + h * 16;
  const int trofs = ((lane >> 4) & 1) * 32 + (lane & 3) * 8 + w * 128;
  const int vofs = (4 * h + ((lane & 15) >> 2)) * VP + trofs;
  asm volatile("s_barrier" ::: "memory");
  wait_vm<0>();
  if (lane == 0) asm volatile("ds_add_u32 %0, %1" ::"v"(flags + F_Q), "v"(1u) : "memory");

  unsigned nonce = RFL((unsigned)(hs_word >> 32)) + 1, peer_nonce = RFL((unsigned)hs_word);
  if (nonce == RFL(rdy0)) ++nonce;
  bool confirmed = w == split;
  if (!confirmed) {
    if (lane == 0) __hip_atomic_store(hs_out, ((u64)peer_nonce << 32) | nonce, RLX_AGENT);
    flag_set(flags + F_NONCE + w * 4, nonce);
    hs_word = __hip_atomic_load(hs_in, RLX_AGENT);
  }
  auto hs_step = [&]() {
    const unsigned x = RFL((unsigned)hs_word), y = RFL((unsigned)(hs_word >> 32));
    if (x != peer_nonce) {
      peer_nonce = x;
      if (lane == 0) __hip_atomic_store(hs_out, ((u64)peer_nonce << 32) | nonce, RLX_AGENT);
    }
    if (y == nonce) confirmed = true;
    else hs_word = __hip_atomic_load(hs_in, RLX_AGENT);
  };

  flag_wait(flags + F_Q, NW);
  STAMP(1);
  bf16x8 qf[16];
#pragma unroll
  for (int kk = 0; kk < 16; ++kk) qf[kk] = ld_frag((lds_cptr)lds + Q_OFF + koff + kk * 32);

  const lds_ptr refp = (lds_ptr)lds + REF_OFF + r * 4;
  float m = -INFINITY, l = 0.f;
  if (w == 3) {
    const lds_cptr tk = (lds_cptr)lds + TK_OFF + (r & 3) * ROWB + h * 16;
    f32x16 s = f32x16{};
#pragma unroll
    for (int kk = 0; kk < 16; ++kk) s = MFMA32(ld_frag(tk + kk * 32), qf[kk], s);
#pragma unroll
    for (int i = 0; i < 4; ++i) {
      const float mk = __uint_as_float((i & 1) ? (mbits[i >> 1] & 0xffff0000u) : (mbits[i >> 1] << 16));
      s[i] = fmaf(s[i], SCALE, (1.0f - mk) * -1000000000.0f) * LOG2E;
      m = fmaxf(m, s[i]);
    }
#pragma unroll
    for (int i = 0; i < 4; ++i) { s[i] = __builtin_amdgcn_exp2f(s[i] - m); l += s[i]; }
    if (h) l = 0.f;
    *(LAS float*)refp = m;
    *(LAS u32x2*)((lds_ptr)lds + PT_OFF + r * 8) = u32x2{pack2(s[0], s[1]), pack2(s[2], s[3])};
    flag_set(flags + F_REF, 1);
  }
  STAMP(2);
  if (TA_PROBE == 1) { flag_wait(flags + F_LAND, 1); PROBE(1); }

  int slot_off = w * SLOT;
  const lds_ptr pbuf = (lds_ptr)lds + P_OFF + lane * 16;
  bf16x8 preg[NT / 8][2];

  auto k_tile = [&](int c, bf16x8& p0, bf16x8& p1) {
    flag_wait(flags + F_LAND, c + 1);
    const lds_cptr kfrag = (lds_cptr)lds + slot_off + koff;
    f32x16 s = f32x16{};
    bf16x8 kf[8];
#pragma unroll
    for (int kk = 0; kk < 8; ++kk) kf[kk] = ld_frag(kfrag + kk * 32);
    SBAR();
#pragma unroll
    for (int kk = 0; kk < 8; ++kk) { s = MFMA32(kf[kk], qf[kk], s); kf[kk] = ld_frag(kfrag + (kk + 8) * 32); SBAR(); }
    flag_set(flags + F_DONE + w * 4, min(c + NW, NT));
    slot_off = slot_off == 0 ? (NSLOT - 1) * SLOT : slot_off - SLOT;
    SBAR();
#pragma unroll
    for (int kk = 0; kk < 8; ++kk) s = MFMA32(kf[kk], qf[kk + 8], s);
    float tmax = fmaxf(s[0], s[1]);
#pragma unroll
    for (int i = 2; i < 16; ++i) tmax = fmaxf(tmax, s[i]);
    tmax = xhalf_max(tmax) * C2;
    PIN(tmax);
    flag_wait(flags + F_REF, c + 1);
    const float mn = fmaxf(*(const LAS float*)(refp + c * 128), tmax);
    *(LAS float*)(refp + (c + 1) * 128) = mn;
    flag_set(flags + F_REF, c + 2);
    const float alpha = __builtin_amdgcn_exp2f(m - mn);
    m = mn;
    float ps = 0.f;
#pragma unroll
    for (int i = 0; i < 16; ++i) { s[i] = __builtin_amdgcn_exp2f(fmaf(s[i], C2, -mn)); ps += s[i]; }
    l = fmaf(l, alpha, ps);
    p0 = pack8(s, 0); p1 = pack8(s, 8);
    PIN(l); PIN(m);
  };
#pragma unroll 1
  for (int c = w; c < NT / 2; c += NW) {
    bf16x8 p0, p1;
    k_tile(c, p0, p1);
    *(LAS bf16x8*)(pbuf + c * 2048) = p0;
    *(LAS bf16x8*)(pbuf + c * 2048 + 1024) = p1;
    if (!confirmed) hs_step();
  }
#pragma unroll
  for (int t = 0; t < NT / 8; ++t) { k_tile(NT / 2 + 4 * t + w, preg[t][0], preg[t][1]); PIN(preg[t][0]); PIN(preg[t][1]); }
  flag_set(flags + F_PRDY + w * 4, 1);

  f32x16 o[2];
  float rcur = *(const LAS float*)refp;
  {
    const lds_cptr tv = (lds_cptr)lds + TV_OFF + ((lane & 15) >> 2) * ROWB + trofs;
    u32x2 pt2 = *(const LAS u32x2*)((lds_cptr)lds + PT_OFF + r * 8);
    if (h) pt2 = u32x2{0u, 0u};
    const bf16x8 pt = __builtin_bit_cast(bf16x8, u32x4{pt2[0], pt2[1], 0u, 0u});
#pragma unroll
    for (int cb = 0; cb < 2; ++cb) { const s16x4 v4 = ld_tr(tv + cb * 64); o[cb] = MFMA32(join(v4, v4), pt, f32x16{}); }
  }
  flag_wait4(flags + F_PRDY, 1);
  PROBE(2);
  const float M = *(const LAS float*)(refp + NT * 128);
  *(LAS float*)((lds_ptr)lds + L_OFF + w * 128 + r * 4) = xhalf_sum(l) * __builtin_amdgcn_exp2f(m - M);
  slot_off = (NT % NSLOT) * SLOT;
#pragma unroll 1
  for (int v = 0; v < NT; ++v) {
    if (v == 20 || v == 36) {
      flag_wait4(flags + F_DONE, NT + v - 4);
      if (v == 20) {
#pragma unroll
        for (int j = 0; j < 4; ++j) { *(LAS bf16x8*)(pbuf + (4 * j + w) * 2048) = preg[j][0]; *(LAS bf16x8*)(pbuf + (4 * j + w) * 2048 + 1024) = preg[j][1]; }
      } else {
#pragma unroll
        for (int j = 4; j < 8; ++j) { *(LAS bf16x8*)(pbuf + (4 * j + w) * 2048) = preg[j][0]; *(LAS bf16x8*)(pbuf + (4 * j + w) * 2048 + 1024) = preg[j][1]; }
      }
      flag_set(flags + F_PRDY + w * 4, v == 20 ? 2 : 3);
    }
    if (v == 32 || v == 48) flag_wait4(flags + F_PRDY, v == 32 ? 2 : 3);
    const float rnew = *(const LAS float*)(refp + (v + 1) * 128);
    bf16x8 p0 = ld_frag(pbuf + (v & (NT / 2 - 1)) * 2048), p1 = ld_frag(pbuf + (v & (NT / 2 - 1)) * 2048 + 1024);
    if (__any(rnew != rcur)) {
      const float alpha = __builtin_amdgcn_exp2f(rcur - rnew);
      o[0] *= alpha; o[1] *= alpha;
      rcur = rnew;
    }
    PIN(p0); PIN(p1);
    flag_wait(flags + F_LAND, NT + v + 1);
    const lds_cptr vfrag = (lds_cptr)lds + slot_off + vofs;
    bf16x8 vf[4];
#pragma unroll
    for (int i = 0; i < 4; ++i)
      vf[i] = join(ld_tr(vfrag + (i >> 1) * 512 + (i & 1) * 64), ld_tr(vfrag + 8 * VP + (i >> 1) * 512 + (i & 1) * 64));
    SBAR();
    o[0] = MFMA32(vf[0], p0, o[0]);
    o[1] = MFMA32(vf[1], p0, o[1]);
    SBAR();
    flag_set(flags + F_DONE + w * 4, NT + v + 1);
    slot_off = slot_off == (NSLOT - 1) * SLOT ? 0 : slot_off + SLOT;
    o[0] = MFMA32(vf[2], p1, o[0]);
    o[1] = MFMA32(vf[3], p1, o[1]);
  }
  STAMP(3);
  PROBE(3);
  float L = 0.f;
#pragma unroll
  for (int i = 0; i < NW; ++i) L += *(const LAS float*)((lds_cptr)lds + L_OFF + i * 128 + r * 4);

  char* zones = ws + CTRL_BYTES + (size_t)pair * 16 * ZONE;
  if (w != split) {
    const srd_t sZ = make_srd(zones + (w * 4 + split) * ZONE, ZONE);
#pragma unroll
    for (int j = 0; j < 8; ++j) {
      const f32x4 x = {o[j >> 2][4 * (j & 3)], o[j >> 2][4 * (j & 3) + 1], o[j >> 2][4 * (j & 3) + 2], o[j >> 2][4 * (j & 3) + 3]};
      __builtin_amdgcn_raw_buffer_store_b128(__builtin_bit_cast(u32x4, x), sZ, voff + j * 1024, 0, 16);
    }
    __builtin_amdgcn_raw_buffer_store_b32(__float_as_uint(h ? L : M), sZ, 8192 + lane * 4, 0, 16);
#pragma unroll 1
    for (int spin = 0; !confirmed && spin < (1 << 16); ++spin) { hs_step(); __builtin_amdgcn_s_sleep(1); }
    wait_vm<0>();
    STAMP(4);
    if (lane == 0) __hip_atomic_store((GAS unsigned*)(hs_out + 16), peer_nonce, RLX_AGENT);
  } else {
    const srd_t sZ = make_srd(zones + split * 4 * ZONE, 4 * ZONE);
    const int src = (split + 1 + (lane & 3)) & 3;
    GAS unsigned* rdy = (GAS unsigned*)(ws + ((pair * 4 + src) * 4 + split) * 256 + 128);
    const unsigned want = *(const LAS unsigned*)((lds_cptr)lds + FLAG_OFF + F_NONCE + src * 4);
    u32x4 a[3][8];
    unsigned ml[3];
    unsigned have = 0;
#pragma unroll 1
    for (int spin = 0; have != 7u && spin < (1 << 16); ++spin) {
      const unsigned got = __hip_atomic_load(rdy, RLX_AGENT);
      const unsigned fresh = (unsigned)__ballot(got == want) & 7u & ~have;
      __builtin_amdgcn_fence(__ATOMIC_ACQUIRE, "wavefront");
#pragma unroll
      for (int k = 0; k < 3; ++k)
        if (fresh & (1u << k)) {
          const unsigned zo = ((split + 1 + k) & 3) * ZONE;
#pragma unroll
          for (int j = 0; j < 8; ++j) a[k][j] = __builtin_amdgcn_raw_buffer_load_b128(sZ, voff + j * 1024, zo, 16);
          ml[k] = __builtin_amdgcn_raw_buffer_load_b32(sZ, 8192 + lane * 4, zo, 16);
        }
      have |= fresh;
      if (!fresh) __builtin_amdgcn_s_sleep(1);
    }
    STAMP(4);
    float mk[3], lk[3], G = M;
#pragma unroll
    for (int k = 0; k < 3; ++k) {
      auto x = __builtin_amdgcn_permlane32_swap(ml[k], ml[k], false, false);
      mk[k] = __uint_as_float(x[0]); lk[k] = __uint_as_float(x[1]);
      G = fmaxf(G, mk[k]);
    }
    float w0 = __builtin_amdgcn_exp2f(M - G), wk[3], den = w0 * L;
#pragma unroll
    for (int k = 0; k < 3; ++k) { wk[k] = __builtin_amdgcn_exp2f(mk[k] - G); den = fmaf(wk[k], lk[k], den); }
    const float inv = 1.0f / den;
    w0 *= inv;
#pragma unroll
    for (int k = 0; k < 3; ++k) wk[k] *= inv;
    u32x2 pk[8];
#pragma unroll
    for (int j = 0; j < 8; ++j) {
      float y[4];
#pragma unroll
      for (int e = 0; e < 4; ++e) {
        y[e] = w0 * o[j >> 2][4 * (j & 3) + e];
#pragma unroll
        for (int k = 0; k < 3; ++k) y[e] = fmaf(wk[k], __uint_as_float(a[k][j][e]), y[e]);
      }
      pk[j] = u32x2{pack2(y[0], y[1]), pack2(y[2], y[3])};
    }
    char* row = (char*)out + ((size_t)pair * 32 + qrow) * ROWB + w * 128 + h * 16;
#pragma unroll
    for (int j = 0; j < 8; j += 2) {
      auto x = __builtin_amdgcn_permlane32_swap(pk[j][0], pk[j + 1][0], false, false);
      auto y = __builtin_amdgcn_permlane32_swap(pk[j][1], pk[j + 1][1], false, false);
      *(u32x4*)(row + j * 16) = u32x4{x[0], y[0], x[1], y[1]};
    }
    if (TA_PROBE == 4) { wait_vm<0>(); PROBE(4); }
    while ((TA_PSEL) && (unsigned)__builtin_amdgcn_s_memrealtime() - probe_t < TA_HOLD) __builtin_amdgcn_s_sleep(1);
  }
}

}

extern "C" void kernel_launch(void* const* d_in, const int* in_sizes, int n_in, void* d_out, int out_size, void* d_ws,
                              size_t ws_size, hipStream_t stream) {
  using namespace ta;
  (void)in_sizes; (void)n_in; (void)out_size; (void)ws_size;
  (void)hipFuncSetAttribute((const void*)tree_attn, hipFuncAttributeMaxDynamicSharedMemorySize, LDS_BYTES);
  hipLaunchKernelGGL(tree_attn, dim3(NPAIR * NSPLIT), dim3((NW + 1) * 64), LDS_BYTES, stream,
                     (const __hip_bfloat16*)d_in[0], (const __hip_bfloat16*)d_in[1], (const __hip_bfloat16*)d_in[2],
                     (const __hip_bfloat16*)d_in[3], (const __hip_bfloat16*)d_in[4], (const __hip_bfloat16*)d_in[5],
                     (char*)d_ws, (__hip_bfloat16*)d_out);
}
```

```cpp
#include <hip/hip_runtime.h>
#include <hip/hip_bf16.h>

#define TA_WIN 20
#define TA_WSTEP 2
#define TA_TAU 545
#define TA_OFF 1000
#define TA_LSTEP 700
#define TA_BOOST0 0
#define TA_BOOST1 0
#define TA_CODEPF 20
#define TA_KNEE 7
#define TA_KDIV 4
#define TA_POLL2 1
#define TA_QNT 0
#define TA_SIGLINE 1
#define TA_KMAX 7

#define STAMP(k) do {} while (0)

namespace ta {

constexpr int NB = 8, HKV = 8, CTX = 8192, HD = 256;
constexpr int NPAIR = NB * HKV, NSPLIT = 4, NW = 4;
constexpr int ROWB = HD * 2;
constexpr int WG_BYTES = CTX / NSPLIT * ROWB;
constexpr int NT = CTX / NSPLIT / 32;
constexpr int KP = 1040, VP = 1056;
constexpr int SLOT = 16 * VP, NSLOT = 5;
constexpr int P_OFF = NSLOT * SLOT;
constexpr int REF_OFF = P_OFF + NT / 2 * 2048;
constexpr int TV_OFF = REF_OFF + (NT + 1) * 128;
constexpr int PT_OFF = TV_OFF + 4 * ROWB;
constexpr int L_OFF = PT_OFF + 256;
constexpr int FLAG_OFF = L_OFF + NW * 128;
constexpr int F_DONE = 0, F_PRDY = 16, F_LAND = 32, F_REF = 36, F_Q = 40;
constexpr int LDS_BYTES = FLAG_OFF + 48;
constexpr int TK_OFF = REF_OFF - 4 * ROWB, Q_OFF = TK_OFF - 16 * KP;
constexpr float SCALE = 0.0625f, LOG2E = 1.4426950408889634f, C2 = SCALE * LOG2E;
constexpr int WIN = TA_WIN;
constexpr int MSG = 1024 + 256;
constexpr int CTRL_BYTES = NPAIR * 64 * 128;
static_assert(LDS_BYTES <= 160 * 1024 && Q_OFF >= P_OFF + 16 * 2048 && FLAG_OFF % 16 == 0, "LDS map");

static_assert(WIN >= 17 && WIN + 7 * TA_WSTEP <= 48, "publish / drain ladder assumes 1..3 chunks in flight");

typedef __bf16 bf16x8 __attribute__((ext_vector_type(8)));
typedef __bf16 bf16x2 __attribute__((ext_vector_type(2)));
typedef short s16x4 __attribute__((ext_vector_type(4)));
typedef float f32x16 __attribute__((ext_vector_type(16)));
typedef float f32x4 __attribute__((ext_vector_type(4)));
typedef float f32x2 __attribute__((ext_vector_type(2)));
typedef unsigned u32x4 __attribute__((ext_vector_type(4)));
typedef unsigned u32x2 __attribute__((ext_vector_type(2)));
typedef unsigned long long u64;
#define LAS __attribute__((address_space(3)))
#define GAS __attribute__((address_space(1)))
typedef LAS const char* lds_cptr;
typedef LAS char* lds_ptr;
typedef __amdgpu_buffer_rsrc_t srd_t;

#define SBAR() __builtin_amdgcn_sched_barrier(0)
#define PIN(x) asm volatile("" : "+v"(x))
#define MFMA32(a, b, c) __builtin_amdgcn_mfma_f32_32x32x16_bf16((a), (b), (c), 0, 0, 0)
#define RLX_AGENT __ATOMIC_RELAXED, __HIP_MEMORY_SCOPE_AGENT
#define RFL(x) __builtin_amdgcn_readfirstlane(x)

__device__ __forceinline__ srd_t make_srd(const void* p, int bytes) {
  return __builtin_amdgcn_make_buffer_rsrc((void*)p, (short)0, bytes, 0x00020000);
}

template <int PAD, bool NTL>
__device__ __forceinline__ void dma4(srd_t srd, unsigned voff, unsigned soff, unsigned ldsb) {
  unsigned keep;
  if constexpr (NTL) {
  asm volatile(
      "s_mov_b32 %0, m0\n\ts_mov_b32 m0, %4\n\ts_nop 3\n\t"
      "buffer_load_dwordx4 %1, %2, %3 offen nt lds\n\t"
      "s_add_u32 m0, %4, %5\n\ts_nop 0\n\t"
      "buffer_load_dwordx4 %1, %2, %3 offen offset:1024 nt lds\n\t"
      "s_add_u32 m0, %4, %6\n\ts_nop 0\n\t"
      "buffer_load_dwordx4 %1, %2, %3 offen offset:2048 nt lds\n\t"
      "s_add_u32 m0, %4, %7\n\ts_nop 0\n\t"
      "buffer_load_dwordx4 %1, %2, %3 offen offset:3072 nt lds\n\t"
      "s_mov_b32 m0, %0"
      : "=&s"(keep)
      : "v"(voff), "s"(srd), "s"(soff), "s"(ldsb), "n"(PAD), "n"(2 * PAD), "n"(3 * PAD)
      : "memory", "scc");
  } else {
  asm volatile(
      "s_mov_b32 %0, m0\n\ts_mov_b32 m0, %4\n\ts_nop 3\n\t"
      "buffer_load_dwordx4 %1, %2, %3 offen lds\n\t"
      "s_add_u32 m0, %4, %5\n\ts_nop 0\n\t"
      "buffer_load_dwordx4 %1, %2, %3 offen offset:1024 lds\n\t"
      "s_add_u32 m0, %4, %6\n\ts_nop 0\n\t"
      "buffer_load_dwordx4 %1, %2, %3 offen offset:2048 lds\n\t"
      "s_add_u32 m0, %4, %7\n\ts_nop 0\n\t"
      "buffer_load_dwordx4 %1, %2, %3 offen offset:3072 lds\n\t"
      "s_mov_b32 m0, %0"
      : "=&s"(keep)
      : "v"(voff), "s"(srd), "s"(soff), "s"(ldsb), "n"(PAD), "n"(2 * PAD), "n"(3 * PAD)
      : "memory", "scc");
  }
}
#define TA_PIECE(I, OFS, STEP) \
      "s_waitcnt vmcnt(%[n])\n\t" \
      "buffer_load_dwordx4 %[vo], %[srd], %[so] offen " OFS " nt lds\n\t" \
      "s_add_u32 m0, m0, " STEP "\n\t" \
      ".if (%[n] & 15) == " #I "\n\tds_write_b32 %[la], %[lv]\n\t.endif\n\t"
#define TA_GROUP(I0, I1, I2, I3) \
      TA_PIECE(I0, "", "%[pad]") TA_PIECE(I1, "offset:1024", "%[pad]") TA_PIECE(I2, "offset:2048", "%[pad]") \
      TA_PIECE(I3, "offset:3072", "%[padg]") "s_add_u32 %[so], %[so], 0x1000\n\t"
template <int N>
__device__ __forceinline__ void dma_chunk(srd_t srd, unsigned voff, unsigned soff, unsigned ldsb, unsigned pad, unsigned land_addr,
                                          int c, unsigned done_addr, u32x4& done, u64& now) {
  unsigned keep;
  asm volatile(
      "s_mov_b32 %[keep], m0\n\ts_mov_b32 m0, %[lds]\n\ts_nop 2\n\t"
      TA_GROUP(0, 1, 2, 3) TA_GROUP(4, 5, 6, 7) TA_GROUP(8, 9, 10, 11)
      "ds_read_b128 %[done], %[da]\n\ts_memrealtime %[now]\n\t"
      TA_GROUP(12, 13, 14, 15)
      "s_mov_b32 m0, %[keep]\n\ts_waitcnt lgkmcnt(0)"
      : [keep] "=&s"(keep), [so] "+s"(soff), [done] "=&v"(done), [now] "=&s"(now)
      : [vo] "v"(voff), [srd] "s"(srd), [lds] "s"(ldsb), [pad] "s"(pad), [padg] "s"(pad + 4096), [n] "n"(N),
        [la] "v"(land_addr), [lv] "v"(c - N / 16), [da] "v"(done_addr)
      : "memory", "scc");
}
__device__ __forceinline__ void dma1(srd_t srd, unsigned voff, unsigned soff, unsigned ldsb) {
  unsigned keep;
  asm volatile("s_mov_b32 %0, m0\n\ts_mov_b32 m0, %4\n\ts_nop 3\n\t"
               "buffer_load_dwordx4 %1, %2, %3 offen lds\n\t"
               "s_mov_b32 m0, %0"
               : "=&s"(keep) : "v"(voff), "s"(srd), "s"(soff), "s"(ldsb) : "memory");
}
__device__ __forceinline__ void dma2(srd_t srd, unsigned voff, unsigned soff, unsigned ldsb) {
  unsigned keep;
  asm volatile("s_mov_b32 %0, m0\n\ts_mov_b32 m0, %4\n\ts_nop 3\n\t"
               "buffer_load_dwordx4 %1, %2, %3 offen lds\n\t"
               "buffer_load_dwordx4 %1, %2, %3 offen offset:1024 lds\n\t"
               "s_mov_b32 m0, %0"
               : "=&s"(keep) : "v"(voff), "s"(srd), "s"(soff), "s"(ldsb) : "memory");
}
template <int N> __device__ __forceinline__ void wait_vm() { asm volatile("s_waitcnt vmcnt(%0)" ::"n"(N) : "memory"); }

__device__ __forceinline__ bf16x8 ld_frag(lds_cptr p) { return *(const LAS bf16x8*)p; }
__device__ __forceinline__ s16x4 ld_tr(lds_cptr p) { return __builtin_amdgcn_ds_read_tr16_b64_v4i16((LAS s16x4*)p); }
__device__ __forceinline__ bf16x8 join(s16x4 lo, s16x4 hi) {
  return __builtin_bit_cast(bf16x8, __builtin_shufflevector(lo, hi, 0, 1, 2, 3, 4, 5, 6, 7));
}
__device__ __forceinline__ float xhalf_max(float x) {
  auto r = __builtin_amdgcn_permlane32_swap(__float_as_uint(x), __float_as_uint(x), false, false);
  return fmaxf(__uint_as_float(r[0]), __uint_as_float(r[1]));
}
__device__ __forceinline__ float xhalf_sum(float x) {
  auto r = __builtin_amdgcn_permlane32_swap(__float_as_uint(x), __float_as_uint(x), false, false);
  return __uint_as_float(r[0]) + __uint_as_float(r[1]);
}
__device__ __forceinline__ unsigned pack2(float a, float b) {
  const bf16x2 v = {(__bf16)a, (__bf16)b};
  return __builtin_bit_cast(unsigned, v);
}
__device__ __forceinline__ bf16x8 pack8(const f32x16& p, int o) {
  bf16x8 r;
#pragma unroll
  for (int j = 0; j < 8; ++j) r[j] = (__bf16)p[o + j];
  return r;
}
__device__ __forceinline__ void flag_set(unsigned addr, unsigned v) {
  asm volatile("s_waitcnt lgkmcnt(0)\n\tds_write_b32 %0, %1" ::"v"(addr), "v"(v) : "memory");
}
__device__ __forceinline__ void flag_wait(unsigned addr, int need) {
#pragma unroll 1
  for (int spin = 0; spin < (1 << 16); ++spin) {
    unsigned v;
    asm volatile("ds_read_b32 %0, %1\n\ts_waitcnt lgkmcnt(0)" : "=v"(v) : "v"(addr) : "memory");
    if ((int)RFL(v) >= need) break;
    __builtin_amdgcn_s_sleep(1);
  }
}
__device__ __forceinline__ void flag_wait4(unsigned addr, int need) {
#pragma unroll 1
  for (int spin = 0; spin < (1 << 16); ++spin) {
    u32x4 v;
    asm volatile("ds_read_b128 %0, %1\n\ts_waitcnt lgkmcnt(0)" : "=v"(v) : "v"(addr) : "memory");
    if ((int)RFL(min(min(v[0], v[1]), min(v[2], v[3]))) >= need) break;
    __builtin_amdgcn_s_sleep(1);
  }
}

__global__ __launch_bounds__((NW + 1) * 64, 2) void tree_attn(
    const __hip_bfloat16* __restrict__ q, const __hip_bfloat16* __restrict__ kc, const __hip_bfloat16* __restrict__ vc,
    const __hip_bfloat16* __restrict__ kt, const __hip_bfloat16* __restrict__ vt, const __hip_bfloat16* __restrict__ mask,
    char* ws, __hip_bfloat16* __restrict__ out) {
  extern __shared__ __attribute__((aligned(16))) char lds[];
  const int lane = threadIdx.x & 63, wid = RFL(threadIdx.x >> 6);
  STAMP(0);
#define PROBE(k) do {} while (0)
  PROBE(0);
  const unsigned lds0 = (unsigned)(uintptr_t)lds;
  const unsigned voff = lane * 16;
  const unsigned flags = lds0 + FLAG_OFF;

  const int pair = blockIdx.x >> 2, split = blockIdx.x & 3;
  u64 now = __builtin_amdgcn_s_memrealtime();
  if (wid == 1) dma1(make_srd((const void*)(__builtin_amdgcn_s_getpc() & ~255ull), TA_CODEPF * 1024), voff + (blockIdx.x >> 3) * 1024, 0, lds0 + P_OFF + 20 * 2048);
  {
    const char* src = wid == 0 ? (const char*)kc + (size_t)blockIdx.x * WG_BYTES : (const char*)q + (size_t)pair * 32 * ROWB + (wid - 1) * 4096;
    const srd_t s0 = make_srd(src, 16384);
    const unsigned d0 = wid == 0 ? lds0 : lds0 + Q_OFF + (wid - 1) * 4 * KP;
    const int ng = wid == 0 ? 4 : 1;
#pragma unroll 1
    for (int g = 0; g < ng; ++g) {
      if (TA_QNT || wid == 0) dma4<KP - 1024, true>(s0, voff, g * 4096, d0 + g * 4 * KP);
      else dma4<KP - 1024, false>(s0, voff, g * 4096, d0 + g * 4 * KP);
    }
  }
  if (threadIdx.x < 12) flag_set(flags + lane * 4, lane < 4 ? lane : 0);
  asm volatile("s_waitcnt lgkmcnt(0)\n\ts_barrier" ::: "memory");

  if (wid == 0) {
    __builtin_amdgcn_s_setprio(3);
    const srd_t sK = make_srd((const char*)kc + (size_t)blockIdx.x * WG_BYTES, WG_BYTES);
    const srd_t sV = make_srd((const char*)vc + (size_t)blockIdx.x * WG_BYTES, WG_BYTES);
    const unsigned t0 = (unsigned)now + TA_OFF / 10;
    u32x4 done = {0u, 0u, 0u, 0u};
    unsigned dst = lds0 + SLOT;
#pragma unroll 1
    for (int c = 1; c < 2 * NT; ++c) {
      if (c >= NSLOT && (int)RFL(min(min(done[0], done[1]), min(done[2], done[3]))) < c - NSLOT + 1)
        flag_wait4(flags + F_DONE, c - NSLOT + 1);
      const int lag = (int)(RFL((unsigned)now) - t0) - (((4 * c - 1) * (TA_TAU * 64 / 10)) >> 8);
      int k = lag <= 0 ? 0 : (lag * (655360 / TA_LSTEP)) >> 16;
      if (k > TA_KNEE) k = min(TA_KNEE + (k - TA_KNEE) / TA_KDIV, TA_KMAX);
      if (c < TA_BOOST0 || c >= 2 * NT - TA_BOOST1) k = 7;
      const srd_t srd = c < NT ? sK : sV;
      const unsigned soff = (c & (NT - 1)) * 16384, pad = c < NT ? KP - 1024 : VP - 1024;
#define TA_CH(K) dma_chunk<WIN - 1 + (K) * TA_WSTEP>(srd, voff, soff, dst, pad, flags + F_LAND, c, flags + F_DONE, done, now)
      if (k < 4) {
        if (k < 2) { if (k == 0) TA_CH(0); else TA_CH(1); } else { if (k == 2) TA_CH(2); else TA_CH(3); }
      } else {
        if (k < 6) { if (k == 4) TA_CH(4); else TA_CH(5); } else { if (k == 6) TA_CH(6); else TA_CH(7); }
      }
#undef TA_CH
      dst = dst == lds0 + (NSLOT - 1) * SLOT ? lds0 : dst + SLOT;
    }
    wait_vm<32>(); flag_set(flags + F_LAND, 2 * NT - 2);
    wait_vm<16>(); flag_set(flags + F_LAND, 2 * NT - 1);
    wait_vm<0>(); flag_set(flags + F_LAND, 2 * NT);
    return;
  }

  const int w = wid - 1;
  const int r = lane & 31, h = lane >> 5;
  if (w == 3) dma2(make_srd((const char*)kt + (size_t)pair * 16 * ROWB, 16 * ROWB), voff, split * 4 * ROWB, lds0 + TK_OFF);
  if (w == 2) dma2(make_srd((const char*)vt + (size_t)pair * 16 * ROWB, 16 * ROWB), voff, split * 4 * ROWB, lds0 + TV_OFF);
  const int qrow = 2 * (r & 15) + (r >> 4);
  u32x2 mbits = {0u, 0u};
  if (w == 3) mbits = *(const u32x2*)((const char*)mask + (((pair >> 3) * 16 + (qrow & 15)) * 16 + 4 * split) * 2);
  const int koff = (r & 15) * KP + (r >> 4) * 512 + h * 16;
  const int trofs = ((lane >> 4) & 1) * 128 + w * 32 + (lane & 3) * 8;
  const int vofs = (4 * h + ((lane & 15) >> 2)) * VP + trofs;
  const srd_t sZ = make_srd(ws + CTRL_BYTES + (size_t)pair * 64 * MSG, 64 * MSG);
  const int peer = (split + 1 + (lane & 3)) & 3;
  GAS u64* sig_in = (GAS u64*)(ws + ((pair * 4 + split) * 4 + w) * 128) + peer;
  GAS u64* sig_out = (GAS u64*)(ws + ((pair * 4 + peer) * 4 + w) * 128) + split;
  unsigned zin[3];
#pragma unroll
  for (int k = 0; k < 3; ++k) zin[k] = ((split * 4 + ((split + 1 + k) & 3)) * 4 + w) * MSG;
  wait_vm<0>();
  if (lane == 0) asm volatile("ds_add_u32 %0, %1" ::"v"(flags + F_Q), "v"(1u) : "memory");
  flag_wait(flags + F_Q, NW);
  STAMP(1);
  bf16x8 qf[16];
#pragma unroll
  for (int kk = 0; kk < 16; ++kk) qf[kk] = ld_frag((lds_cptr)lds + Q_OFF + koff + kk * 32);

  const lds_ptr refp = (lds_ptr)lds + REF_OFF + r * 4;
  float m = -INFINITY, l = 0.f;
  if (w == 3) {
    const lds_cptr tk = (lds_cptr)lds + TK_OFF + (r & 3) * ROWB + h * 16;
    f32x16 s = f32x16{};
#pragma unroll
    for (int kk = 0; kk < 16; ++kk) s = MFMA32(ld_frag(tk + kk * 32), qf[kk], s);
#pragma unroll
    for (int i = 0; i < 4; ++i) {
      const float mk = __uint_as_float((i & 1) ? (mbits[i >> 1] & 0xffff0000u) : (mbits[i >> 1] << 16));
      s[i] = fmaf(s[i], SCALE, (1.0f - mk) * -1000000000.0f) * LOG2E;
      m = fmaxf(m, s[i]);
    }
#pragma unroll
    for (int i = 0; i < 4; ++i) { s[i] = __builtin_amdgcn_exp2f(s[i] - m); l += s[i]; }
    if (h) l = 0.f;
    *(LAS float*)refp = m;
    *(LAS u32x2*)((lds_ptr)lds + PT_OFF + r * 8) = u32x2{pack2(s[0], s[1]), pack2(s[2], s[3])};
    flag_set(flags + F_REF, 1);
  }
  STAMP(2);

  int slot_off = w * SLOT;
  const lds_ptr pbuf = (lds_ptr)lds + P_OFF + lane * 16;
  bf16x8 preg[NT / 8][2];

  auto k_tile = [&](int c, bf16x8& p0, bf16x8& p1) {
    flag_wait(flags + F_LAND, c + 1);
    const lds_cptr kfrag = (lds_cptr)lds + slot_off + koff;
    f32x16 s = f32x16{};
    bf16x8 kf[8];
#pragma unroll
    for (int kk = 0; kk < 8; ++kk) kf[kk] = ld_frag(kfrag + kk * 32);
    SBAR();
#pragma unroll
    for (int kk = 0; kk < 8; ++kk) { s = MFMA32(kf[kk], qf[kk], s); kf[kk] = ld_frag(kfrag + (kk + 8) * 32); SBAR(); }
    flag_set(flags + F_DONE + w * 4, min(c + NW, NT));
    slot_off = slot_off == 0 ? (NSLOT - 1) * SLOT : slot_off - SLOT;
    SBAR();
#pragma unroll
    for (int kk = 0; kk < 8; ++kk) s = MFMA32(kf[kk], qf[kk + 8], s);
    float tmax = fmaxf(s[0], s[1]);
#pragma unroll
    for (int i = 2; i < 16; ++i) tmax = fmaxf(tmax, s[i]);
    tmax = xhalf_max(tmax) * C2;
    PIN(tmax);
    flag_wait(flags + F_REF, c + 1);
    const float mn = fmaxf(*(const LAS float*)(refp + c * 128), tmax);
    *(LAS float*)(refp + (c + 1) * 128) = mn;
    flag_set(flags + F_REF, c + 2);
    const float alpha = __builtin_amdgcn_exp2f(m - mn);
    m = mn;
    float ps = 0.f;
#pragma unroll
    for (int i = 0; i < 16; ++i) { s[i] = __builtin_amdgcn_exp2f(fmaf(s[i], C2, -mn)); ps += s[i]; }
    l = fmaf(l, alpha, ps);
    p0 = pack8(s, 0); p1 = pack8(s, 8);
    PIN(l); PIN(m);
  };
#pragma unroll 1
  for (int c = w; c < NT / 2; c += NW) {
    bf16x8 p0, p1;
    k_tile(c, p0, p1);
    *(LAS bf16x8*)(pbuf + c * 2048) = p0;
    *(LAS bf16x8*)(pbuf + c * 2048 + 1024) = p1;
  }
  unsigned in0 = __hip_atomic_load((GAS unsigned*)sig_in, RLX_AGENT);
  unsigned out1 = __hip_atomic_load((GAS unsigned*)sig_out, RLX_AGENT) + 1;
#pragma unroll
  for (int t = 0; t < NT / 8; ++t) { k_tile(NT / 2 + 4 * t + w, preg[t][0], preg[t][1]); PIN(preg[t][0]); PIN(preg[t][1]); }
  flag_set(flags + F_PRDY + w * 4, 1);

  f32x16 o[2];
  float rcur = *(const LAS float*)refp;
  {
    const lds_cptr tv = (lds_cptr)lds + TV_OFF + ((lane & 15) >> 2) * ROWB + trofs;
    u32x2 pt2 = *(const LAS u32x2*)((lds_cptr)lds + PT_OFF + r * 8);
    if (h) pt2 = u32x2{0u, 0u};
    const bf16x8 pt = __builtin_bit_cast(bf16x8, u32x4{pt2[0], pt2[1], 0u, 0u});
#pragma unroll
    for (int cb = 0; cb < 2; ++cb) { const s16x4 v4 = ld_tr(tv + cb * 256); o[cb] = MFMA32(join(v4, v4), pt, f32x16{}); }
  }
  flag_wait4(flags + F_PRDY, 1);
  PROBE(2);
  const float M = *(const LAS float*)(refp + NT * 128);
  *(LAS float*)((lds_ptr)lds + L_OFF + w * 128 + r * 4) = xhalf_sum(l) * __builtin_amdgcn_exp2f(m - M);
  slot_off = (NT % NSLOT) * SLOT;
  auto v_tiles = [&](int v0, int v1) {
#pragma unroll 1
    for (int v = v0; v < v1; ++v) {
      const float rnew = *(const LAS float*)(refp + (v + 1) * 128);
      bf16x8 p0 = ld_frag(pbuf + (v & (NT / 2 - 1)) * 2048), p1 = ld_frag(pbuf + (v & (NT / 2 - 1)) * 2048 + 1024);
      if (__any(rnew != rcur)) {
        const float alpha = __builtin_amdgcn_exp2f(rcur - rnew);
        o[0] *= alpha; o[1] *= alpha;
        rcur = rnew;
      }
      PIN(p0); PIN(p1);
      flag_wait(flags + F_LAND, NT + v + 1);
      const lds_cptr vfrag = (lds_cptr)lds + slot_off + vofs;
      bf16x8 vf[4];
#pragma unroll
      for (int i = 0; i < 4; ++i)
        vf[i] = join(ld_tr(vfrag + (i >> 1) * 512 + (i & 1) * 256), ld_tr(vfrag + 8 * VP + (i >> 1) * 512 + (i & 1) * 256));
      SBAR();
      o[0] = MFMA32(vf[0], p0, o[0]);
      o[1] = MFMA32(vf[1], p0, o[1]);
      SBAR();
      flag_set(flags + F_DONE + w * 4, NT + v + 1);
      slot_off = slot_off == (NSLOT - 1) * SLOT ? 0 : slot_off + SLOT;
      o[0] = MFMA32(vf[2], p1, o[0]);
      o[1] = MFMA32(vf[3], p1, o[1]);
    }
  };
  v_tiles(0, 20);
  flag_wait4(flags + F_DONE, NT + 16);
#pragma unroll
  for (int j = 0; j < 4; ++j) { *(LAS bf16x8*)(pbuf + (4 * j + w) * 2048) = preg[j][0]; *(LAS bf16x8*)(pbuf + (4 * j + w) * 2048 + 1024) = preg[j][1]; }
  flag_set(flags + F_PRDY + w * 4, 2);
  v_tiles(20, 32);
  flag_wait4(flags + F_PRDY, 2);
  float L = 0.f;
#pragma unroll
  for (int i = 0; i < NW; ++i) L += *(const LAS float*)((lds_cptr)lds + L_OFF + i * 128 + r * 4);
  v_tiles(32, 36);
  flag_wait4(flags + F_DONE, NT + 32);
#pragma unroll
  for (int j = 4; j < 8; ++j) { *(LAS bf16x8*)(pbuf + (4 * j + w) * 2048) = preg[j][0]; *(LAS bf16x8*)(pbuf + (4 * j + w) * 2048 + 1024) = preg[j][1]; }
  flag_set(flags + F_PRDY + w * 4, 3);
  v_tiles(36, 48);
  flag_wait4(flags + F_PRDY, 3);
  unsigned zout[4];
#pragma unroll
  for (int j = 0; j < 4; ++j) { zout[j] = (j == split ? 64 : (j * 4 + split) * 4 + w) * MSG + voff; PIN(zout[j]); }
  char* orow = (char*)out + ((size_t)pair * 32 + qrow) * ROWB + split * 128 + w * 32 + h * 16;
  PIN(orow);
  unsigned mlw = __float_as_uint(h ? L : M);
  PIN(mlw); PIN(in0); PIN(out1);
  v_tiles(48, NT);
  STAMP(3);
  PROBE(3);

#pragma unroll
  for (int j = 0; j < 4; ++j) {
    u32x4 x;
#pragma unroll
    for (int e = 0; e < 4; ++e) x[e] = pack2(o[j >> 1][8 * (j & 1) + 2 * e], o[j >> 1][8 * (j & 1) + 2 * e + 1]);
    __builtin_amdgcn_raw_buffer_store_b128(x, sZ, zout[j], 0, 16);
    __builtin_amdgcn_raw_buffer_store_b32(mlw, sZ, zout[j] + 1024 - 12 * lane, 0, 16);
  }
  unsigned g0 = __hip_atomic_load((GAS unsigned*)sig_in, RLX_AGENT), g1 = 0, g2 = 0;
  SBAR();
  float own[8];
#pragma unroll
  for (int e = 0; e < 8; ++e) {
    const float lo = (split & 1) ? o[0][8 + e] : o[0][e], hi = (split & 1) ? o[1][8 + e] : o[1][e];
    own[e] = (split & 2) ? hi : lo;
    PIN(own[e]);
  }
  wait_vm<0>();
  PIN(g0);
  STAMP(5);
  if (lane < 3) __hip_atomic_store(sig_out, ((u64)in0 << 32) | out1, RLX_AGENT);
  if (((unsigned)__ballot(g0 != in0) & 7u) != 7u) {
    unsigned left = 1u << 15, t;
#define TA_POLL(G) "s_waitcnt vmcnt(2)\n\tv_cmp_ne_u32 vcc, %[in0], " G "\n\ts_and_b32 %[t], vcc_lo, 7\n\ts_cmp_eq_u32 %[t], 7\n\t" \
                   "s_cbranch_scc1 2f\n\tglobal_load_dword " G ", %[p], off sc1\n\t"
    asm volatile(
        "global_load_dword %[g0], %[p], off sc1\n\ts_sleep 3\n\t"
        "global_load_dword %[g1], %[p], off sc1\n\ts_sleep 3\n\t"
        "global_load_dword %[g2], %[p], off sc1\n"
        "1:\n\t" TA_POLL("%[g0]") TA_POLL("%[g1]") TA_POLL("%[g2]")
        "s_sub_u32 %[left], %[left], 1\n\ts_cbranch_scc0 1b\n"
        "2:"
        : [g0] "=&v"(g0), [g1] "=&v"(g1), [g2] "=&v"(g2), [left] "+s"(left), [t] "=&s"(t)
        : [p] "v"(sig_in), [in0] "v"(in0)
        : "memory", "vcc", "scc");
#undef TA_POLL
  }
  __builtin_amdgcn_fence(__ATOMIC_ACQUIRE, "wavefront");
  asm volatile("" ::: "memory");
  u32x4 a[3];
  unsigned ml[3];
#pragma unroll
  for (int k = 0; k < 3; ++k) {
    ml[k] = __builtin_amdgcn_raw_buffer_load_b32(sZ, 1024 + lane * 4, zin[k], 16);
    a[k] = __builtin_amdgcn_raw_buffer_load_b128(sZ, voff, zin[k], 16);
  }
  const unsigned peer_view = __hip_atomic_load((GAS unsigned*)sig_in + 1, RLX_AGENT);
  STAMP(4);
  {
    float mk[3], lk[3], wk[3], G = M;
#pragma unroll
    for (int k = 0; k < 3; ++k) {
      auto x = __builtin_amdgcn_permlane32_swap(ml[k], ml[k], false, false);
      mk[k] = __uint_as_float(x[0]); lk[k] = __uint_as_float(x[1]);
      G = fmaxf(G, mk[k]);
    }
    const float w0 = __builtin_amdgcn_exp2f(M - G);
    float den = w0 * L;
#pragma unroll
    for (int k = 0; k < 3; ++k) { wk[k] = __builtin_amdgcn_exp2f(mk[k] - G); den = fmaf(wk[k], lk[k], den); }
    const float inv = 1.0f / den;
#pragma unroll
    for (int e = 0; e < 8; ++e) own[e] *= w0;
#pragma unroll
    for (int k = 0; k < 3; ++k)
#pragma unroll
      for (int e = 0; e < 4; ++e) {
        own[2 * e] = fmaf(wk[k], __uint_as_float(a[k][e] << 16), own[2 * e]);
        own[2 * e + 1] = fmaf(wk[k], __uint_as_float(a[k][e] & 0xffff0000u), own[2 * e + 1]);
      }
#pragma unroll
    for (int e = 0; e < 8; ++e) own[e] *= inv;
  }
  auto x = __builtin_amdgcn_permlane32_swap(pack2(own[0], own[1]), pack2(own[4], own[5]), false, false);
  auto y = __builtin_amdgcn_permlane32_swap(pack2(own[2], own[3]), pack2(own[6], own[7]), false, false);
  *(GAS u32x4*)orow = u32x4{x[0], y[0], x[1], y[1]};
  asm volatile("" ::"v"(g0), "v"(g1), "v"(g2), "v"(peer_view));
  if (lane < 3 && peer_view == out1) __hip_atomic_store(sig_out, ((u64)in0 << 32) | (out1 + 1), RLX_AGENT);
}

}

extern "C" void kernel_launch(void* const* d_in, const int* in_sizes, int n_in, void* d_out, int out_size, void* d_ws,
                              size_t ws_size, hipStream_t stream) {
  using namespace ta;
  (void)in_sizes; (void)n_in; (void)out_size; (void)ws_size;
  (void)hipFuncSetAttribute((const void*)tree_attn, hipFuncAttributeMaxDynamicSharedMemorySize, LDS_BYTES);
  hipLaunchKernelGGL(tree_attn, dim3(NPAIR * NSPLIT), dim3((NW + 1) * 64), LDS_BYTES, stream,
                     (const __hip_bfloat16*)d_in[0], (const __hip_bfloat16*)d_in[1], (const __hip_bfloat16*)d_in[2],
                     (const __hip_bfloat16*)d_in[3], (const __hip_bfloat16*)d_in[4], (const __hip_bfloat16*)d_in[5],
                     (char*)d_ws, (__hip_bfloat16*)d_out);
}
```

```cpp
#include <hip/hip_runtime.h>
#include <hip/hip_bf16.h>

#define TA_WIN 20
#define TA_WSTEP 2
#define TA_TAU 545
#define TA_OFF 1000
#define TA_LSTEP 700
#define TA_BOOST0 0
#define TA_BOOST1 0
#define TA_CODEPF 128
#define TA_KNEE 7
#define TA_KDIV 4
#define TA_POLL2 1
#define TA_SIGLINE 1
#define TA_KMAX 7

#define STAMP(k) do {} while (0)

namespace ta {

constexpr int NB = 8, HKV = 8, CTX = 8192, HD = 256;
constexpr int NPAIR = NB * HKV, NSPLIT = 4, NW = 4;
constexpr int ROWB = HD * 2;
constexpr int WG_BYTES = CTX / NSPLIT * ROWB;
constexpr int NT = CTX / NSPLIT / 32;
constexpr int KP = 1040, VP = 1056;
constexpr int SLOT = 16 * VP, NSLOT = 5;
constexpr int P_OFF = NSLOT * SLOT;
constexpr int REF_OFF = P_OFF + NT / 2 * 2048;
constexpr int TV_OFF = REF_OFF + (NT + 1) * 128;
constexpr int PT_OFF = TV_OFF + 4 * ROWB;
constexpr int L_OFF = PT_OFF + 256;
constexpr int FLAG_OFF = L_OFF + NW * 128;
constexpr int F_DONE = 0, F_PRDY = 16, F_LAND = 32, F_REF = 36, F_Q = 40;
constexpr int LDS_BYTES = FLAG_OFF + 48;
constexpr int TK_OFF = REF_OFF - 4 * ROWB, Q_OFF = TK_OFF - 16 * KP, MK_OFF = Q_OFF - 2048;
constexpr float SCALE = 0.0625f, LOG2E = 1.4426950408889634f, C2 = SCALE * LOG2E;
constexpr int WIN = TA_WIN;
constexpr int MSG = 1024 + 256;
constexpr int CTRL_BYTES = NPAIR * 64 * 128;
static_assert(LDS_BYTES <= 160 * 1024 && MK_OFF >= P_OFF + 16 * 2048 && FLAG_OFF % 16 == 0, "LDS map");

static_assert(WIN >= 17 && WIN + 7 * TA_WSTEP <= 48, "publish / drain ladder assumes 1..3 chunks in flight");

typedef __bf16 bf16x8 __attribute__((ext_vector_type(8)));
typedef __bf16 bf16x2 __attribute__((ext_vector_type(2)));
typedef short s16x4 __attribute__((ext_vector_type(4)));
typedef float f32x16 __attribute__((ext_vector_type(16)));
typedef float f32x4 __attribute__((ext_vector_type(4)));
typedef float f32x2 __attribute__((ext_vector_type(2)));
typedef unsigned u32x4 __attribute__((ext_vector_type(4)));
typedef unsigned u32x2 __attribute__((ext_vector_type(2)));
typedef unsigned long long u64;
#define LAS __attribute__((address_space(3)))
#define GAS __attribute__((address_space(1)))
typedef LAS const char* lds_cptr;
typedef LAS char* lds_ptr;
typedef __amdgpu_buffer_rsrc_t srd_t;

#define SBAR() __builtin_amdgcn_sched_barrier(0)
#define PIN(x) asm volatile("" : "+v"(x))
#define MFMA32(a, b, c) __builtin_amdgcn_mfma_f32_32x32x16_bf16((a), (b), (c), 0, 0, 0)
#define RLX_AGENT __ATOMIC_RELAXED, __HIP_MEMORY_SCOPE_AGENT
#define RFL(x) __builtin_amdgcn_readfirstlane(x)

__device__ __forceinline__ srd_t make_srd(const void* p, int bytes) {
  return __builtin_amdgcn_make_buffer_rsrc((void*)p, (short)0, bytes, 0x00020000);
}

template <int PAD, bool NTL>
__device__ __forceinline__ void dma4(srd_t srd, unsigned voff, unsigned soff, unsigned ldsb) {
  unsigned keep;
  if constexpr (NTL) {
  asm volatile(
      "s_mov_b32 %0, m0\n\ts_mov_b32 m0, %4\n\ts_nop 3\n\t"
      "buffer_load_dwordx4 %1, %2, %3 offen nt lds\n\t"
      "s_add_u32 m0, %4, %5\n\ts_nop 0\n\t"
      "buffer_load_dwordx4 %1, %2, %3 offen offset:1024 nt lds\n\t"
      "s_add_u32 m0, %4, %6\n\ts_nop 0\n\t"
      "buffer_load_dwordx4 %1, %2, %3 offen offset:2048 nt lds\n\t"
      "s_add_u32 m0, %4, %7\n\ts_nop 0\n\t"
      "buffer_load_dwordx4 %1, %2, %3 offen offset:3072 nt lds\n\t"
      "s_mov_b32 m0, %0"
      : "=&s"(keep)
      : "v"(voff), "s"(srd), "s"(soff), "s"(ldsb), "n"(PAD), "n"(2 * PAD), "n"(3 * PAD)
      : "memory", "scc");
  } else {
  asm volatile(
      "s_mov_b32 %0, m0\n\ts_mov_b32 m0, %4\n\ts_nop 3\n\t"
      "buffer_load_dwordx4 %1, %2, %3 offen lds\n\t"
      "s_add_u32 m0, %4, %5\n\ts_nop 0\n\t"
      "buffer_load_dwordx4 %1, %2, %3 offen offset:1024 lds\n\t"
      "s_add_u32 m0, %4, %6\n\ts_nop 0\n\t"
      "buffer_load_dwordx4 %1, %2, %3 offen offset:2048 lds\n\t"
      "s_add_u32 m0, %4, %7\n\ts_nop 0\n\t"
      "buffer_load_dwordx4 %1, %2, %3 offen offset:3072 lds\n\t"
      "s_mov_b32 m0, %0"
      : "=&s"(keep)
      : "v"(voff), "s"(srd), "s"(soff), "s"(ldsb), "n"(PAD), "n"(2 * PAD), "n"(3 * PAD)
      : "memory", "scc");
  }
}
#define TA_PIECE(I, OFS, STEP) \
      "s_waitcnt vmcnt(%[n])\n\t" \
      "buffer_load_dwordx4 %[vo], %[srd], %[so] offen " OFS " nt lds\n\t" \
      "s_add_u32 m0, m0, " STEP "\n\t" \
      ".if (%[n] & 15) == " #I "\n\tds_write_b32 %[la], %[lv]\n\t.endif\n\t"
#define TA_GROUP(I0, I1, I2, I3) \
      TA_PIECE(I0, "", "%[pad]") TA_PIECE(I1, "offset:1024", "%[pad]") TA_PIECE(I2, "offset:2048", "%[pad]") \
      TA_PIECE(I3, "offset:3072", "%[padg]") "s_add_u32 %[so], %[so], 0x1000\n\t"
template <int N>
__device__ __forceinline__ void dma_chunk(srd_t srd, unsigned voff, unsigned soff, unsigned ldsb, unsigned pad, unsigned land_addr,
                                          int c, unsigned done_addr, u32x4& done, u64& now) {
  unsigned keep;
  asm volatile(
      "s_mov_b32 %[keep], m0\n\ts_mov_b32 m0, %[lds]\n\ts_nop 2\n\t"
      TA_GROUP(0, 1, 2, 3) TA_GROUP(4, 5, 6, 7) TA_GROUP(8, 9, 10, 11)
      "ds_read_b128 %[done], %[da]\n\ts_memrealtime %[now]\n\t"
      TA_GROUP(12, 13, 14, 15)
      "s_mov_b32 m0, %[keep]\n\ts_waitcnt lgkmcnt(0)"
      : [keep] "=&s"(keep), [so] "+s"(soff), [done] "=&v"(done), [now] "=&s"(now)
      : [vo] "v"(voff), [srd] "s"(srd), [lds] "s"(ldsb), [pad] "s"(pad), [padg] "s"(pad + 4096), [n] "n"(N),
        [la] "v"(land_addr), [lv] "v"(c - N / 16), [da] "v"(done_addr)
      : "memory", "scc");
}
__device__ __forceinline__ void dma2(srd_t srd, unsigned voff, unsigned soff, unsigned ldsb) {
  unsigned keep;
  asm volatile("s_mov_b32 %0, m0\n\ts_mov_b32 m0, %4\n\ts_nop 3\n\t"
               "buffer_load_dwordx4 %1, %2, %3 offen lds\n\t"
               "buffer_load_dwordx4 %1, %2, %3 offen offset:1024 lds\n\t"
               "s_mov_b32 m0, %0"
               : "=&s"(keep) : "v"(voff), "s"(srd), "s"(soff), "s"(ldsb) : "memory");
}
template <int N> __device__ __forceinline__ void wait_vm() { asm volatile("s_waitcnt vmcnt(%0)" ::"n"(N) : "memory"); }

__device__ __forceinline__ bf16x8 ld_frag(lds_cptr p) { return *(const LAS bf16x8*)p; }
__device__ __forceinline__ s16x4 ld_tr(lds_cptr p) { return __builtin_amdgcn_ds_read_tr16_b64_v4i16((LAS s16x4*)p); }
__device__ __forceinline__ bf16x8 join(s16x4 lo, s16x4 hi) {
  return __builtin_bit_cast(bf16x8, __builtin_shufflevector(lo, hi, 0, 1, 2, 3, 4, 5, 6, 7));
}
__device__ __forceinline__ float xhalf_max(float x) {
  auto r = __builtin_amdgcn_permlane32_swap(__float_as_uint(x), __float_as_uint(x), false, false);
  return fmaxf(__uint_as_float(r[0]), __uint_as_float(r[1]));
}
__device__ __forceinline__ float xhalf_sum(float x) {
  auto r = __builtin_amdgcn_permlane32_swap(__float_as_uint(x), __float_as_uint(x), false, false);
  return __uint_as_float(r[0]) + __uint_as_float(r[1]);
}
__device__ __forceinline__ unsigned pack2(float a, float b) {
  const bf16x2 v = {(__bf16)a, (__bf16)b};
  return __builtin_bit_cast(unsigned, v);
}
__device__ __forceinline__ bf16x8 pack8(const f32x16& p, int o) {
  bf16x8 r;
#pragma unroll
  for (int j = 0; j < 8; ++j) r[j] = (__bf16)p[o + j];
  return r;
}
__device__ __forceinline__ void flag_set(unsigned addr, unsigned v) {
  asm volatile("s_waitcnt lgkmcnt(0)\n\tds_write_b32 %0, %1" ::"v"(addr), "v"(v) : "memory");
}
__device__ __forceinline__ void flag_wait(unsigned addr, int need) {
#pragma unroll 1
  for (int spin = 0; spin < (1 << 16); ++spin) {
    unsigned v;
    asm volatile("ds_read_b32 %0, %1\n\ts_waitcnt lgkmcnt(0)" : "=v"(v) : "v"(addr) : "memory");
    if ((int)RFL(v) >= need) break;
    __builtin_amdgcn_s_sleep(1);
  }
}
__device__ __forceinline__ void flag_wait4(unsigned addr, int need) {
#pragma unroll 1
  for (int spin = 0; spin < (1 << 16); ++spin) {
    u32x4 v;
    asm volatile("ds_read_b128 %0, %1\n\ts_waitcnt lgkmcnt(0)" : "=v"(v) : "v"(addr) : "memory");
    if ((int)RFL(min(min(v[0], v[1]), min(v[2], v[3]))) >= need) break;
    __builtin_amdgcn_s_sleep(1);
  }
}

__global__ __launch_bounds__((NW + 1) * 64, 2) void tree_attn(
    const __hip_bfloat16* __restrict__ q, const __hip_bfloat16* __restrict__ kc, const __hip_bfloat16* __restrict__ vc,
    const __hip_bfloat16* __restrict__ kt, const __hip_bfloat16* __restrict__ vt, const __hip_bfloat16* __restrict__ mask,
    char* ws, __hip_bfloat16* __restrict__ out) {
  extern __shared__ __attribute__((aligned(16))) char lds[];
  u64 kbase, base2, now, pc;
  unsigned d0, d1;
  const int wid = RFL(threadIdx.x >> 6);
  asm volatile("s_load_dwordx2 %[kb], %[ka], 0x8\n\t"
               "s_load_dwordx2 %[b2], %[ka], %[sel]\n\t"
               "s_getpc_b64 %[pc]\n\t"
               "s_load_dword %[d0], %[pc], %[line]\n\t"
               "s_load_dword %[d1], %[pc], %[line] offset:64\n\t"
               "s_memrealtime %[now]"
               : [kb] "=&s"(kbase), [b2] "=&s"(base2), [pc] "=&s"(pc), [d0] "=&s"(d0), [d1] "=&s"(d1), [now] "=&s"(now)
               : [ka] "s"(__builtin_amdgcn_kernarg_segment_ptr()), [sel] "s"((wid >> 2) * 8),
                 [line] "s"(((blockIdx.x >> 3) * (NW + 1) + wid) * TA_CODEPF)
               : "memory");
  SBAR();
  const int lane = threadIdx.x & 63;
  const unsigned lds0 = (unsigned)(uintptr_t)lds;
  const unsigned voff = lane * 16;
  const int pair = blockIdx.x >> 2, split = blockIdx.x & 3;
  {
    unsigned keep;
#define TA_G4(VO, BASE, POL) \
        "global_load_lds_dwordx4 " VO ", " BASE POL "\n\ts_add_u32 m0, m0, %[pad]\n\ts_nop 0\n\t" \
        "global_load_lds_dwordx4 " VO ", " BASE " offset:1024" POL "\n\ts_add_u32 m0, m0, %[pad]\n\ts_nop 0\n\t" \
        "global_load_lds_dwordx4 " VO ", " BASE " offset:2048" POL "\n\ts_add_u32 m0, m0, %[pad]\n\ts_nop 0\n\t" \
        "global_load_lds_dwordx4 " VO ", " BASE " offset:3072" POL "\n\t"
    asm volatile(
        "s_mov_b32 %[keep], m0\n\ts_mov_b32 m0, %[dst2]\n\t"
        "s_waitcnt lgkmcnt(0)\n\t"
        TA_G4("%[vo2]", "%[b2]", "")
        "s_mov_b32 m0, %[dst]\n\ts_nop 0\n\t"
        TA_G4("%[vo]", "%[kb]", " nt")
        "s_mov_b32 m0, %[keep]"
        : [keep] "=&s"(keep), [kb] "+s"(kbase), [b2] "+s"(base2), [now] "+s"(now), [d0] "+s"(d0), [d1] "+s"(d1)
        : [vo] "v"(blockIdx.x * WG_BYTES + wid * 4096 + voff),
          [dst] "s"(lds0 + wid * 4 * KP + (wid >> 2) * (SLOT - 16 * KP)),
          [vo2] "v"((wid == NW ? blockIdx.x * WG_BYTES + 5 * 4096 : pair * 32 * ROWB + wid * 4096) + voff),
          [dst2] "s"(lds0 + (wid == NW ? SLOT + 4 * KP : Q_OFF + wid * 4 * KP)), [pad] "n"(KP - 1024)
        : "memory", "scc");
#undef TA_G4
  }
#define PROBE(k) do {} while (0)
  const unsigned flags = lds0 + FLAG_OFF;
  if (threadIdx.x < 12) flag_set(flags + lane * 4, lane < 4 ? lane : lane == F_LAND / 4 ? 1 : 0);
  asm volatile("s_waitcnt lgkmcnt(0)\n\ts_barrier" ::: "memory");

  if (wid == NW) {
    __builtin_amdgcn_s_setprio(3);
    const srd_t sK = make_srd((const char*)kbase + (size_t)blockIdx.x * WG_BYTES, WG_BYTES);
#pragma unroll 1
    for (int g = 2; g < 4; ++g) dma4<KP - 1024, true>(sK, voff, 16384 + g * 4096, lds0 + SLOT + g * 4 * KP);
    const srd_t sV = make_srd((const char*)vc + (size_t)blockIdx.x * WG_BYTES, WG_BYTES);
    const unsigned t0 = (unsigned)now + TA_OFF / 10;
    u32x4 done = {0u, 0u, 0u, 0u};
    unsigned dst = lds0 + 2 * SLOT;
#pragma unroll 1
    for (int c = 2; c < 2 * NT; ++c) {
      if (c >= NSLOT && (int)RFL(min(min(done[0], done[1]), min(done[2], done[3]))) < c - NSLOT + 1)
        flag_wait4(flags + F_DONE, c - NSLOT + 1);
      const int lag = (int)(RFL((unsigned)now) - t0) - (((4 * c - 1) * (TA_TAU * 64 / 10)) >> 8);
      int k = lag <= 0 ? 0 : (lag * (655360 / TA_LSTEP)) >> 16;
      if (k > TA_KNEE) k = min(TA_KNEE + (k - TA_KNEE) / TA_KDIV, TA_KMAX);
      if (c < TA_BOOST0 || c >= 2 * NT - TA_BOOST1) k = 7;
      const srd_t srd = c < NT ? sK : sV;
      const unsigned soff = (c & (NT - 1)) * 16384, pad = c < NT ? KP - 1024 : VP - 1024;
#define TA_CH(K) dma_chunk<WIN - 1 + (K) * TA_WSTEP>(srd, voff, soff, dst, pad, flags + F_LAND, c, flags + F_DONE, done, now)
      if (k < 4) {
        if (k < 2) { if (k == 0) TA_CH(0); else TA_CH(1); } else { if (k == 2) TA_CH(2); else TA_CH(3); }
      } else {
        if (k < 6) { if (k == 4) TA_CH(4); else TA_CH(5); } else { if (k == 6) TA_CH(6); else TA_CH(7); }
      }
#undef TA_CH
      dst = dst == lds0 + (NSLOT - 1) * SLOT ? lds0 : dst + SLOT;
    }
    wait_vm<32>(); flag_set(flags + F_LAND, 2 * NT - 2);
    wait_vm<16>(); flag_set(flags + F_LAND, 2 * NT - 1);
    wait_vm<0>(); flag_set(flags + F_LAND, 2 * NT);
    return;
  }

  const int w = wid;
  const int r = lane & 31, h = lane >> 5;
  if (w == 3) {
    dma2(make_srd((const char*)kt + (size_t)pair * 16 * ROWB, 16 * ROWB), voff, split * 4 * ROWB, lds0 + TK_OFF);
    dma2(make_srd((const char*)mask + (pair >> 3) * 512, 512), voff, 0, lds0 + MK_OFF);
  }
  if (w == 2) dma2(make_srd((const char*)vt + (size_t)pair * 16 * ROWB, 16 * ROWB), voff, split * 4 * ROWB, lds0 + TV_OFF);
  const int qrow = 2 * (r & 15) + (r >> 4);
  const int koff = (r & 15) * KP + (r >> 4) * 512 + h * 16;
  const int trofs = ((lane >> 4) & 1) * 128 + w * 32 + (lane & 3) * 8;
  const int vofs = (4 * h + ((lane & 15) >> 2)) * VP + trofs;
  const srd_t sZ = make_srd(ws + CTRL_BYTES + (size_t)pair * 64 * MSG, 64 * MSG);
  const int peer = (split + 1 + (lane & 3)) & 3;
  GAS u64* sig_in = (GAS u64*)(ws + ((pair * 4 + split) * 4 + w) * 128) + peer;
  GAS u64* sig_out = (GAS u64*)(ws + ((pair * 4 + peer) * 4 + w) * 128) + split;
  unsigned zin[3];
#pragma unroll
  for (int k = 0; k < 3; ++k) zin[k] = ((split * 4 + ((split + 1 + k) & 3)) * 4 + w) * MSG;
  if (w == 3) wait_vm<4>(); else if (w == 2) wait_vm<2>(); else wait_vm<0>();
  if (lane == 0) asm volatile("ds_add_u32 %0, %1" ::"v"(flags + F_Q), "v"(1u) : "memory");
  flag_wait(flags + F_Q, NW);
  STAMP(1);
  bf16x8 qf[16];
#pragma unroll
  for (int kk = 0; kk < 16; ++kk) qf[kk] = ld_frag((lds_cptr)lds + Q_OFF + koff + kk * 32);

  const lds_ptr refp = (lds_ptr)lds + REF_OFF + r * 4;
  float m = -INFINITY, l = 0.f;
  if (w == 3) {
    wait_vm<0>();
    const u32x2 mbits = *(const LAS u32x2*)((lds_cptr)lds + MK_OFF + (qrow & 15) * 32 + split * 8);
    const lds_cptr tk = (lds_cptr)lds + TK_OFF + (r & 3) * ROWB + h * 16;
    f32x16 s = f32x16{};
#pragma unroll
    for (int kk = 0; kk < 16; ++kk) s = MFMA32(ld_frag(tk + kk * 32), qf[kk], s);
#pragma unroll
    for (int i = 0; i < 4; ++i) {
      const float mk = __uint_as_float((i & 1) ? (mbits[i >> 1] & 0xffff0000u) : (mbits[i >> 1] << 16));
      s[i] = fmaf(s[i], SCALE, (1.0f - mk) * -1000000000.0f) * LOG2E;
      m = fmaxf(m, s[i]);
    }
#pragma unroll
    for (int i = 0; i < 4; ++i) { s[i] = __builtin_amdgcn_exp2f(s[i] - m); l += s[i]; }
    if (h) l = 0.f;
    *(LAS float*)refp = m;
    *(LAS u32x2*)((lds_ptr)lds + PT_OFF + r * 8) = u32x2{pack2(s[0], s[1]), pack2(s[2], s[3])};
    flag_set(flags + F_REF, 1);
  }
  STAMP(2);

  int slot_off = w * SLOT;
  const lds_ptr pbuf = (lds_ptr)lds + P_OFF + lane * 16;
  bf16x8 preg[NT / 8][2];

  auto k_tile = [&](int c, bf16x8& p0, bf16x8& p1) {
    flag_wait(flags + F_LAND, c + 1);
    const lds_cptr kfrag = (lds_cptr)lds + slot_off + koff;
    f32x16 s = f32x16{};
    bf16x8 kf[8];
#pragma unroll
    for (int kk = 0; kk < 8; ++kk) kf[kk] = ld_frag(kfrag + kk * 32);
    SBAR();
#pragma unroll
    for (int kk = 0; kk < 8; ++kk) { s = MFMA32(kf[kk], qf[kk], s); kf[kk] = ld_frag(kfrag + (kk + 8) * 32); SBAR(); }
    flag_set(flags + F_DONE + w * 4, min(c + NW, NT));
    slot_off = slot_off == 0 ? (NSLOT - 1) * SLOT : slot_off - SLOT;
    SBAR();
#pragma unroll
    for (int kk = 0; kk < 8; ++kk) s = MFMA32(kf[kk], qf[kk + 8], s);
    float tmax = fmaxf(s[0], s[1]);
#pragma unroll
    for (int i = 2; i < 16; ++i) tmax = fmaxf(tmax, s[i]);
    tmax = xhalf_max(tmax) * C2;
    PIN(tmax);
    flag_wait(flags + F_REF, c + 1);
    const float mn = fmaxf(*(const LAS float*)(refp + c * 128), tmax);
    *(LAS float*)(refp + (c + 1) * 128) = mn;
    flag_set(flags + F_REF, c + 2);
    const float alpha = __builtin_amdgcn_exp2f(m - mn);
    m = mn;
    float ps = 0.f;
#pragma unroll
    for (int i = 0; i < 16; ++i) { s[i] = __builtin_amdgcn_exp2f(fmaf(s[i], C2, -mn)); ps += s[i]; }
    l = fmaf(l, alpha, ps);
    p0 = pack8(s, 0); p1 = pack8(s, 8);
    PIN(l); PIN(m);
  };
#pragma unroll 1
  for (int c = w; c < NT / 2; c += NW) {
    bf16x8 p0, p1;
    k_tile(c, p0, p1);
    *(LAS bf16x8*)(pbuf + c * 2048) = p0;
    *(LAS bf16x8*)(pbuf + c * 2048 + 1024) = p1;
  }
  unsigned in0 = __hip_atomic_load((GAS unsigned*)sig_in, RLX_AGENT);
  unsigned out1 = __hip_atomic_load((GAS unsigned*)sig_out, RLX_AGENT) + 1;
#pragma unroll
  for (int t = 0; t < NT / 8; ++t) { k_tile(NT / 2 + 4 * t + w, preg[t][0], preg[t][1]); PIN(preg[t][0]); PIN(preg[t][1]); }
  wait_vm<0>();
  flag_set(flags + F_PRDY + w * 4, 1);
  flag_wait4(flags + F_PRDY, 1);
  PROBE(2);

  f32x16 o[2];
  float rcur = *(const LAS float*)refp;
  {
    const lds_cptr tv = (lds_cptr)lds + TV_OFF + ((lane & 15) >> 2) * ROWB + trofs;
    u32x2 pt2 = *(const LAS u32x2*)((lds_cptr)lds + PT_OFF + r * 8);
    if (h) pt2 = u32x2{0u, 0u};
    const bf16x8 pt = __builtin_bit_cast(bf16x8, u32x4{pt2[0], pt2[1], 0u, 0u});
#pragma unroll
    for (int cb = 0; cb < 2; ++cb) { const s16x4 v4 = ld_tr(tv + cb * 256); o[cb] = MFMA32(join(v4, v4), pt, f32x16{}); }
  }
  const float M = *(const LAS float*)(refp + NT * 128);
  *(LAS float*)((lds_ptr)lds + L_OFF + w * 128 + r * 4) = xhalf_sum(l) * __builtin_amdgcn_exp2f(m - M);
  slot_off = (NT % NSLOT) * SLOT;
  auto v_tiles = [&](int v0, int v1) {
#pragma unroll 1
    for (int v = v0; v < v1; ++v) {
      const float rnew = *(const LAS float*)(refp + (v + 1) * 128);
      bf16x8 p0 = ld_frag(pbuf + (v & (NT / 2 - 1)) * 2048), p1 = ld_frag(pbuf + (v & (NT / 2 - 1)) * 2048 + 1024);
      if (__any(rnew != rcur)) {
        const float alpha = __builtin_amdgcn_exp2f(rcur - rnew);
        o[0] *= alpha; o[1] *= alpha;
        rcur = rnew;
      }
      PIN(p0); PIN(p1);
      flag_wait(flags + F_LAND, NT + v + 1);
      const lds_cptr vfrag = (lds_cptr)lds + slot_off + vofs;
      bf16x8 vf[4];
#pragma unroll
      for (int i = 0; i < 4; ++i)
        vf[i] = join(ld_tr(vfrag + (i >> 1) * 512 + (i & 1) * 256), ld_tr(vfrag + 8 * VP + (i >> 1) * 512 + (i & 1) * 256));
      SBAR();
      o[0] = MFMA32(vf[0], p0, o[0]);
      o[1] = MFMA32(vf[1], p0, o[1]);
      SBAR();
      flag_set(flags + F_DONE + w * 4, NT + v + 1);
      slot_off = slot_off == (NSLOT - 1) * SLOT ? 0 : slot_off + SLOT;
      o[0] = MFMA32(vf[2], p1, o[0]);
      o[1] = MFMA32(vf[3], p1, o[1]);
    }
  };
  v_tiles(0, 20);
  flag_wait4(flags + F_DONE, NT + 16);
#pragma unroll
  for (int j = 0; j < 4; ++j) { *(LAS bf16x8*)(pbuf + (4 * j + w) * 2048) = preg[j][0]; *(LAS bf16x8*)(pbuf + (4 * j + w) * 2048 + 1024) = preg[j][1]; }
  flag_set(flags + F_PRDY + w * 4, 2);
  v_tiles(20, 32);
  flag_wait4(flags + F_PRDY, 2);
  float L = 0.f;
#pragma unroll
  for (int i = 0; i < NW; ++i) L += *(const LAS float*)((lds_cptr)lds + L_OFF + i * 128 + r * 4);
  v_tiles(32, 36);
  flag_wait4(flags + F_DONE, NT + 32);
#pragma unroll
  for (int j = 4; j < 8; ++j) { *(LAS bf16x8*)(pbuf + (4 * j + w) * 2048) = preg[j][0]; *(LAS bf16x8*)(pbuf + (4 * j + w) * 2048 + 1024) = preg[j][1]; }
  flag_set(flags + F_PRDY + w * 4, 3);
  v_tiles(36, 48);
  flag_wait4(flags + F_PRDY, 3);
  unsigned zout[4];
#pragma unroll
  for (int j = 0; j < 4; ++j) { zout[j] = (j == split ? 64 : (j * 4 + split) * 4 + w) * MSG + voff; PIN(zout[j]); }
  char* orow = (char*)out + ((size_t)pair * 32 + qrow) * ROWB + split * 128 + w * 32 + h * 16;
  PIN(orow);
  unsigned mlw = __float_as_uint(h ? L : M);
  PIN(mlw); PIN(in0); PIN(out1);
  v_tiles(48, NT);
  STAMP(3);
  PROBE(3);

#pragma unroll
  for (int j = 0; j < 4; ++j) {
    u32x4 x;
#pragma unroll
    for (int e = 0; e < 4; ++e) x[e] = pack2(o[j >> 1][8 * (j & 1) + 2 * e], o[j >> 1][8 * (j & 1) + 2 * e + 1]);
    __builtin_amdgcn_raw_buffer_store_b128(x, sZ, zout[j], 0, 16);
    __builtin_amdgcn_raw_buffer_store_b32(mlw, sZ, zout[j] + 1024 - 12 * lane, 0, 16);
  }
  unsigned g0 = __hip_atomic_load((GAS unsigned*)sig_in, RLX_AGENT), g1 = 0, g2 = 0;
  SBAR();
  float own[8];
#pragma unroll
  for (int e = 0; e < 8; ++e) {
    const float lo = (split & 1) ? o[0][8 + e] : o[0][e], hi = (split & 1) ? o[1][8 + e] : o[1][e];
    own[e] = (split & 2) ? hi : lo;
    PIN(own[e]);
  }
  wait_vm<0>();
  PIN(g0);
  STAMP(5);
  if (lane < 3) __hip_atomic_store(sig_out, ((u64)in0 << 32) | out1, RLX_AGENT);
  if (((unsigned)__ballot(g0 != in0) & 7u) != 7u) {
    unsigned left = 1u << 15, t;
#define TA_POLL(G) "s_waitcnt vmcnt(2)\n\tv_cmp_ne_u32 vcc, %[in0], " G "\n\ts_and_b32 %[t], vcc_lo, 7\n\ts_cmp_eq_u32 %[t], 7\n\t" \
                   "s_cbranch_scc1 2f\n\tglobal_load_dword " G ", %[p], off sc1\n\t"
    asm volatile(
        "global_load_dword %[g0], %[p], off sc1\n\ts_sleep 3\n\t"
        "global_load_dword %[g1], %[p], off sc1\n\ts_sleep 3\n\t"
        "global_load_dword %[g2], %[p], off sc1\n"
        "1:\n\t" TA_POLL("%[g0]") TA_POLL("%[g1]") TA_POLL("%[g2]")
        "s_sub_u32 %[left], %[left], 1\n\ts_cbranch_scc0 1b\n"
        "2:"
        : [g0] "=&v"(g0), [g1] "=&v"(g1), [g2] "=&v"(g2), [left] "+s"(left), [t] "=&s"(t)
        : [p] "v"(sig_in), [in0] "v"(in0)
        : "memory", "vcc", "scc");
#undef TA_POLL
  }
  __builtin_amdgcn_fence(__ATOMIC_ACQUIRE, "wavefront");
  asm volatile("" ::: "memory");
  u32x4 a[3];
  unsigned ml[3];
#pragma unroll
  for (int k = 0; k < 3; ++k) {
    ml[k] = __builtin_amdgcn_raw_buffer_load_b32(sZ, 1024 + lane * 4, zin[k], 16);
    a[k] = __builtin_amdgcn_raw_buffer_load_b128(sZ, voff, zin[k], 16);
  }
  const unsigned peer_view = __hip_atomic_load((GAS unsigned*)sig_in + 1, RLX_AGENT);
  STAMP(4);
  {
    float mk[3], lk[3], wk[3], G = M;
#pragma unroll
    for (int k = 0; k < 3; ++k) {
      auto x = __builtin_amdgcn_permlane32_swap(ml[k], ml[k], false, false);
      mk[k] = __uint_as_float(x[0]); lk[k] = __uint_as_float(x[1]);
      G = fmaxf(G, mk[k]);
    }
    const float w0 = __builtin_amdgcn_exp2f(M - G);
    float den = w0 * L;
#pragma unroll
    for (int k = 0; k < 3; ++k) { wk[k] = __builtin_amdgcn_exp2f(mk[k] - G); den = fmaf(wk[k], lk[k], den); }
    const float inv = 1.0f / den;
#pragma unroll
    for (int e = 0; e < 8; ++e) own[e] *= w0;
#pragma unroll
    for (int k = 0; k < 3; ++k)
#pragma unroll
      for (int e = 0; e < 4; ++e) {
        own[2 * e] = fmaf(wk[k], __uint_as_float(a[k][e] << 16), own[2 * e]);
        own[2 * e + 1] = fmaf(wk[k], __uint_as_float(a[k][e] & 0xffff0000u), own[2 * e + 1]);
      }
#pragma unroll
    for (int e = 0; e < 8; ++e) own[e] *= inv;
  }
  auto x = __builtin_amdgcn_permlane32_swap(pack2(own[0], own[1]), pack2(own[4], own[5]), false, false);
  auto y = __builtin_amdgcn_permlane32_swap(pack2(own[2], own[3]), pack2(own[6], own[7]), false, false);
  *(GAS u32x4*)orow = u32x4{x[0], y[0], x[1], y[1]};
  asm volatile("" ::"v"(g0), "v"(g1), "v"(g2), "v"(peer_view));
  if (lane < 3 && peer_view == out1) __hip_atomic_store(sig_out, ((u64)in0 << 32) | (out1 + 1), RLX_AGENT);
}

}

extern "C" void kernel_launch(void* const* d_in, const int* in_sizes, int n_in, void* d_out, int out_size, void* d_ws,
                              size_t ws_size, hipStream_t stream) {
  using namespace ta;
  (void)in_sizes; (void)n_in; (void)out_size; (void)ws_size;
  (void)hipFuncSetAttribute((const void*)tree_attn, hipFuncAttributeMaxDynamicSharedMemorySize, LDS_BYTES);
  hipLaunchKernelGGL(tree_attn, dim3(NPAIR * NSPLIT), dim3((NW + 1) * 64), LDS_BYTES, stream,
                     (const __hip_bfloat16*)d_in[0], (const __hip_bfloat16*)d_in[1], (const __hip_bfloat16*)d_in[2],
                     (const __hip_bfloat16*)d_in[3], (const __hip_bfloat16*)d_in[4], (const __hip_bfloat16*)d_in[5],
                     (char*)d_ws, (__hip_bfloat16*)d_out);
}
```

```cpp
#include <hip/hip_runtime.h>
#include <hip/hip_bf16.h>

#define TA_PROBE 3
#define TA_HOLD 2000u
#define TA_WIN 20
#define TA_WSTEP 2
#define TA_TAU 545
#define TA_OFF 1000
#define TA_LSTEP 700
#define TA_BOOST0 0
#define TA_BOOST1 0
#define TA_CODEPF 128
#define TA_KNEE 7
#define TA_KDIV 4
#define TA_POLL2 1
#define TA_SIGLINE 1
#define TA_KMAX 7

#define STAMP(k) do {} while (0)

namespace ta {

constexpr int NB = 8, HKV = 8, CTX = 8192, HD = 256;
constexpr int NPAIR = NB * HKV, NSPLIT = 4, NW = 4;
constexpr int ROWB = HD * 2;
constexpr int WG_BYTES = CTX / NSPLIT * ROWB;
constexpr int NT = CTX / NSPLIT / 32;
constexpr int KP = 1040, VP = 1056;
constexpr int SLOT = 16 * VP, NSLOT = 5;
constexpr int P_OFF = NSLOT * SLOT;
constexpr int REF_OFF = P_OFF + NT / 2 * 2048;
constexpr int TV_OFF = REF_OFF + (NT + 1) * 128;
constexpr int PT_OFF = TV_OFF + 4 * ROWB;
constexpr int L_OFF = PT_OFF + 256;
constexpr int FLAG_OFF = L_OFF + NW * 128;
constexpr int F_DONE = 0, F_PRDY = 16, F_LAND = 32, F_REF = 36, F_Q = 40;
constexpr int LDS_BYTES = FLAG_OFF + 48;
constexpr int TK_OFF = REF_OFF - 4 * ROWB, Q_OFF = TK_OFF - 16 * KP, MK_OFF = Q_OFF - 2048;
constexpr float SCALE = 0.0625f, LOG2E = 1.4426950408889634f, C2 = SCALE * LOG2E;
constexpr int WIN = TA_WIN;
constexpr int MSG = 1024 + 256;
constexpr int CTRL_BYTES = NPAIR * 64 * 128;
static_assert(LDS_BYTES <= 160 * 1024 && MK_OFF >= P_OFF + 16 * 2048 && FLAG_OFF % 16 == 0, "LDS map");

static_assert(WIN >= 17 && WIN + 7 * TA_WSTEP <= 48, "publish / drain ladder assumes 1..3 chunks in flight");

typedef __bf16 bf16x8 __attribute__((ext_vector_type(8)));
typedef __bf16 bf16x2 __attribute__((ext_vector_type(2)));
typedef short s16x4 __attribute__((ext_vector_type(4)));
typedef float f32x16 __attribute__((ext_vector_type(16)));
typedef float f32x4 __attribute__((ext_vector_type(4)));
typedef float f32x2 __attribute__((ext_vector_type(2)));
typedef unsigned u32x4 __attribute__((ext_vector_type(4)));
typedef unsigned u32x2 __attribute__((ext_vector_type(2)));
typedef unsigned long long u64;
#define LAS __attribute__((address_space(3)))
#define GAS __attribute__((address_space(1)))
typedef LAS const char* lds_cptr;
typedef LAS char* lds_ptr;
typedef __amdgpu_buffer_rsrc_t srd_t;

#define SBAR() __builtin_amdgcn_sched_barrier(0)
#define PIN(x) asm volatile("" : "+v"(x))
#define MFMA32(a, b, c) __builtin_amdgcn_mfma_f32_32x32x16_bf16((a), (b), (c), 0, 0, 0)
#define RLX_AGENT __ATOMIC_RELAXED, __HIP_MEMORY_SCOPE_AGENT
#define RFL(x) __builtin_amdgcn_readfirstlane(x)

__device__ __forceinline__ srd_t make_srd(const void* p, int bytes) {
  return __builtin_amdgcn_make_buffer_rsrc((void*)p, (short)0, bytes, 0x00020000);
}

template <int PAD, bool NTL>
__device__ __forceinline__ void dma4(srd_t srd, unsigned voff, unsigned soff, unsigned ldsb) {
  unsigned keep;
  if constexpr (NTL) {
  asm volatile(
      "s_mov_b32 %0, m0\n\ts_mov_b32 m0, %4\n\ts_nop 3\n\t"
      "buffer_load_dwordx4 %1, %2, %3 offen nt lds\n\t"
      "s_add_u32 m0, %4, %5\n\ts_nop 0\n\t"
      "buffer_load_dwordx4 %1, %2, %3 offen offset:1024 nt lds\n\t"
      "s_add_u32 m0, %4, %6\n\ts_nop 0\n\t"
      "buffer_load_dwordx4 %1, %2, %3 offen offset:2048 nt lds\n\t"
      "s_add_u32 m0, %4, %7\n\ts_nop 0\n\t"
      "buffer_load_dwordx4 %1, %2, %3 offen offset:3072 nt lds\n\t"
      "s_mov_b32 m0, %0"
      : "=&s"(keep)
      : "v"(voff), "s"(srd), "s"(soff), "s"(ldsb), "n"(PAD), "n"(2 * PAD), "n"(3 * PAD)
      : "memory", "scc");
  } else {
  asm volatile(
      "s_mov_b32 %0, m0\n\ts_mov_b32 m0, %4\n\ts_nop 3\n\t"
      "buffer_load_dwordx4 %1, %2, %3 offen lds\n\t"
      "s_add_u32 m0, %4, %5\n\ts_nop 0\n\t"
      "buffer_load_dwordx4 %1, %2, %3 offen offset:1024 lds\n\t"
      "s_add_u32 m0, %4, %6\n\ts_nop 0\n\t"
      "buffer_load_dwordx4 %1, %2, %3 offen offset:2048 lds\n\t"
      "s_add_u32 m0, %4, %7\n\ts_nop 0\n\t"
      "buffer_load_dwordx4 %1, %2, %3 offen offset:3072 lds\n\t"
      "s_mov_b32 m0, %0"
      : "=&s"(keep)
      : "v"(voff), "s"(srd), "s"(soff), "s"(ldsb), "n"(PAD), "n"(2 * PAD), "n"(3 * PAD)
      : "memory", "scc");
  }
}
#define TA_PIECE(I, OFS, STEP) \
      "s_waitcnt vmcnt(%[n])\n\t" \
      "buffer_load_dwordx4 %[vo], %[srd], %[so] offen " OFS " nt lds\n\t" \
      "s_add_u32 m0, m0, " STEP "\n\t" \
      ".if (%[n] & 15) == " #I "\n\tds_write_b32 %[la], %[lv]\n\t.endif\n\t"
#define TA_GROUP(I0, I1, I2, I3) \
      TA_PIECE(I0, "", "%[pad]") TA_PIECE(I1, "offset:1024", "%[pad]") TA_PIECE(I2, "offset:2048", "%[pad]") \
      TA_PIECE(I3, "offset:3072", "%[padg]") "s_add_u32 %[so], %[so], 0x1000\n\t"
template <int N>
__device__ __forceinline__ void dma_chunk(srd_t srd, unsigned voff, unsigned soff, unsigned ldsb, unsigned pad, unsigned land_addr,
                                          int c, unsigned done_addr, u32x4& done, u64& now) {
  unsigned keep;
  asm volatile(
      "s_mov_b32 %[keep], m0\n\ts_mov_b32 m0, %[lds]\n\ts_nop 2\n\t"
      TA_GROUP(0, 1, 2, 3) TA_GROUP(4, 5, 6, 7) TA_GROUP(8, 9, 10, 11)
      "ds_read_b128 %[done], %[da]\n\ts_memrealtime %[now]\n\t"
      TA_GROUP(12, 13, 14, 15)
      "s_mov_b32 m0, %[keep]\n\ts_waitcnt lgkmcnt(0)"
      : [keep] "=&s"(keep), [so] "+s"(soff), [done] "=&v"(done), [now] "=&s"(now)
      : [vo] "v"(voff), [srd] "s"(srd), [lds] "s"(ldsb), [pad] "s"(pad), [padg] "s"(pad + 4096), [n] "n"(N),
        [la] "v"(land_addr), [lv] "v"(c - N / 16), [da] "v"(done_addr)
      : "memory", "scc");
}
__device__ __forceinline__ void dma2(srd_t srd, unsigned voff, unsigned soff, unsigned ldsb) {
  unsigned keep;
  asm volatile("s_mov_b32 %0, m0\n\ts_mov_b32 m0, %4\n\ts_nop 3\n\t"
               "buffer_load_dwordx4 %1, %2, %3 offen lds\n\t"
               "buffer_load_dwordx4 %1, %2, %3 offen offset:1024 lds\n\t"
               "s_mov_b32 m0, %0"
               : "=&s"(keep) : "v"(voff), "s"(srd), "s"(soff), "s"(ldsb) : "memory");
}
template <int N> __device__ __forceinline__ void wait_vm() { asm volatile("s_waitcnt vmcnt(%0)" ::"n"(N) : "memory"); }

__device__ __forceinline__ bf16x8 ld_frag(lds_cptr p) { return *(const LAS bf16x8*)p; }
__device__ __forceinline__ s16x4 ld_tr(lds_cptr p) { return __builtin_amdgcn_ds_read_tr16_b64_v4i16((LAS s16x4*)p); }
__device__ __forceinline__ bf16x8 join(s16x4 lo, s16x4 hi) {
  return __builtin_bit_cast(bf16x8, __builtin_shufflevector(lo, hi, 0, 1, 2, 3, 4, 5, 6, 7));
}
__device__ __forceinline__ float xhalf_max(float x) {
  auto r = __builtin_amdgcn_permlane32_swap(__float_as_uint(x), __float_as_uint(x), false, false);
  return fmaxf(__uint_as_float(r[0]), __uint_as_float(r[1]));
}
__device__ __forceinline__ float xhalf_sum(float x) {
  auto r = __builtin_amdgcn_permlane32_swap(__float_as_uint(x), __float_as_uint(x), false, false);
  return __uint_as_float(r[0]) + __uint_as_float(r[1]);
}
__device__ __forceinline__ unsigned pack2(float a, float b) {
  const bf16x2 v = {(__bf16)a, (__bf16)b};
  return __builtin_bit_cast(unsigned, v);
}
__device__ __forceinline__ bf16x8 pack8(const f32x16& p, int o) {
  bf16x8 r;
#pragma unroll
  for (int j = 0; j < 8; ++j) r[j] = (__bf16)p[o + j];
  return r;
}
__device__ __forceinline__ void flag_set(unsigned addr, unsigned v) {
  asm volatile("s_waitcnt lgkmcnt(0)\n\tds_write_b32 %0, %1" ::"v"(addr), "v"(v) : "memory");
}
__device__ __forceinline__ void flag_wait(unsigned addr, int need) {
#pragma unroll 1
  for (int spin = 0; spin < (1 << 16); ++spin) {
    unsigned v;
    asm volatile("ds_read_b32 %0, %1\n\ts_waitcnt lgkmcnt(0)" : "=v"(v) : "v"(addr) : "memory");
    if ((int)RFL(v) >= need) break;
    __builtin_amdgcn_s_sleep(1);
  }
}
__device__ __forceinline__ void flag_wait4(unsigned addr, int need) {
#pragma unroll 1
  for (int spin = 0; spin < (1 << 16); ++spin) {
    u32x4 v;
    asm volatile("ds_read_b128 %0, %1\n\ts_waitcnt lgkmcnt(0)" : "=v"(v) : "v"(addr) : "memory");
    if ((int)RFL(min(min(v[0], v[1]), min(v[2], v[3]))) >= need) break;
    __builtin_amdgcn_s_sleep(1);
  }
}

__global__ __launch_bounds__((NW + 1) * 64, 2) void tree_attn(
    const __hip_bfloat16* __restrict__ q, const __hip_bfloat16* __restrict__ kc, const __hip_bfloat16* __restrict__ vc,
    const __hip_bfloat16* __restrict__ kt, const __hip_bfloat16* __restrict__ vt, const __hip_bfloat16* __restrict__ mask,
    char* ws, __hip_bfloat16* __restrict__ out) {
  extern __shared__ __attribute__((aligned(16))) char lds[];
  u64 kbase, base2, now, pc;
  unsigned d0, d1;
  const int wid = RFL(threadIdx.x >> 6);
  asm volatile("s_load_dwordx2 %[kb], %[ka], 0x8\n\t"
               "s_load_dwordx2 %[b2], %[ka], %[sel]\n\t"
               "s_getpc_b64 %[pc]\n\t"
               "s_load_dword %[d0], %[pc], %[line]\n\t"
               "s_load_dword %[d1], %[pc], %[line] offset:64\n\t"
               "s_memrealtime %[now]"
               : [kb] "=&s"(kbase), [b2] "=&s"(base2), [pc] "=&s"(pc), [d0] "=&s"(d0), [d1] "=&s"(d1), [now] "=&s"(now)
               : [ka] "s"(__builtin_amdgcn_kernarg_segment_ptr()), [sel] "s"((wid >> 2) * 8),
                 [line] "s"(((blockIdx.x >> 3) * (NW + 1) + wid) * TA_CODEPF)
               : "memory");
  SBAR();
  const int lane = threadIdx.x & 63;
  const unsigned lds0 = (unsigned)(uintptr_t)lds;
  const unsigned voff = lane * 16;
  const int pair = blockIdx.x >> 2, split = blockIdx.x & 3;
  {
    unsigned keep;
#define TA_G4(VO, BASE, POL) \
        "global_load_lds_dwordx4 " VO ", " BASE POL "\n\ts_add_u32 m0, m0, %[pad]\n\ts_nop 0\n\t" \
        "global_load_lds_dwordx4 " VO ", " BASE " offset:1024" POL "\n\ts_add_u32 m0, m0, %[pad]\n\ts_nop 0\n\t" \
        "global_load_lds_dwordx4 " VO ", " BASE " offset:2048" POL "\n\ts_add_u32 m0, m0, %[pad]\n\ts_nop 0\n\t" \
        "global_load_lds_dwordx4 " VO ", " BASE " offset:3072" POL "\n\t"
    asm volatile(
        "s_mov_b32 %[keep], m0\n\ts_mov_b32 m0, %[dst2]\n\t"
        "s_waitcnt lgkmcnt(0)\n\t"
        TA_G4("%[vo2]", "%[b2]", "")
        "s_mov_b32 m0, %[dst]\n\ts_nop 0\n\t"
        TA_G4("%[vo]", "%[kb]", " nt")
        "s_mov_b32 m0, %[keep]"
        : [keep] "=&s"(keep), [kb] "+s"(kbase), [b2] "+s"(base2), [now] "+s"(now), [d0] "+s"(d0), [d1] "+s"(d1)
        : [vo] "v"(blockIdx.x * WG_BYTES + wid * 4096 + voff),
          [dst] "s"(lds0 + wid * 4 * KP + (wid >> 2) * (SLOT - 16 * KP)),
          [vo2] "v"((wid == NW ? blockIdx.x * WG_BYTES + 5 * 4096 : pair * 32 * ROWB + wid * 4096) + voff),
          [dst2] "s"(lds0 + (wid == NW ? SLOT + 4 * KP : Q_OFF + wid * 4 * KP)), [pad] "n"(KP - 1024)
        : "memory", "scc");
#undef TA_G4
  }
  unsigned probe_t = (unsigned)now;
#define PROBE(k) do { if (TA_PROBE == (k)) probe_t = (unsigned)__builtin_amdgcn_s_memrealtime(); } while (0)
  PROBE(5);
  const unsigned flags = lds0 + FLAG_OFF;
  if (threadIdx.x < 12) flag_set(flags + lane * 4, lane < 4 ? lane : lane == F_LAND / 4 ? 1 : 0);
  asm volatile("s_waitcnt lgkmcnt(0)\n\ts_barrier" ::: "memory");

  if (wid == NW) {
    __builtin_amdgcn_s_setprio(3);
    const srd_t sK = make_srd((const char*)kbase + (size_t)blockIdx.x * WG_BYTES, WG_BYTES);
#pragma unroll 1
    for (int g = 2; g < 4; ++g) dma4<KP - 1024, true>(sK, voff, 16384 + g * 4096, lds0 + SLOT + g * 4 * KP);
    const srd_t sV = make_srd((const char*)vc + (size_t)blockIdx.x * WG_BYTES, WG_BYTES);
    const unsigned t0 = (unsigned)now + TA_OFF / 10;
    u32x4 done = {0u, 0u, 0u, 0u};
    unsigned dst = lds0 + 2 * SLOT;
#pragma unroll 1
    for (int c = 2; c < 2 * NT; ++c) {
      if (c >= NSLOT && (int)RFL(min(min(done[0], done[1]), min(done[2], done[3]))) < c - NSLOT + 1)
        flag_wait4(flags + F_DONE, c - NSLOT + 1);
      const int lag = (int)(RFL((unsigned)now) - t0) - (((4 * c - 1) * (TA_TAU * 64 / 10)) >> 8);
      int k = lag <= 0 ? 0 : (lag * (655360 / TA_LSTEP)) >> 16;
      if (k > TA_KNEE) k = min(TA_KNEE + (k - TA_KNEE) / TA_KDIV, TA_KMAX);
      if (c < TA_BOOST0 || c >= 2 * NT - TA_BOOST1) k = 7;
      const srd_t srd = c < NT ? sK : sV;
      const unsigned soff = (c & (NT - 1)) * 16384, pad = c < NT ? KP - 1024 : VP - 1024;
#define TA_CH(K) dma_chunk<WIN - 1 + (K) * TA_WSTEP>(srd, voff, soff, dst, pad, flags + F_LAND, c, flags + F_DONE, done, now)
      if (k < 4) {
        if (k < 2) { if (k == 0) TA_CH(0); else TA_CH(1); } else { if (k == 2) TA_CH(2); else TA_CH(3); }
      } else {
        if (k < 6) { if (k == 4) TA_CH(4); else TA_CH(5); } else { if (k == 6) TA_CH(6); else TA_CH(7); }
      }
#undef TA_CH
      dst = dst == lds0 + (NSLOT - 1) * SLOT ? lds0 : dst + SLOT;
    }
    wait_vm<32>(); flag_set(flags + F_LAND, 2 * NT - 2);
    wait_vm<16>(); flag_set(flags + F_LAND, 2 * NT - 1);
    wait_vm<0>(); flag_set(flags + F_LAND, 2 * NT);
    return;
  }

  const int w = wid;
  const int r = lane & 31, h = lane >> 5;
  if (w == 3) {
    dma2(make_srd((const char*)kt + (size_t)pair * 16 * ROWB, 16 * ROWB), voff, split * 4 * ROWB, lds0 + TK_OFF);
    dma2(make_srd((const char*)mask + (pair >> 3) * 512, 512), voff, 0, lds0 + MK_OFF);
  }
  if (w == 2) dma2(make_srd((const char*)vt + (size_t)pair * 16 * ROWB, 16 * ROWB), voff, split * 4 * ROWB, lds0 + TV_OFF);
  const int qrow = 2 * (r & 15) + (r >> 4);
  const int koff = (r & 15) * KP + (r >> 4) * 512 + h * 16;
  const int trofs = ((lane >> 4) & 1) * 128 + w * 32 + (lane & 3) * 8;
  const int vofs = (4 * h + ((lane & 15) >> 2)) * VP + trofs;
  const srd_t sZ = make_srd(ws + CTRL_BYTES + (size_t)pair * 64 * MSG, 64 * MSG);
  const int peer = (split + 1 + (lane & 3)) & 3;
  GAS u64* sig_in = (GAS u64*)(ws + ((pair * 4 + split) * 4 + w) * 128) + peer;
  GAS u64* sig_out = (GAS u64*)(ws + ((pair * 4 + peer) * 4 + w) * 128) + split;
  unsigned zin[3];
#pragma unroll
  for (int k = 0; k < 3; ++k) zin[k] = ((split * 4 + ((split + 1 + k) & 3)) * 4 + w) * MSG;
  if (w == 3) wait_vm<4>(); else if (w == 2) wait_vm<2>(); else wait_vm<0>();
  PROBE(6);
  if (lane == 0) asm volatile("ds_add_u32 %0, %1" ::"v"(flags + F_Q), "v"(1u) : "memory");
  flag_wait(flags + F_Q, NW);
  STAMP(1);
  bf16x8 qf[16];
#pragma unroll
  for (int kk = 0; kk < 16; ++kk) qf[kk] = ld_frag((lds_cptr)lds + Q_OFF + koff + kk * 32);

  const lds_ptr refp = (lds_ptr)lds + REF_OFF + r * 4;
  float m = -INFINITY, l = 0.f;
  if (w == 3) {
    wait_vm<0>();
    const u32x2 mbits = *(const LAS u32x2*)((lds_cptr)lds + MK_OFF + (qrow & 15) * 32 + split * 8);
    const lds_cptr tk = (lds_cptr)lds + TK_OFF + (r & 3) * ROWB + h * 16;
    f32x16 s = f32x16{};
#pragma unroll
    for (int kk = 0; kk < 16; ++kk) s = MFMA32(ld_frag(tk + kk * 32), qf[kk], s);
#pragma unroll
    for (int i = 0; i < 4; ++i) {
      const float mk = __uint_as_float((i & 1) ? (mbits[i >> 1] & 0xffff0000u) : (mbits[i >> 1] << 16));
      s[i] = fmaf(s[i], SCALE, (1.0f - mk) * -1000000000.0f) * LOG2E;
      m = fmaxf(m, s[i]);
    }
#pragma unroll
    for (int i = 0; i < 4; ++i) { s[i] = __builtin_amdgcn_exp2f(s[i] - m); l += s[i]; }
    if (h) l = 0.f;
    *(LAS float*)refp = m;
    *(LAS u32x2*)((lds_ptr)lds + PT_OFF + r * 8) = u32x2{pack2(s[0], s[1]), pack2(s[2], s[3])};
    flag_set(flags + F_REF, 1);
  }
  STAMP(2);
  if (TA_PROBE == 1) { flag_wait(flags + F_LAND, 1); PROBE(1); }

  int slot_off = w * SLOT;
  const lds_ptr pbuf = (lds_ptr)lds + P_OFF + lane * 16;
  bf16x8 preg[NT / 8][2];

  auto k_tile = [&](int c, bf16x8& p0, bf16x8& p1) {
    flag_wait(flags + F_LAND, c + 1);
    const lds_cptr kfrag = (lds_cptr)lds + slot_off + koff;
    f32x16 s = f32x16{};
    bf16x8 kf[8];
#pragma unroll
    for (int kk = 0; kk < 8; ++kk) kf[kk] = ld_frag(kfrag + kk * 32);
    SBAR();
#pragma unroll
    for (int kk = 0; kk < 8; ++kk) { s = MFMA32(kf[kk], qf[kk], s); kf[kk] = ld_frag(kfrag + (kk + 8) * 32); SBAR(); }
    flag_set(flags + F_DONE + w * 4, min(c + NW, NT));
    slot_off = slot_off == 0 ? (NSLOT - 1) * SLOT : slot_off - SLOT;
    SBAR();
#pragma unroll
    for (int kk = 0; kk < 8; ++kk) s = MFMA32(kf[kk], qf[kk + 8], s);
    float tmax = fmaxf(s[0], s[1]);
#pragma unroll
    for (int i = 2; i < 16; ++i) tmax = fmaxf(tmax, s[i]);
    tmax = xhalf_max(tmax) * C2;
    PIN(tmax);
    flag_wait(flags + F_REF, c + 1);
    const float mn = fmaxf(*(const LAS float*)(refp + c * 128), tmax);
    *(LAS float*)(refp + (c + 1) * 128) = mn;
    flag_set(flags + F_REF, c + 2);
    const float alpha = __builtin_amdgcn_exp2f(m - mn);
    m = mn;
    float ps = 0.f;
#pragma unroll
    for (int i = 0; i < 16; ++i) { s[i] = __builtin_amdgcn_exp2f(fmaf(s[i], C2, -mn)); ps += s[i]; }
    l = fmaf(l, alpha, ps);
    p0 = pack8(s, 0); p1 = pack8(s, 8);
    PIN(l); PIN(m);
  };
#pragma unroll 1
  for (int c = w; c < NT / 2; c += NW) {
    bf16x8 p0, p1;
    k_tile(c, p0, p1);
    *(LAS bf16x8*)(pbuf + c * 2048) = p0;
    *(LAS bf16x8*)(pbuf + c * 2048 + 1024) = p1;
    if (c >= 8 && c < 12) PROBE(7);
  }
  unsigned in0 = __hip_atomic_load((GAS unsigned*)sig_in, RLX_AGENT);
  unsigned out1 = __hip_atomic_load((GAS unsigned*)sig_out, RLX_AGENT) + 1;
#pragma unroll
  for (int t = 0; t < NT / 8; ++t) { k_tile(NT / 2 + 4 * t + w, preg[t][0], preg[t][1]); PIN(preg[t][0]); PIN(preg[t][1]); }
  wait_vm<0>();
  flag_set(flags + F_PRDY + w * 4, 1);
  flag_wait4(flags + F_PRDY, 1);
  PROBE(2);

  f32x16 o[2];
  float rcur = *(const LAS float*)refp;
  {
    const lds_cptr tv = (lds_cptr)lds + TV_OFF + ((lane & 15) >> 2) * ROWB + trofs;
    u32x2 pt2 = *(const LAS u32x2*)((lds_cptr)lds + PT_OFF + r * 8);
    if (h) pt2 = u32x2{0u, 0u};
    const bf16x8 pt = __builtin_bit_cast(bf16x8, u32x4{pt2[0], pt2[1], 0u, 0u});
#pragma unroll
    for (int cb = 0; cb < 2; ++cb) { const s16x4 v4 = ld_tr(tv + cb * 256); o[cb] = MFMA32(join(v4, v4), pt, f32x16{}); }
  }
  const float M = *(const LAS float*)(refp + NT * 128);
  *(LAS float*)((lds_ptr)lds + L_OFF + w * 128 + r * 4) = xhalf_sum(l) * __builtin_amdgcn_exp2f(m - M);
  slot_off = (NT % NSLOT) * SLOT;
  auto v_tiles = [&](int v0, int v1) {
#pragma unroll 1
    for (int v = v0; v < v1; ++v) {
      const float rnew = *(const LAS float*)(refp + (v + 1) * 128);
      bf16x8 p0 = ld_frag(pbuf + (v & (NT / 2 - 1)) * 2048), p1 = ld_frag(pbuf + (v & (NT / 2 - 1)) * 2048 + 1024);
      if (__any(rnew != rcur)) {
        const float alpha = __builtin_amdgcn_exp2f(rcur - rnew);
        o[0] *= alpha; o[1] *= alpha;
        rcur = rnew;
      }
      PIN(p0); PIN(p1);
      flag_wait(flags + F_LAND, NT + v + 1);
      const lds_cptr vfrag = (lds_cptr)lds + slot_off + vofs;
      bf16x8 vf[4];
#pragma unroll
      for (int i = 0; i < 4; ++i)
        vf[i] = join(ld_tr(vfrag + (i >> 1) * 512 + (i & 1) * 256), ld_tr(vfrag + 8 * VP + (i >> 1) * 512 + (i & 1) * 256));
      SBAR();
      o[0] = MFMA32(vf[0], p0, o[0]);
      o[1] = MFMA32(vf[1], p0, o[1]);
      SBAR();
      flag_set(flags + F_DONE + w * 4, NT + v + 1);
      slot_off = slot_off == (NSLOT - 1) * SLOT ? 0 : slot_off + SLOT;
      o[0] = MFMA32(vf[2], p1, o[0]);
      o[1] = MFMA32(vf[3], p1, o[1]);
    }
  };
  v_tiles(0, 20);
  flag_wait4(flags + F_DONE, NT + 16);
#pragma unroll
  for (int j = 0; j < 4; ++j) { *(LAS bf16x8*)(pbuf + (4 * j + w) * 2048) = preg[j][0]; *(LAS bf16x8*)(pbuf + (4 * j + w) * 2048 + 1024) = preg[j][1]; }
  flag_set(flags + F_PRDY + w * 4, 2);
  v_tiles(20, 32);
  flag_wait4(flags + F_PRDY, 2);
  float L = 0.f;
#pragma unroll
  for (int i = 0; i < NW; ++i) L += *(const LAS float*)((lds_cptr)lds + L_OFF + i * 128 + r * 4);
  v_tiles(32, 36);
  flag_wait4(flags + F_DONE, NT + 32);
#pragma unroll
  for (int j = 4; j < 8; ++j) { *(LAS bf16x8*)(pbuf + (4 * j + w) * 2048) = preg[j][0]; *(LAS bf16x8*)(pbuf + (4 * j + w) * 2048 + 1024) = preg[j][1]; }
  flag_set(flags + F_PRDY + w * 4, 3);
  v_tiles(36, 48);
  flag_wait4(flags + F_PRDY, 3);
  unsigned zout[4];
#pragma unroll
  for (int j = 0; j < 4; ++j) { zout[j] = (j == split ? 64 : (j * 4 + split) * 4 + w) * MSG + voff; PIN(zout[j]); }
  char* orow = (char*)out + ((size_t)pair * 32 + qrow) * ROWB + split * 128 + w * 32 + h * 16;
  PIN(orow);
  unsigned mlw = __float_as_uint(h ? L : M);
  PIN(mlw); PIN(in0); PIN(out1);
  v_tiles(48, NT);
  STAMP(3);
  PROBE(3);

#pragma unroll
  for (int j = 0; j < 4; ++j) {
    u32x4 x;
#pragma unroll
    for (int e = 0; e < 4; ++e) x[e] = pack2(o[j >> 1][8 * (j & 1) + 2 * e], o[j >> 1][8 * (j & 1) + 2 * e + 1]);
    __builtin_amdgcn_raw_buffer_store_b128(x, sZ, zout[j], 0, 16);
    __builtin_amdgcn_raw_buffer_store_b32(mlw, sZ, zout[j] + 1024 - 12 * lane, 0, 16);
  }
  unsigned g0 = __hip_atomic_load((GAS unsigned*)sig_in, RLX_AGENT), g1 = 0, g2 = 0;
  SBAR();
  float own[8];
#pragma unroll
  for (int e = 0; e < 8; ++e) {
    const float lo = (split & 1) ? o[0][8 + e] : o[0][e], hi = (split & 1) ? o[1][8 + e] : o[1][e];
    own[e] = (split & 2) ? hi : lo;
    PIN(own[e]);
  }
  wait_vm<0>();
  PIN(g0);
  STAMP(5);
  if (lane < 3) __hip_atomic_store(sig_out, ((u64)in0 << 32) | out1, RLX_AGENT);
  if (((unsigned)__ballot(g0 != in0) & 7u) != 7u) {
    unsigned left = 1u << 15, t;
#define TA_POLL(G) "s_waitcnt vmcnt(2)\n\tv_cmp_ne_u32 vcc, %[in0], " G "\n\ts_and_b32 %[t], vcc_lo, 7\n\ts_cmp_eq_u32 %[t], 7\n\t" \
                   "s_cbranch_scc1 2f\n\tglobal_load_dword " G ", %[p], off sc1\n\t"
    asm volatile(
        "global_load_dword %[g0], %[p], off sc1\n\ts_sleep 3\n\t"
        "global_load_dword %[g1], %[p], off sc1\n\ts_sleep 3\n\t"
        "global_load_dword %[g2], %[p], off sc1\n"
        "1:\n\t" TA_POLL("%[g0]") TA_POLL("%[g1]") TA_POLL("%[g2]")
        "s_sub_u32 %[left], %[left], 1\n\ts_cbranch_scc0 1b\n"
        "2:"
        : [g0] "=&v"(g0), [g1] "=&v"(g1), [g2] "=&v"(g2), [left] "+s"(left), [t] "=&s"(t)
        : [p] "v"(sig_in), [in0] "v"(in0)
        : "memory", "vcc", "scc");
#undef TA_POLL
  }
  __builtin_amdgcn_fence(__ATOMIC_ACQUIRE, "wavefront");
  asm volatile("" ::: "memory");
  u32x4 a[3];
  unsigned ml[3];
#pragma unroll
  for (int k = 0; k < 3; ++k) {
    ml[k] = __builtin_amdgcn_raw_buffer_load_b32(sZ, 1024 + lane * 4, zin[k], 16);
    a[k] = __builtin_amdgcn_raw_buffer_load_b128(sZ, voff, zin[k], 16);
  }
  const unsigned peer_view = __hip_atomic_load((GAS unsigned*)sig_in + 1, RLX_AGENT);
  STAMP(4);
  {
    float mk[3], lk[3], wk[3], G = M;
#pragma unroll
    for (int k = 0; k < 3; ++k) {
      auto x = __builtin_amdgcn_permlane32_swap(ml[k], ml[k], false, false);
      mk[k] = __uint_as_float(x[0]); lk[k] = __uint_as_float(x[1]);
      G = fmaxf(G, mk[k]);
    }
    const float w0 = __builtin_amdgcn_exp2f(M - G);
    float den = w0 * L;
#pragma unroll
    for (int k = 0; k < 3; ++k) { wk[k] = __builtin_amdgcn_exp2f(mk[k] - G); den = fmaf(wk[k], lk[k], den); }
    const float inv = 1.0f / den;
#pragma unroll
    for (int e = 0; e < 8; ++e) own[e] *= w0;
#pragma unroll
    for (int k = 0; k < 3; ++k)
#pragma unroll
      for (int e = 0; e < 4; ++e) {
        own[2 * e] = fmaf(wk[k], __uint_as_float(a[k][e] << 16), own[2 * e]);
        own[2 * e + 1] = fmaf(wk[k], __uint_as_float(a[k][e] & 0xffff0000u), own[2 * e + 1]);
      }
#pragma unroll
    for (int e = 0; e < 8; ++e) own[e] *= inv;
  }
  auto x = __builtin_amdgcn_permlane32_swap(pack2(own[0], own[1]), pack2(own[4], own[5]), false, false);
  auto y = __builtin_amdgcn_permlane32_swap(pack2(own[2], own[3]), pack2(own[6], own[7]), false, false);
  *(GAS u32x4*)orow = u32x4{x[0], y[0], x[1], y[1]};
  asm volatile("" ::"v"(g0), "v"(g1), "v"(g2), "v"(peer_view));
  if (lane < 3 && peer_view == out1) __hip_atomic_store(sig_out, ((u64)in0 << 32) | (out1 + 1), RLX_AGENT);
  if (TA_PROBE == 4) { wait_vm<0>(); PROBE(4); }
  while ((unsigned)__builtin_amdgcn_s_memrealtime() - probe_t < TA_HOLD) __builtin_amdgcn_s_sleep(1);
}

}

extern "C" void kernel_launch(void* const* d_in, const int* in_sizes, int n_in, void* d_out, int out_size, void* d_ws,
                              size_t ws_size, hipStream_t stream) {
  using namespace ta;
  (void)in_sizes; (void)n_in; (void)out_size; (void)ws_size;
  (void)hipFuncSetAttribute((const void*)tree_attn, hipFuncAttributeMaxDynamicSharedMemorySize, LDS_BYTES);
  hipLaunchKernelGGL(tree_attn, dim3(NPAIR * NSPLIT), dim3((NW + 1) * 64), LDS_BYTES, stream,
                     (const __hip_bfloat16*)d_in[0], (const __hip_bfloat16*)d_in[1], (const __hip_bfloat16*)d_in[2],
                     (const __hip_bfloat16*)d_in[3], (const __hip_bfloat16*)d_in[4], (const __hip_bfloat16*)d_in[5],
                     (char*)d_ws, (__hip_bfloat16*)d_out);
}
```

```cpp
#include <hip/hip_runtime.h>
#include <hip/hip_bf16.h>

#define TA_OUTPOL 1
#define TA_WIN 20
#define TA_WSTEP 2
#define TA_TAU 545
#define TA_OFF 1000
#define TA_LSTEP 700
#define TA_BOOST0 0
#define TA_BOOST1 0
#define TA_CODEPF 128
#define TA_KNEE 7
#define TA_KDIV 4
#define TA_POLL2 1
#define TA_SIGLINE 1
#define TA_KMAX 7

#define STAMP(k) do {} while (0)

namespace ta {

constexpr int NB = 8, HKV = 8, CTX = 8192, HD = 256;
constexpr int NPAIR = NB * HKV, NSPLIT = 4, NW = 4;
constexpr int ROWB = HD * 2;
constexpr int WG_BYTES = CTX / NSPLIT * ROWB;
constexpr int NT = CTX / NSPLIT / 32;
constexpr int KP = 1040, VP = 1056;
constexpr int SLOT = 16 * VP, NSLOT = 5;
constexpr int P_OFF = NSLOT * SLOT;
constexpr int REF_OFF = P_OFF + NT / 2 * 2048;
constexpr int TV_OFF = REF_OFF + (NT + 1) * 128;
constexpr int PT_OFF = TV_OFF + 4 * ROWB;
constexpr int L_OFF = PT_OFF + 256;
constexpr int FLAG_OFF = L_OFF + NW * 128;
constexpr int F_DONE = 0, F_PRDY = 16, F_LAND = 32, F_REF = 36, F_Q = 40;
constexpr int LDS_BYTES = FLAG_OFF + 48;
constexpr int TK_OFF = REF_OFF - 4 * ROWB, Q_OFF = TK_OFF - 16 * KP, MK_OFF = Q_OFF - 2048;
constexpr float SCALE = 0.0625f, LOG2E = 1.4426950408889634f, C2 = SCALE * LOG2E;
constexpr int WIN = TA_WIN;
constexpr int MSG = 1024 + 256;
constexpr int CTRL_BYTES = NPAIR * 64 * 128;
static_assert(LDS_BYTES <= 160 * 1024 && MK_OFF >= P_OFF + 16 * 2048 && FLAG_OFF % 16 == 0, "LDS map");

static_assert(WIN >= 17 && WIN + 7 * TA_WSTEP <= 48, "publish / drain ladder assumes 1..3 chunks in flight");

typedef __bf16 bf16x8 __attribute__((ext_vector_type(8)));
typedef __bf16 bf16x2 __attribute__((ext_vector_type(2)));
typedef short s16x4 __attribute__((ext_vector_type(4)));
typedef float f32x16 __attribute__((ext_vector_type(16)));
typedef float f32x4 __attribute__((ext_vector_type(4)));
typedef float f32x2 __attribute__((ext_vector_type(2)));
typedef unsigned u32x4 __attribute__((ext_vector_type(4)));
typedef unsigned u32x2 __attribute__((ext_vector_type(2)));
typedef unsigned long long u64;
#define LAS __attribute__((address_space(3)))
#define GAS __attribute__((address_space(1)))
typedef LAS const char* lds_cptr;
typedef LAS char* lds_ptr;
typedef __amdgpu_buffer_rsrc_t srd_t;

#define SBAR() __builtin_amdgcn_sched_barrier(0)
#define PIN(x) asm volatile("" : "+v"(x))
#define MFMA32(a, b, c) __builtin_amdgcn_mfma_f32_32x32x16_bf16((a), (b), (c), 0, 0, 0)
#define RLX_AGENT __ATOMIC_RELAXED, __HIP_MEMORY_SCOPE_AGENT
#define RFL(x) __builtin_amdgcn_readfirstlane(x)

__device__ __forceinline__ srd_t make_srd(const void* p, int bytes) {
  return __builtin_amdgcn_make_buffer_rsrc((void*)p, (short)0, bytes, 0x00020000);
}

template <int PAD, bool NTL>
__device__ __forceinline__ void dma4(srd_t srd, unsigned voff, unsigned soff, unsigned ldsb) {
  unsigned keep;
  if constexpr (NTL) {
  asm volatile(
      "s_mov_b32 %0, m0\n\ts_mov_b32 m0, %4\n\ts_nop 3\n\t"
      "buffer_load_dwordx4 %1, %2, %3 offen nt lds\n\t"
      "s_add_u32 m0, %4, %5\n\ts_nop 0\n\t"
      "buffer_load_dwordx4 %1, %2, %3 offen offset:1024 nt lds\n\t"
      "s_add_u32 m0, %4, %6\n\ts_nop 0\n\t"
      "buffer_load_dwordx4 %1, %2, %3 offen offset:2048 nt lds\n\t"
      "s_add_u32 m0, %4, %7\n\ts_nop 0\n\t"
      "buffer_load_dwordx4 %1, %2, %3 offen offset:3072 nt lds\n\t"
      "s_mov_b32 m0, %0"
      : "=&s"(keep)
      : "v"(voff), "s"(srd), "s"(soff), "s"(ldsb), "n"(PAD), "n"(2 * PAD), "n"(3 * PAD)
      : "memory", "scc");
  } else {
  asm volatile(
      "s_mov_b32 %0, m0\n\ts_mov_b32 m0, %4\n\ts_nop 3\n\t"
      "buffer_load_dwordx4 %1, %2, %3 offen lds\n\t"
      "s_add_u32 m0, %4, %5\n\ts_nop 0\n\t"
      "buffer_load_dwordx4 %1, %2, %3 offen offset:1024 lds\n\t"
      "s_add_u32 m0, %4, %6\n\ts_nop 0\n\t"
      "buffer_load_dwordx4 %1, %2, %3 offen offset:2048 lds\n\t"
      "s_add_u32 m0, %4, %7\n\ts_nop 0\n\t"
      "buffer_load_dwordx4 %1, %2, %3 offen offset:3072 lds\n\t"
      "s_mov_b32 m0, %0"
      : "=&s"(keep)
      : "v"(voff), "s"(srd), "s"(soff), "s"(ldsb), "n"(PAD), "n"(2 * PAD), "n"(3 * PAD)
      : "memory", "scc");
  }
}
#define TA_PIECE(I, OFS, STEP) \
      "s_waitcnt vmcnt(%[n])\n\t" \
      "buffer_load_dwordx4 %[vo], %[srd], %[so] offen " OFS " nt lds\n\t" \
      "s_add_u32 m0, m0, " STEP "\n\t" \
      ".if (%[n] & 15) == " #I "\n\tds_write_b32 %[la], %[lv]\n\t.endif\n\t"
#define TA_GROUP(I0, I1, I2, I3) \
      TA_PIECE(I0, "", "%[pad]") TA_PIECE(I1, "offset:1024", "%[pad]") TA_PIECE(I2, "offset:2048", "%[pad]") \
      TA_PIECE(I3, "offset:3072", "%[padg]") "s_add_u32 %[so], %[so], 0x1000\n\t"
template <int N>
__device__ __forceinline__ void dma_chunk(srd_t srd, unsigned voff, unsigned soff, unsigned ldsb, unsigned pad, unsigned land_addr,
                                          int c, unsigned done_addr, u32x4& done, u64& now) {
  unsigned keep;
  asm volatile(
      "s_mov_b32 %[keep], m0\n\ts_mov_b32 m0, %[lds]\n\ts_nop 2\n\t"
      TA_GROUP(0, 1, 2, 3) TA_GROUP(4, 5, 6, 7) TA_GROUP(8, 9, 10, 11)
      "ds_read_b128 %[done], %[da]\n\ts_memrealtime %[now]\n\t"
      TA_GROUP(12, 13, 14, 15)
      "s_mov_b32 m0, %[keep]\n\ts_waitcnt lgkmcnt(0)"
      : [keep] "=&s"(keep), [so] "+s"(soff), [done] "=&v"(done), [now] "=&s"(now)
      : [vo] "v"(voff), [srd] "s"(srd), [lds] "s"(ldsb), [pad] "s"(pad), [padg] "s"(pad + 4096), [n] "n"(N),
        [la] "v"(land_addr), [lv] "v"(c - N / 16), [da] "v"(done_addr)
      : "memory", "scc");
}
__device__ __forceinline__ void dma2(srd_t srd, unsigned voff, unsigned soff, unsigned ldsb) {
  unsigned keep;
  asm volatile("s_mov_b32 %0, m0\n\ts_mov_b32 m0, %4\n\ts_nop 3\n\t"
               "buffer_load_dwordx4 %1, %2, %3 offen lds\n\t"
               "buffer_load_dwordx4 %1, %2, %3 offen offset:1024 lds\n\t"
               "s_mov_b32 m0, %0"
               : "=&s"(keep) : "v"(voff), "s"(srd), "s"(soff), "s"(ldsb) : "memory");
}
template <int N> __device__ __forceinline__ void wait_vm() { asm volatile("s_waitcnt vmcnt(%0)" ::"n"(N) : "memory"); }

__device__ __forceinline__ bf16x8 ld_frag(lds_cptr p) { return *(const LAS bf16x8*)p; }
__device__ __forceinline__ s16x4 ld_tr(lds_cptr p) { return __builtin_amdgcn_ds_read_tr16_b64_v4i16((LAS s16x4*)p); }
__device__ __forceinline__ bf16x8 join(s16x4 lo, s16x4 hi) {
  return __builtin_bit_cast(bf16x8, __builtin_shufflevector(lo, hi, 0, 1, 2, 3, 4, 5, 6, 7));
}
__device__ __forceinline__ float xhalf_max(float x) {
  auto r = __builtin_amdgcn_permlane32_swap(__float_as_uint(x), __float_as_uint(x), false, false);
  return fmaxf(__uint_as_float(r[0]), __uint_as_float(r[1]));
}
__device__ __forceinline__ float xhalf_sum(float x) {
  auto r = __builtin_amdgcn_permlane32_swap(__float_as_uint(x), __float_as_uint(x), false, false);
  return __uint_as_float(r[0]) + __uint_as_float(r[1]);
}
__device__ __forceinline__ unsigned pack2(float a, float b) {
  const bf16x2 v = {(__bf16)a, (__bf16)b};
  return __builtin_bit_cast(unsigned, v);
}
__device__ __forceinline__ bf16x8 pack8(const f32x16& p, int o) {
  bf16x8 r;
#pragma unroll
  for (int j = 0; j < 8; ++j) r[j] = (__bf16)p[o + j];
  return r;
}
__device__ __forceinline__ void flag_set(unsigned addr, unsigned v) {
  asm volatile("s_waitcnt lgkmcnt(0)\n\tds_write_b32 %0, %1" ::"v"(addr), "v"(v) : "memory");
}
__device__ __forceinline__ void flag_wait(unsigned addr, int need) {
#pragma unroll 1
  for (int spin = 0; spin < (1 << 16); ++spin) {
    unsigned v;
    asm volatile("ds_read_b32 %0, %1\n\ts_waitcnt lgkmcnt(0)" : "=v"(v) : "v"(addr) : "memory");
    if ((int)RFL(v) >= need) break;
    __builtin_amdgcn_s_sleep(1);
  }
}
__device__ __forceinline__ void flag_wait4(unsigned addr, int need) {
#pragma unroll 1
  for (int spin = 0; spin < (1 << 16); ++spin) {
    u32x4 v;
    asm volatile("ds_read_b128 %0, %1\n\ts_waitcnt lgkmcnt(0)" : "=v"(v) : "v"(addr) : "memory");
    if ((int)RFL(min(min(v[0], v[1]), min(v[2], v[3]))) >= need) break;
    __builtin_amdgcn_s_sleep(1);
  }
}

__global__ __launch_bounds__((NW + 1) * 64, 2) void tree_attn(
    const __hip_bfloat16* __restrict__ q, const __hip_bfloat16* __restrict__ kc, const __hip_bfloat16* __restrict__ vc,
    const __hip_bfloat16* __restrict__ kt, const __hip_bfloat16* __restrict__ vt, const __hip_bfloat16* __restrict__ mask,
    char* ws, __hip_bfloat16* __restrict__ out) {
  extern __shared__ __attribute__((aligned(16))) char lds[];
  u64 kbase, base2, now, pc;
  unsigned d0, d1;
  const int wid = RFL(threadIdx.x >> 6);
  asm volatile("s_load_dwordx2 %[kb], %[ka], 0x8\n\t"
               "s_load_dwordx2 %[b2], %[ka], %[sel]\n\t"
               "s_getpc_b64 %[pc]\n\t"
               "s_load_dword %[d0], %[pc], %[line]\n\t"
               "s_load_dword %[d1], %[pc], %[line] offset:64\n\t"
               "s_memrealtime %[now]"
               : [kb] "=&s"(kbase), [b2] "=&s"(base2), [pc] "=&s"(pc), [d0] "=&s"(d0), [d1] "=&s"(d1), [now] "=&s"(now)
               : [ka] "s"(__builtin_amdgcn_kernarg_segment_ptr()), [sel] "s"((wid >> 2) * 8),
                 [line] "s"(((blockIdx.x >> 3) * (NW + 1) + wid) * TA_CODEPF)
               : "memory");
  SBAR();
  const int lane = threadIdx.x & 63;
  const unsigned lds0 = (unsigned)(uintptr_t)lds;
  const unsigned voff = lane * 16;
  const int pair = blockIdx.x >> 2, split = blockIdx.x & 3;
  {
    unsigned keep;
#define TA_G4(VO, BASE, POL) \
        "global_load_lds_dwordx4 " VO ", " BASE POL "\n\ts_add_u32 m0, m0, %[pad]\n\ts_nop 0\n\t" \
        "global_load_lds_dwordx4 " VO ", " BASE " offset:1024" POL "\n\ts_add_u32 m0, m0, %[pad]\n\ts_nop 0\n\t" \
        "global_load_lds_dwordx4 " VO ", " BASE " offset:2048" POL "\n\ts_add_u32 m0, m0, %[pad]\n\ts_nop 0\n\t" \
        "global_load_lds_dwordx4 " VO ", " BASE " offset:3072" POL "\n\t"
    asm volatile(
        "s_mov_b32 %[keep], m0\n\ts_mov_b32 m0, %[dst2]\n\t"
        "s_waitcnt lgkmcnt(0)\n\t"
        TA_G4("%[vo2]", "%[b2]", "")
        "s_mov_b32 m0, %[dst]\n\ts_nop 0\n\t"
        TA_G4("%[vo]", "%[kb]", " nt")
        "s_mov_b32 m0, %[keep]"
        : [keep] "=&s"(keep), [kb] "+s"(kbase), [b2] "+s"(base2), [now] "+s"(now), [d0] "+s"(d0), [d1] "+s"(d1)
        : [vo] "v"(blockIdx.x * WG_BYTES + wid * 4096 + voff),
          [dst] "s"(lds0 + wid * 4 * KP + (wid >> 2) * (SLOT - 16 * KP)),
          [vo2] "v"((wid == NW ? blockIdx.x * WG_BYTES + 5 * 4096 : pair * 32 * ROWB + wid * 4096) + voff),
          [dst2] "s"(lds0 + (wid == NW ? SLOT + 4 * KP : Q_OFF + wid * 4 * KP)), [pad] "n"(KP - 1024)
        : "memory", "scc");
#undef TA_G4
  }
#define PROBE(k) do {} while (0)
  const unsigned flags = lds0 + FLAG_OFF;
  if (threadIdx.x < 12) flag_set(flags + lane * 4, lane < 4 ? lane : lane == F_LAND / 4 ? 1 : 0);
  asm volatile("s_waitcnt lgkmcnt(0)\n\ts_barrier" ::: "memory");

  if (wid == NW) {
    __builtin_amdgcn_s_setprio(3);
    const srd_t sK = make_srd((const char*)kbase + (size_t)blockIdx.x * WG_BYTES, WG_BYTES);
#pragma unroll 1
    for (int g = 2; g < 4; ++g) dma4<KP - 1024, true>(sK, voff, 16384 + g * 4096, lds0 + SLOT + g * 4 * KP);
    const srd_t sV = make_srd((const char*)vc + (size_t)blockIdx.x * WG_BYTES, WG_BYTES);
    const unsigned t0 = (unsigned)now + TA_OFF / 10;
    u32x4 done = {0u, 0u, 0u, 0u};
    unsigned dst = lds0 + 2 * SLOT;
#pragma unroll 1
    for (int c = 2; c < 2 * NT; ++c) {
      if (c >= NSLOT && (int)RFL(min(min(done[0], done[1]), min(done[2], done[3]))) < c - NSLOT + 1)
        flag_wait4(flags + F_DONE, c - NSLOT + 1);
      const int lag = (int)(RFL((unsigned)now) - t0) - (((4 * c - 1) * (TA_TAU * 64 / 10)) >> 8);
      int k = lag <= 0 ? 0 : (lag * (655360 / TA_LSTEP)) >> 16;
      if (k > TA_KNEE) k = min(TA_KNEE + (k - TA_KNEE) / TA_KDIV, TA_KMAX);
      if (c < TA_BOOST0 || c >= 2 * NT - TA_BOOST1) k = 7;
      const srd_t srd = c < NT ? sK : sV;
      const unsigned soff = (c & (NT - 1)) * 16384, pad = c < NT ? KP - 1024 : VP - 1024;
#define TA_CH(K) dma_chunk<WIN - 1 + (K) * TA_WSTEP>(srd, voff, soff, dst, pad, flags + F_LAND, c, flags + F_DONE, done, now)
      if (k < 4) {
        if (k < 2) { if (k == 0) TA_CH(0); else TA_CH(1); } else { if (k == 2) TA_CH(2); else TA_CH(3); }
      } else {
        if (k < 6) { if (k == 4) TA_CH(4); else TA_CH(5); } else { if (k == 6) TA_CH(6); else TA_CH(7); }
      }
#undef TA_CH
      dst = dst == lds0 + (NSLOT - 1) * SLOT ? lds0 : dst + SLOT;
    }
    wait_vm<32>(); flag_set(flags + F_LAND, 2 * NT - 2);
    wait_vm<16>(); flag_set(flags + F_LAND, 2 * NT - 1);
    wait_vm<0>(); flag_set(flags + F_LAND, 2 * NT);
    return;
  }

  const int w = wid;
  const int r = lane & 31, h = lane >> 5;
  if (w == 3) {
    dma2(make_srd((const char*)kt + (size_t)pair * 16 * ROWB, 16 * ROWB), voff, split * 4 * ROWB, lds0 + TK_OFF);
    dma2(make_srd((const char*)mask + (pair >> 3) * 512, 512), voff, 0, lds0 + MK_OFF);
  }
  if (w == 2) dma2(make_srd((const char*)vt + (size_t)pair * 16 * ROWB, 16 * ROWB), voff, split * 4 * ROWB, lds0 + TV_OFF);
  const int qrow = 2 * (r & 15) + (r >> 4);
  const int koff = (r & 15) * KP + (r >> 4) * 512 + h * 16;
  const int trofs = ((lane >> 4) & 1) * 128 + w * 32 + (lane & 3) * 8;
  const int vofs = (4 * h + ((lane & 15) >> 2)) * VP + trofs;
  const srd_t sZ = make_srd(ws + CTRL_BYTES + (size_t)pair * 64 * MSG, 64 * MSG);
  const int peer = (split + 1 + (lane & 3)) & 3;
  GAS u64* sig_in = (GAS u64*)(ws + ((pair * 4 + split) * 4 + w) * 128) + peer;
  GAS u64* sig_out = (GAS u64*)(ws + ((pair * 4 + peer) * 4 + w) * 128) + split;
  unsigned zin[3];
#pragma unroll
  for (int k = 0; k < 3; ++k) zin[k] = ((split * 4 + ((split + 1 + k) & 3)) * 4 + w) * MSG;
  if (w == 3) wait_vm<4>(); else if (w == 2) wait_vm<2>(); else wait_vm<0>();
  if (lane == 0) asm volatile("ds_add_u32 %0, %1" ::"v"(flags + F_Q), "v"(1u) : "memory");
  flag_wait(flags + F_Q, NW);
  STAMP(1);
  bf16x8 qf[16];
#pragma unroll
  for (int kk = 0; kk < 16; ++kk) qf[kk] = ld_frag((lds_cptr)lds + Q_OFF + koff + kk * 32);

  const lds_ptr refp = (lds_ptr)lds + REF_OFF + r * 4;
  float m = -INFINITY, l = 0.f;
  if (w == 3) {
    wait_vm<0>();
    const u32x2 mbits = *(const LAS u32x2*)((lds_cptr)lds + MK_OFF + (qrow & 15) * 32 + split * 8);
    const lds_cptr tk = (lds_cptr)lds + TK_OFF + (r & 3) * ROWB + h * 16;
    f32x16 s = f32x16{};
#pragma unroll
    for (int kk = 0; kk < 16; ++kk) s = MFMA32(ld_frag(tk + kk * 32), qf[kk], s);
#pragma unroll
    for (int i = 0; i < 4; ++i) {
      const float mk = __uint_as_float((i & 1) ? (mbits[i >> 1] & 0xffff0000u) : (mbits[i >> 1] << 16));
      s[i] = fmaf(s[i], SCALE, (1.0f - mk) * -1000000000.0f) * LOG2E;
      m = fmaxf(m, s[i]);
    }
#pragma unroll
    for (int i = 0; i < 4; ++i) { s[i] = __builtin_amdgcn_exp2f(s[i] - m); l += s[i]; }
    if (h) l = 0.f;
    *(LAS float*)refp = m;
    *(LAS u32x2*)((lds_ptr)lds + PT_OFF + r * 8) = u32x2{pack2(s[0], s[1]), pack2(s[2], s[3])};
    flag_set(flags + F_REF, 1);
  }
  STAMP(2);

  int slot_off = w * SLOT;
  const lds_ptr pbuf = (lds_ptr)lds + P_OFF + lane * 16;
  bf16x8 preg[NT / 8][2];

  auto k_tile = [&](int c, bf16x8& p0, bf16x8& p1) {
    flag_wait(flags + F_LAND, c + 1);
    const lds_cptr kfrag = (lds_cptr)lds + slot_off + koff;
    f32x16 s = f32x16{};
    bf16x8 kf[8];
#pragma unroll
    for (int kk = 0; kk < 8; ++kk) kf[kk] = ld_frag(kfrag + kk * 32);
    SBAR();
#pragma unroll
    for (int kk = 0; kk < 8; ++kk) { s = MFMA32(kf[kk], qf[kk], s); kf[kk] = ld_frag(kfrag + (kk + 8) * 32); SBAR(); }
    flag_set(flags + F_DONE + w * 4, min(c + NW, NT));
    slot_off = slot_off == 0 ? (NSLOT - 1) * SLOT : slot_off - SLOT;
    SBAR();
#pragma unroll
    for (int kk = 0; kk < 8; ++kk) s = MFMA32(kf[kk], qf[kk + 8], s);
    float tmax = fmaxf(s[0], s[1]);
#pragma unroll
    for (int i = 2; i < 16; ++i) tmax = fmaxf(tmax, s[i]);
    tmax = xhalf_max(tmax) * C2;
    PIN(tmax);
    flag_wait(flags + F_REF, c + 1);
    const float mn = fmaxf(*(const LAS float*)(refp + c * 128), tmax);
    *(LAS float*)(refp + (c + 1) * 128) = mn;
    flag_set(flags + F_REF, c + 2);
    const float alpha = __builtin_amdgcn_exp2f(m - mn);
    m = mn;
    float ps = 0.f;
#pragma unroll
    for (int i = 0; i < 16; ++i) { s[i] = __builtin_amdgcn_exp2f(fmaf(s[i], C2, -mn)); ps += s[i]; }
    l = fmaf(l, alpha, ps);
    p0 = pack8(s, 0); p1 = pack8(s, 8);
    PIN(l); PIN(m);
  };
#pragma unroll 1
  for (int c = w; c < NT / 2; c += NW) {
    bf16x8 p0, p1;
    k_tile(c, p0, p1);
    *(LAS bf16x8*)(pbuf + c * 2048) = p0;
    *(LAS bf16x8*)(pbuf + c * 2048 + 1024) = p1;
  }
  unsigned in0 = __hip_atomic_load((GAS unsigned*)sig_in, RLX_AGENT);
  unsigned out1 = __hip_atomic_load((GAS unsigned*)sig_out, RLX_AGENT) + 1;
#pragma unroll
  for (int t = 0; t < NT / 8; ++t) { k_tile(NT / 2 + 4 * t + w, preg[t][0], preg[t][1]); PIN(preg[t][0]); PIN(preg[t][1]); }
  wait_vm<0>();
  flag_set(flags + F_PRDY + w * 4, 1);
  flag_wait4(flags + F_PRDY, 1);
  PROBE(2);

  f32x16 o[2];
  float rcur = *(const LAS float*)refp;
  {
    const lds_cptr tv = (lds_cptr)lds + TV_OFF + ((lane & 15) >> 2) * ROWB + trofs;
    u32x2 pt2 = *(const LAS u32x2*)((lds_cptr)lds + PT_OFF + r * 8);
    if (h) pt2 = u32x2{0u, 0u};
    const bf16x8 pt = __builtin_bit_cast(bf16x8, u32x4{pt2[0], pt2[1], 0u, 0u});
#pragma unroll
    for (int cb = 0; cb < 2; ++cb) { const s16x4 v4 = ld_tr(tv + cb * 256); o[cb] = MFMA32(join(v4, v4), pt, f32x16{}); }
  }
  const float M = *(const LAS float*)(refp + NT * 128);
  *(LAS float*)((lds_ptr)lds + L_OFF + w * 128 + r * 4) = xhalf_sum(l) * __builtin_amdgcn_exp2f(m - M);
  slot_off = (NT % NSLOT) * SLOT;
  auto v_tiles = [&](int v0, int v1) {
#pragma unroll 1
    for (int v = v0; v < v1; ++v) {
      const float rnew = *(const LAS float*)(refp + (v + 1) * 128);
      bf16x8 p0 = ld_frag(pbuf + (v & (NT / 2 - 1)) * 2048), p1 = ld_frag(pbuf + (v & (NT / 2 - 1)) * 2048 + 1024);
      if (__any(rnew != rcur)) {
        const float alpha = __builtin_amdgcn_exp2f(rcur - rnew);
        o[0] *= alpha; o[1] *= alpha;
        rcur = rnew;
      }
      PIN(p0); PIN(p1);
      flag_wait(flags + F_LAND, NT + v + 1);
      const lds_cptr vfrag = (lds_cptr)lds + slot_off + vofs;
      bf16x8 vf[4];
#pragma unroll
      for (int i = 0; i < 4; ++i)
        vf[i] = join(ld_tr(vfrag + (i >> 1) * 512 + (i & 1) * 256), ld_tr(vfrag + 8 * VP + (i >> 1) * 512 + (i & 1) * 256));
      SBAR();
      o[0] = MFMA32(vf[0], p0, o[0]);
      o[1] = MFMA32(vf[1], p0, o[1]);
      SBAR();
      flag_set(flags + F_DONE + w * 4, NT + v + 1);
      slot_off = slot_off == (NSLOT - 1) * SLOT ? 0 : slot_off + SLOT;
      o[0] = MFMA32(vf[2], p1, o[0]);
      o[1] = MFMA32(vf[3], p1, o[1]);
    }
  };
  v_tiles(0, 20);
  flag_wait4(flags + F_DONE, NT + 16);
#pragma unroll
  for (int j = 0; j < 4; ++j) { *(LAS bf16x8*)(pbuf + (4 * j + w) * 2048) = preg[j][0]; *(LAS bf16x8*)(pbuf + (4 * j + w) * 2048 + 1024) = preg[j][1]; }
  flag_set(flags + F_PRDY + w * 4, 2);
  v_tiles(20, 32);
  flag_wait4(flags + F_PRDY, 2);
  float L = 0.f;
#pragma unroll
  for (int i = 0; i < NW; ++i) L += *(const LAS float*)((lds_cptr)lds + L_OFF + i * 128 + r * 4);
  v_tiles(32, 36);
  flag_wait4(flags + F_DONE, NT + 32);
#pragma unroll
  for (int j = 4; j < 8; ++j) { *(LAS bf16x8*)(pbuf + (4 * j + w) * 2048) = preg[j][0]; *(LAS bf16x8*)(pbuf + (4 * j + w) * 2048 + 1024) = preg[j][1]; }
  flag_set(flags + F_PRDY + w * 4, 3);
  v_tiles(36, 48);
  flag_wait4(flags + F_PRDY, 3);
  unsigned zout[4];
#pragma unroll
  for (int j = 0; j < 4; ++j) { zout[j] = (j == split ? 64 : (j * 4 + split) * 4 + w) * MSG + voff; PIN(zout[j]); }
  char* orow = (char*)out + ((size_t)pair * 32 + qrow) * ROWB + split * 128 + w * 32 + h * 16;
  PIN(orow);
  unsigned mlw = __float_as_uint(h ? L : M);
  PIN(mlw); PIN(in0); PIN(out1);
  v_tiles(48, NT);
  STAMP(3);
  PROBE(3);

#pragma unroll
  for (int j = 0; j < 4; ++j) {
    u32x4 x;
#pragma unroll
    for (int e = 0; e < 4; ++e) x[e] = pack2(o[j >> 1][8 * (j & 1) + 2 * e], o[j >> 1][8 * (j & 1) + 2 * e + 1]);
    __builtin_amdgcn_raw_buffer_store_b128(x, sZ, zout[j], 0, 16);
    __builtin_amdgcn_raw_buffer_store_b32(mlw, sZ, zout[j] + 1024 - 12 * lane, 0, 16);
  }
  unsigned g0 = __hip_atomic_load((GAS unsigned*)sig_in, RLX_AGENT), g1 = 0, g2 = 0;
  SBAR();
  float own[8];
#pragma unroll
  for (int e = 0; e < 8; ++e) {
    const float lo = (split & 1) ? o[0][8 + e] : o[0][e], hi = (split & 1) ? o[1][8 + e] : o[1][e];
    own[e] = (split & 2) ? hi : lo;
    PIN(own[e]);
  }
  wait_vm<0>();
  PIN(g0);
  STAMP(5);
  if (lane < 3) __hip_atomic_store(sig_out, ((u64)in0 << 32) | out1, RLX_AGENT);
  if (((unsigned)__ballot(g0 != in0) & 7u) != 7u) {
    unsigned left = 1u << 15, t;
#define TA_POLL(G) "s_waitcnt vmcnt(2)\n\tv_cmp_ne_u32 vcc, %[in0], " G "\n\ts_and_b32 %[t], vcc_lo, 7\n\ts_cmp_eq_u32 %[t], 7\n\t" \
                   "s_cbranch_scc1 2f\n\tglobal_load_dword " G ", %[p], off sc1\n\t"
    asm volatile(
        "global_load_dword %[g0], %[p], off sc1\n\ts_sleep 3\n\t"
        "global_load_dword %[g1], %[p], off sc1\n\ts_sleep 3\n\t"
        "global_load_dword %[g2], %[p], off sc1\n"
        "1:\n\t" TA_POLL("%[g0]") TA_POLL("%[g1]") TA_POLL("%[g2]")
        "s_sub_u32 %[left], %[left], 1\n\ts_cbranch_scc0 1b\n"
        "2:"
        : [g0] "=&v"(g0), [g1] "=&v"(g1), [g2] "=&v"(g2), [left] "+s"(left), [t] "=&s"(t)
        : [p] "v"(sig_in), [in0] "v"(in0)
        : "memory", "vcc", "scc");
#undef TA_POLL
  }
  __builtin_amdgcn_fence(__ATOMIC_ACQUIRE, "wavefront");
  asm volatile("" ::: "memory");
  u32x4 a[3];
  unsigned ml[3];
#pragma unroll
  for (int k = 0; k < 3; ++k) {
    ml[k] = __builtin_amdgcn_raw_buffer_load_b32(sZ, 1024 + lane * 4, zin[k], 16);
    a[k] = __builtin_amdgcn_raw_buffer_load_b128(sZ, voff, zin[k], 16);
  }
  const unsigned peer_view = __hip_atomic_load((GAS unsigned*)sig_in + 1, RLX_AGENT);
  STAMP(4);
  {
    float mk[3], lk[3], wk[3], G = M;
#pragma unroll
    for (int k = 0; k < 3; ++k) {
      auto x = __builtin_amdgcn_permlane32_swap(ml[k], ml[k], false, false);
      mk[k] = __uint_as_float(x[0]); lk[k] = __uint_as_float(x[1]);
      G = fmaxf(G, mk[k]);
    }
    const float w0 = __builtin_amdgcn_exp2f(M - G);
    float den = w0 * L;
#pragma unroll
    for (int k = 0; k < 3; ++k) { wk[k] = __builtin_amdgcn_exp2f(mk[k] - G); den = fmaf(wk[k], lk[k], den); }
    const float inv = 1.0f / den;
#pragma unroll
    for (int e = 0; e < 8; ++e) own[e] *= w0;
#pragma unroll
    for (int k = 0; k < 3; ++k)
#pragma unroll
      for (int e = 0; e < 4; ++e) {
        own[2 * e] = fmaf(wk[k], __uint_as_float(a[k][e] << 16), own[2 * e]);
        own[2 * e + 1] = fmaf(wk[k], __uint_as_float(a[k][e] & 0xffff0000u), own[2 * e + 1]);
      }
#pragma unroll
    for (int e = 0; e < 8; ++e) own[e] *= inv;
  }
  auto x = __builtin_amdgcn_permlane32_swap(pack2(own[0], own[1]), pack2(own[4], own[5]), false, false);
  auto y = __builtin_amdgcn_permlane32_swap(pack2(own[2], own[3]), pack2(own[6], own[7]), false, false);
  asm volatile("global_store_dwordx4 %0, %1, off sc1\n\ts_nop 1" ::"v"(orow), "v"(u32x4{x[0], y[0], x[1], y[1]}) : "memory");
  asm volatile("" ::"v"(g0), "v"(g1), "v"(g2), "v"(peer_view));
  if (lane < 3 && peer_view == out1) __hip_atomic_store(sig_out, ((u64)in0 << 32) | (out1 + 1), RLX_AGENT);
}

}

extern "C" void kernel_launch(void* const* d_in, const int* in_sizes, int n_in, void* d_out, int out_size, void* d_ws,
                              size_t ws_size, hipStream_t stream) {
  using namespace ta;
  (void)in_sizes; (void)n_in; (void)out_size; (void)ws_size;
  (void)hipFuncSetAttribute((const void*)tree_attn, hipFuncAttributeMaxDynamicSharedMemorySize, LDS_BYTES);
  hipLaunchKernelGGL(tree_attn, dim3(NPAIR * NSPLIT), dim3((NW + 1) * 64), LDS_BYTES, stream,
                     (const __hip_bfloat16*)d_in[0], (const __hip_bfloat16*)d_in[1], (const __hip_bfloat16*)d_in[2],
                     (const __hip_bfloat16*)d_in[3], (const __hip_bfloat16*)d_in[4], (const __hip_bfloat16*)d_in[5],
                     (char*)d_ws, (__hip_bfloat16*)d_out);
}
```

```cpp
#include <hip/hip_runtime.h>
#include <hip/hip_bf16.h>

#define TA_WIN 20
#define TA_WSTEP 2
#define TA_TAU 545
#define TA_OFF 1000
#define TA_LSTEP 700
#define TA_BOOST0 0
#define TA_BOOST1 0
#define TA_CODEPF 128
#define TA_KNEE 7
#define TA_KDIV 4
#define TA_POLL2 1
#define TA_SIGLINE 1
#define TA_OUTPOL 0
#define TA_RUNLEN 2
#define TA_KMAX 7

#define STAMP(k) do {} while (0)

namespace ta {

constexpr int NB = 8, HKV = 8, CTX = 8192, HD = 256;
constexpr int NPAIR = NB * HKV, NSPLIT = 4, NW = 4;
constexpr int ROWB = HD * 2;
constexpr int WG_BYTES = CTX / NSPLIT * ROWB;
constexpr int NT = CTX / NSPLIT / 32;
constexpr int KP = 1040, VP = 1056;
constexpr int SLOT = 16 * VP, NSLOT = 5;
constexpr int P_OFF = NSLOT * SLOT;
constexpr int REF_OFF = P_OFF + NT / 2 * 2048;
constexpr int TV_OFF = REF_OFF + (NT + 1) * 128;
constexpr int PT_OFF = TV_OFF + 4 * ROWB;
constexpr int L_OFF = PT_OFF + 256;
constexpr int FLAG_OFF = L_OFF + NW * 128;
constexpr int F_DONE = 0, F_PRDY = 16, F_LAND = 32, F_REF = 36, F_Q = 40;
constexpr int LDS_BYTES = FLAG_OFF + 48;
constexpr int TK_OFF = REF_OFF - 4 * ROWB, Q_OFF = TK_OFF - 16 * KP, MK_OFF = Q_OFF - 2048;
constexpr float SCALE = 0.0625f, LOG2E = 1.4426950408889634f, C2 = SCALE * LOG2E;
constexpr int WIN = TA_WIN;
constexpr int MSG = 1024 + 256;
constexpr int CTRL_BYTES = NPAIR * 64 * 128;
static_assert(LDS_BYTES <= 160 * 1024 && MK_OFF >= P_OFF + 16 * 2048 && FLAG_OFF % 16 == 0, "LDS map");

static_assert(WIN >= 17 && WIN + 7 * TA_WSTEP <= 48, "publish / drain ladder assumes 1..3 chunks in flight");

typedef __bf16 bf16x8 __attribute__((ext_vector_type(8)));
typedef __bf16 bf16x2 __attribute__((ext_vector_type(2)));
typedef short s16x4 __attribute__((ext_vector_type(4)));
typedef float f32x16 __attribute__((ext_vector_type(16)));
typedef float f32x4 __attribute__((ext_vector_type(4)));
typedef float f32x2 __attribute__((ext_vector_type(2)));
typedef unsigned u32x4 __attribute__((ext_vector_type(4)));
typedef unsigned u32x2 __attribute__((ext_vector_type(2)));
typedef unsigned long long u64;
#define LAS __attribute__((address_space(3)))
#define GAS __attribute__((address_space(1)))
typedef LAS const char* lds_cptr;
typedef LAS char* lds_ptr;
typedef __amdgpu_buffer_rsrc_t srd_t;

#define SBAR() __builtin_amdgcn_sched_barrier(0)
#define PIN(x) asm volatile("" : "+v"(x))
#define MFMA32(a, b, c) __builtin_amdgcn_mfma_f32_32x32x16_bf16((a), (b), (c), 0, 0, 0)
#define RLX_AGENT __ATOMIC_RELAXED, __HIP_MEMORY_SCOPE_AGENT
#define RFL(x) __builtin_amdgcn_readfirstlane(x)

__device__ __forceinline__ srd_t make_srd(const void* p, int bytes) {
  return __builtin_amdgcn_make_buffer_rsrc((void*)p, (short)0, bytes, 0x00020000);
}

template <int PAD, bool NTL>
__device__ __forceinline__ void dma4(srd_t srd, unsigned voff, unsigned soff, unsigned ldsb) {
  unsigned keep;
  if constexpr (NTL) {
  asm volatile(
      "s_mov_b32 %0, m0\n\ts_mov_b32 m0, %4\n\ts_nop 3\n\t"
      "buffer_load_dwordx4 %1, %2, %3 offen nt lds\n\t"
      "s_add_u32 m0, %4, %5\n\ts_nop 0\n\t"
      "buffer_load_dwordx4 %1, %2, %3 offen offset:1024 nt lds\n\t"
      "s_add_u32 m0, %4, %6\n\ts_nop 0\n\t"
      "buffer_load_dwordx4 %1, %2, %3 offen offset:2048 nt lds\n\t"
      "s_add_u32 m0, %4, %7\n\ts_nop 0\n\t"
      "buffer_load_dwordx4 %1, %2, %3 offen offset:3072 nt lds\n\t"
      "s_mov_b32 m0, %0"
      : "=&s"(keep)
      : "v"(voff), "s"(srd), "s"(soff), "s"(ldsb), "n"(PAD), "n"(2 * PAD), "n"(3 * PAD)
      : "memory", "scc");
  } else {
  asm volatile(
      "s_mov_b32 %0, m0\n\ts_mov_b32 m0, %4\n\ts_nop 3\n\t"
      "buffer_load_dwordx4 %1, %2, %3 offen lds\n\t"
      "s_add_u32 m0, %4, %5\n\ts_nop 0\n\t"
      "buffer_load_dwordx4 %1, %2, %3 offen offset:1024 lds\n\t"
      "s_add_u32 m0, %4, %6\n\ts_nop 0\n\t"
      "buffer_load_dwordx4 %1, %2, %3 offen offset:2048 lds\n\t"
      "s_add_u32 m0, %4, %7\n\ts_nop 0\n\t"
      "buffer_load_dwordx4 %1, %2, %3 offen offset:3072 lds\n\t"
      "s_mov_b32 m0, %0"
      : "=&s"(keep)
      : "v"(voff), "s"(srd), "s"(soff), "s"(ldsb), "n"(PAD), "n"(2 * PAD), "n"(3 * PAD)
      : "memory", "scc");
  }
}
#define TA_PIECE(I, OFS, STEP) \
      "s_waitcnt vmcnt(%[n])\n\t" \
      "buffer_load_dwordx4 %[vo], %[srd], %[so] offen " OFS " nt lds\n\t" \
      "s_add_u32 m0, m0, " STEP "\n\t" \
      ".if (%[n] & 15) == " #I "\n\tds_write_b32 %[la], %[lv]\n\t.endif\n\t"
#define TA_GROUP(I0, I1, I2, I3) \
      TA_PIECE(I0, "", "%[pad]") TA_PIECE(I1, "offset:1024", "%[pad]") TA_PIECE(I2, "offset:2048", "%[pad]") \
      TA_PIECE(I3, "offset:3072", "%[padg]") "s_add_u32 %[so], %[so], 0x1000\n\t"
template <int N>
__device__ __forceinline__ void dma_chunk(srd_t srd, unsigned voff, unsigned soff, unsigned ldsb, unsigned pad, unsigned land_addr,
                                          int c, unsigned done_addr, u32x4& done, u64& now) {
  unsigned keep;
  asm volatile(
      "s_mov_b32 %[keep], m0\n\ts_mov_b32 m0, %[lds]\n\ts_nop 2\n\t"
      TA_GROUP(0, 1, 2, 3) TA_GROUP(4, 5, 6, 7) TA_GROUP(8, 9, 10, 11)
      "ds_read_b128 %[done], %[da]\n\ts_memrealtime %[now]\n\t"
      TA_GROUP(12, 13, 14, 15)
      "s_mov_b32 m0, %[keep]\n\ts_waitcnt lgkmcnt(0)"
      : [keep] "=&s"(keep), [so] "+s"(soff), [done] "=&v"(done), [now] "=&s"(now)
      : [vo] "v"(voff), [srd] "s"(srd), [lds] "s"(ldsb), [pad] "s"(pad), [padg] "s"(pad + 4096), [n] "n"(N),
        [la] "v"(land_addr), [lv] "v"(c - N / 16), [da] "v"(done_addr)
      : "memory", "scc");
}
__device__ __forceinline__ void dma2(srd_t srd, unsigned voff, unsigned soff, unsigned ldsb) {
  unsigned keep;
  asm volatile("s_mov_b32 %0, m0\n\ts_mov_b32 m0, %4\n\ts_nop 3\n\t"
               "buffer_load_dwordx4 %1, %2, %3 offen lds\n\t"
               "buffer_load_dwordx4 %1, %2, %3 offen offset:1024 lds\n\t"
               "s_mov_b32 m0, %0"
               : "=&s"(keep) : "v"(voff), "s"(srd), "s"(soff), "s"(ldsb) : "memory");
}
template <int N> __device__ __forceinline__ void wait_vm() { asm volatile("s_waitcnt vmcnt(%0)" ::"n"(N) : "memory"); }

__device__ __forceinline__ bf16x8 ld_frag(lds_cptr p) { return *(const LAS bf16x8*)p; }
__device__ __forceinline__ s16x4 ld_tr(lds_cptr p) { return __builtin_amdgcn_ds_read_tr16_b64_v4i16((LAS s16x4*)p); }
__device__ __forceinline__ bf16x8 join(s16x4 lo, s16x4 hi) {
  return __builtin_bit_cast(bf16x8, __builtin_shufflevector(lo, hi, 0, 1, 2, 3, 4, 5, 6, 7));
}
__device__ __forceinline__ float xhalf_max(float x) {
  auto r = __builtin_amdgcn_permlane32_swap(__float_as_uint(x), __float_as_uint(x), false, false);
  return fmaxf(__uint_as_float(r[0]), __uint_as_float(r[1]));
}
__device__ __forceinline__ float xhalf_sum(float x) {
  auto r = __builtin_amdgcn_permlane32_swap(__float_as_uint(x), __float_as_uint(x), false, false);
  return __uint_as_float(r[0]) + __uint_as_float(r[1]);
}
__device__ __forceinline__ unsigned pack2(float a, float b) {
  const bf16x2 v = {(__bf16)a, (__bf16)b};
  return __builtin_bit_cast(unsigned, v);
}
__device__ __forceinline__ bf16x8 pack8(const f32x16& p, int o) {
  bf16x8 r;
#pragma unroll
  for (int j = 0; j < 8; ++j) r[j] = (__bf16)p[o + j];
  return r;
}
__device__ __forceinline__ void flag_set(unsigned addr, unsigned v) {
  asm volatile("s_waitcnt lgkmcnt(0)\n\tds_write_b32 %0, %1" ::"v"(addr), "v"(v) : "memory");
}
__device__ __forceinline__ void flag_wait(unsigned addr, int need) {
#pragma unroll 1
  for (int spin = 0; spin < (1 << 16); ++spin) {
    unsigned v;
    asm volatile("ds_read_b32 %0, %1\n\ts_waitcnt lgkmcnt(0)" : "=v"(v) : "v"(addr) : "memory");
    if ((int)RFL(v) >= need) break;
    __builtin_amdgcn_s_sleep(1);
  }
}
__device__ __forceinline__ void flag_wait4(unsigned addr, int need) {
#pragma unroll 1
  for (int spin = 0; spin < (1 << 16); ++spin) {
    u32x4 v;
    asm volatile("ds_read_b128 %0, %1\n\ts_waitcnt lgkmcnt(0)" : "=v"(v) : "v"(addr) : "memory");
    if ((int)RFL(min(min(v[0], v[1]), min(v[2], v[3]))) >= need) break;
    __builtin_amdgcn_s_sleep(1);
  }
}

__global__ __launch_bounds__((NW + 1) * 64, 2) void tree_attn(
    const __hip_bfloat16* __restrict__ q, const __hip_bfloat16* __restrict__ kc, const __hip_bfloat16* __restrict__ vc,
    const __hip_bfloat16* __restrict__ kt, const __hip_bfloat16* __restrict__ vt, const __hip_bfloat16* __restrict__ mask,
    char* ws, __hip_bfloat16* __restrict__ out) {
  extern __shared__ __attribute__((aligned(16))) char lds[];
  u64 kbase, base2, now, pc;
  unsigned d0, d1;
  const int wid = RFL(threadIdx.x >> 6);
  asm volatile("s_load_dwordx2 %[kb], %[ka], 0x8\n\t"
               "s_load_dwordx2 %[b2], %[ka], %[sel]\n\t"
               "s_getpc_b64 %[pc]\n\t"
               "s_load_dword %[d0], %[pc], %[line]\n\t"
               "s_load_dword %[d1], %[pc], %[line] offset:64\n\t"
               "s_memrealtime %[now]"
               : [kb] "=&s"(kbase), [b2] "=&s"(base2), [pc] "=&s"(pc), [d0] "=&s"(d0), [d1] "=&s"(d1), [now] "=&s"(now)
               : [ka] "s"(__builtin_amdgcn_kernarg_segment_ptr()), [sel] "s"((wid >> 2) * 8),
                 [line] "s"(((blockIdx.x >> 3) * (NW + 1) + wid) * TA_CODEPF)
               : "memory");
  SBAR();
  const unsigned bid = (blockIdx.x / (8 * TA_RUNLEN)) * (8 * TA_RUNLEN) + (blockIdx.x & 7) * TA_RUNLEN + ((blockIdx.x >> 3) & (TA_RUNLEN - 1));
  const int lane = threadIdx.x & 63;
  const unsigned lds0 = (unsigned)(uintptr_t)lds;
  const unsigned voff = lane * 16;
  const int pair = bid >> 2, split = bid & 3;
  {
    unsigned keep;
#define TA_G4(VO, BASE, POL) \
        "global_load_lds_dwordx4 " VO ", " BASE POL "\n\ts_add_u32 m0, m0, %[pad]\n\ts_nop 0\n\t" \
        "global_load_lds_dwordx4 " VO ", " BASE " offset:1024" POL "\n\ts_add_u32 m0, m0, %[pad]\n\ts_nop 0\n\t" \
        "global_load_lds_dwordx4 " VO ", " BASE " offset:2048" POL "\n\ts_add_u32 m0, m0, %[pad]\n\ts_nop 0\n\t" \
        "global_load_lds_dwordx4 " VO ", " BASE " offset:3072" POL "\n\t"
    asm volatile(
        "s_mov_b32 %[keep], m0\n\ts_mov_b32 m0, %[dst2]\n\t"
        "s_waitcnt lgkmcnt(0)\n\t"
        TA_G4("%[vo2]", "%[b2]", "")
        "s_mov_b32 m0, %[dst]\n\ts_nop 0\n\t"
        TA_G4("%[vo]", "%[kb]", " nt")
        "s_mov_b32 m0, %[keep]"
        : [keep] "=&s"(keep), [kb] "+s"(kbase), [b2] "+s"(base2), [now] "+s"(now), [d0] "+s"(d0), [d1] "+s"(d1)
        : [vo] "v"(bid * WG_BYTES + wid * 4096 + voff),
          [dst] "s"(lds0 + wid * 4 * KP + (wid >> 2) * (SLOT - 16 * KP)),
          [vo2] "v"((wid == NW ? bid * WG_BYTES + 5 * 4096 : pair * 32 * ROWB + wid * 4096) + voff),
          [dst2] "s"(lds0 + (wid == NW ? SLOT + 4 * KP : Q_OFF + wid * 4 * KP)), [pad] "n"(KP - 1024)
        : "memory", "scc");
#undef TA_G4
  }
#define PROBE(k) do {} while (0)
  const unsigned flags = lds0 + FLAG_OFF;
  if (threadIdx.x < 12) flag_set(flags + lane * 4, lane < 4 ? lane : lane == F_LAND / 4 ? 1 : 0);
  asm volatile("s_waitcnt lgkmcnt(0)\n\ts_barrier" ::: "memory");

  if (wid == NW) {
    __builtin_amdgcn_s_setprio(3);
    const srd_t sK = make_srd((const char*)kbase + (size_t)bid * WG_BYTES, WG_BYTES);
#pragma unroll 1
    for (int g = 2; g < 4; ++g) dma4<KP - 1024, true>(sK, voff, 16384 + g * 4096, lds0 + SLOT + g * 4 * KP);
    const srd_t sV = make_srd((const char*)vc + (size_t)bid * WG_BYTES, WG_BYTES);
    const unsigned t0 = (unsigned)now + TA_OFF / 10;
    u32x4 done = {0u, 0u, 0u, 0u};
    unsigned dst = lds0 + 2 * SLOT;
#pragma unroll 1
    for (int c = 2; c < 2 * NT; ++c) {
      if (c >= NSLOT && (int)RFL(min(min(done[0], done[1]), min(done[2], done[3]))) < c - NSLOT + 1)
        flag_wait4(flags + F_DONE, c - NSLOT + 1);
      const int lag = (int)(RFL((unsigned)now) - t0) - (((4 * c - 1) * (TA_TAU * 64 / 10)) >> 8);
      int k = lag <= 0 ? 0 : (lag * (655360 / TA_LSTEP)) >> 16;
      if (k > TA_KNEE) k = min(TA_KNEE + (k - TA_KNEE) / TA_KDIV, TA_KMAX);
      if (c < TA_BOOST0 || c >= 2 * NT - TA_BOOST1) k = 7;
      const srd_t srd = c < NT ? sK : sV;
      const unsigned soff = (c & (NT - 1)) * 16384, pad = c < NT ? KP - 1024 : VP - 1024;
#define TA_CH(K) dma_chunk<WIN - 1 + (K) * TA_WSTEP>(srd, voff, soff, dst, pad, flags + F_LAND, c, flags + F_DONE, done, now)
      if (k < 4) {
        if (k < 2) { if (k == 0) TA_CH(0); else TA_CH(1); } else { if (k == 2) TA_CH(2); else TA_CH(3); }
      } else {
        if (k < 6) { if (k == 4) TA_CH(4); else TA_CH(5); } else { if (k == 6) TA_CH(6); else TA_CH(7); }
      }
#undef TA_CH
      dst = dst == lds0 + (NSLOT - 1) * SLOT ? lds0 : dst + SLOT;
    }
    wait_vm<32>(); flag_set(flags + F_LAND, 2 * NT - 2);
    wait_vm<16>(); flag_set(flags + F_LAND, 2 * NT - 1);
    wait_vm<0>(); flag_set(flags + F_LAND, 2 * NT);
    return;
  }

  const int w = wid;
  const int r = lane & 31, h = lane >> 5;
  if (w == 3) {
    dma2(make_srd((const char*)kt + (size_t)pair * 16 * ROWB, 16 * ROWB), voff, split * 4 * ROWB, lds0 + TK_OFF);
    dma2(make_srd((const char*)mask + (pair >> 3) * 512, 512), voff, 0, lds0 + MK_OFF);
  }
  if (w == 2) dma2(make_srd((const char*)vt + (size_t)pair * 16 * ROWB, 16 * ROWB), voff, split * 4 * ROWB, lds0 + TV_OFF);
  const int qrow = 2 * (r & 15) + (r >> 4);
  const int koff = (r & 15) * KP + (r >> 4) * 512 + h * 16;
  const int trofs = ((lane >> 4) & 1) * 128 + w * 32 + (lane & 3) * 8;
  const int vofs = (4 * h + ((lane & 15) >> 2)) * VP + trofs;
  const srd_t sZ = make_srd(ws + CTRL_BYTES + (size_t)pair * 64 * MSG, 64 * MSG);
  const int peer = (split + 1 + (lane & 3)) & 3;
  GAS u64* sig_in = (GAS u64*)(ws + ((pair * 4 + split) * 4 + w) * 128) + peer;
  GAS u64* sig_out = (GAS u64*)(ws + ((pair * 4 + peer) * 4 + w) * 128) + split;
  unsigned zin[3];
#pragma unroll
  for (int k = 0; k < 3; ++k) zin[k] = ((split * 4 + ((split + 1 + k) & 3)) * 4 + w) * MSG;
  if (w == 3) wait_vm<4>(); else if (w == 2) wait_vm<2>(); else wait_vm<0>();
  if (lane == 0) asm volatile("ds_add_u32 %0, %1" ::"v"(flags + F_Q), "v"(1u) : "memory");
  flag_wait(flags + F_Q, NW);
  STAMP(1);
  bf16x8 qf[16];
#pragma unroll
  for (int kk = 0; kk < 16; ++kk) qf[kk] = ld_frag((lds_cptr)lds + Q_OFF + koff + kk * 32);

  const lds_ptr refp = (lds_ptr)lds + REF_OFF + r * 4;
  float m = -INFINITY, l = 0.f;
  if (w == 3) {
    wait_vm<0>();
    const u32x2 mbits = *(const LAS u32x2*)((lds_cptr)lds + MK_OFF + (qrow & 15) * 32 + split * 8);
    const lds_cptr tk = (lds_cptr)lds + TK_OFF + (r & 3) * ROWB + h * 16;
    f32x16 s = f32x16{};
#pragma unroll
    for (int kk = 0; kk < 16; ++kk) s = MFMA32(ld_frag(tk + kk * 32), qf[kk], s);
#pragma unroll
    for (int i = 0; i < 4; ++i) {
      const float mk = __uint_as_float((i & 1) ? (mbits[i >> 1] & 0xffff0000u) : (mbits[i >> 1] << 16));
      s[i] = fmaf(s[i], SCALE, (1.0f - mk) * -1000000000.0f) * LOG2E;
      m = fmaxf(m, s[i]);
    }
#pragma unroll
    for (int i = 0; i < 4; ++i) { s[i] = __builtin_amdgcn_exp2f(s[i] - m); l += s[i]; }
    if (h) l = 0.f;
    *(LAS float*)refp = m;
    *(LAS u32x2*)((lds_ptr)lds + PT_OFF + r * 8) = u32x2{pack2(s[0], s[1]), pack2(s[2], s[3])};
    flag_set(flags + F_REF, 1);
  }
  STAMP(2);

  int slot_off = w * SLOT;
  const lds_ptr pbuf = (lds_ptr)lds + P_OFF + lane * 16;
  bf16x8 preg[NT / 8][2];

  auto k_tile = [&](int c, bf16x8& p0, bf16x8& p1) {
    flag_wait(flags + F_LAND, c + 1);
    const lds_cptr kfrag = (lds_cptr)lds + slot_off + koff;
    f32x16 s = f32x16{};
    bf16x8 kf[8];
#pragma unroll
    for (int kk = 0; kk < 8; ++kk) kf[kk] = ld_frag(kfrag + kk * 32);
    SBAR();
#pragma unroll
    for (int kk = 0; kk < 8; ++kk) { s = MFMA32(kf[kk], qf[kk], s); kf[kk] = ld_frag(kfrag + (kk + 8) * 32); SBAR(); }
    flag_set(flags + F_DONE + w * 4, min(c + NW, NT));
    slot_off = slot_off == 0 ? (NSLOT - 1) * SLOT : slot_off - SLOT;
    SBAR();
#pragma unroll
    for (int kk = 0; kk < 8; ++kk) s = MFMA32(kf[kk], qf[kk + 8], s);
    float tmax = fmaxf(s[0], s[1]);
#pragma unroll
    for (int i = 2; i < 16; ++i) tmax = fmaxf(tmax, s[i]);
    tmax = xhalf_max(tmax) * C2;
    PIN(tmax);
    flag_wait(flags + F_REF, c + 1);
    const float mn = fmaxf(*(const LAS float*)(refp + c * 128), tmax);
    *(LAS float*)(refp + (c + 1) * 128) = mn;
    flag_set(flags + F_REF, c + 2);
    const float alpha = __builtin_amdgcn_exp2f(m - mn);
    m = mn;
    float ps = 0.f;
#pragma unroll
    for (int i = 0; i < 16; ++i) { s[i] = __builtin_amdgcn_exp2f(fmaf(s[i], C2, -mn)); ps += s[i]; }
    l = fmaf(l, alpha, ps);
    p0 = pack8(s, 0); p1 = pack8(s, 8);
    PIN(l); PIN(m);
  };
#pragma unroll 1
  for (int c = w; c < NT / 2; c += NW) {
    bf16x8 p0, p1;
    k_tile(c, p0, p1);
    *(LAS bf16x8*)(pbuf + c * 2048) = p0;
    *(LAS bf16x8*)(pbuf + c * 2048 + 1024) = p1;
  }
  unsigned in0 = __hip_atomic_load((GAS unsigned*)sig_in, RLX_AGENT);
  unsigned out1 = __hip_atomic_load((GAS unsigned*)sig_out, RLX_AGENT) + 1;
#pragma unroll
  for (int t = 0; t < NT / 8; ++t) { k_tile(NT / 2 + 4 * t + w, preg[t][0], preg[t][1]); PIN(preg[t][0]); PIN(preg[t][1]); }
  wait_vm<0>();
  flag_set(flags + F_PRDY + w * 4, 1);
  flag_wait4(flags + F_PRDY, 1);
  PROBE(2);

  f32x16 o[2];
  float rcur = *(const LAS float*)refp;
  {
    const lds_cptr tv = (lds_cptr)lds + TV_OFF + ((lane & 15) >> 2) * ROWB + trofs;
    u32x2 pt2 = *(const LAS u32x2*)((lds_cptr)lds + PT_OFF + r * 8);
    if (h) pt2 = u32x2{0u, 0u};
    const bf16x8 pt = __builtin_bit_cast(bf16x8, u32x4{pt2[0], pt2[1], 0u, 0u});
#pragma unroll
    for (int cb = 0; cb < 2; ++cb) { const s16x4 v4 = ld_tr(tv + cb * 256); o[cb] = MFMA32(join(v4, v4), pt, f32x16{}); }
  }
  const float M = *(const LAS float*)(refp + NT * 128);
  *(LAS float*)((lds_ptr)lds + L_OFF + w * 128 + r * 4) = xhalf_sum(l) * __builtin_amdgcn_exp2f(m - M);
  slot_off = (NT % NSLOT) * SLOT;
  auto v_tiles = [&](int v0, int v1) {
#pragma unroll 1
    for (int v = v0; v < v1; ++v) {
      const float rnew = *(const LAS float*)(refp + (v + 1) * 128);
      bf16x8 p0 = ld_frag(pbuf + (v & (NT / 2 - 1)) * 2048), p1 = ld_frag(pbuf + (v & (NT / 2 - 1)) * 2048 + 1024);
      if (__any(rnew != rcur)) {
        const float alpha = __builtin_amdgcn_exp2f(rcur - rnew);
        o[0] *= alpha; o[1] *= alpha;
        rcur = rnew;
      }
      PIN(p0); PIN(p1);
      flag_wait(flags + F_LAND, NT + v + 1);
      const lds_cptr vfrag = (lds_cptr)lds + slot_off + vofs;
      bf16x8 vf[4];
#pragma unroll
      for (int i = 0; i < 4; ++i)
        vf[i] = join(ld_tr(vfrag + (i >> 1) * 512 + (i & 1) * 256), ld_tr(vfrag + 8 * VP + (i >> 1) * 512 + (i & 1) * 256));
      SBAR();
      o[0] = MFMA32(vf[0], p0, o[0]);
      o[1] = MFMA32(vf[1], p0, o[1]);
      SBAR();
      flag_set(flags + F_DONE + w * 4, NT + v + 1);
      slot_off = slot_off == (NSLOT - 1) * SLOT ? 0 : slot_off + SLOT;
      o[0] = MFMA32(vf[2], p1, o[0]);
      o[1] = MFMA32(vf[3], p1, o[1]);
    }
  };
  v_tiles(0, 20);
  flag_wait4(flags + F_DONE, NT + 16);
#pragma unroll
  for (int j = 0; j < 4; ++j) { *(LAS bf16x8*)(pbuf + (4 * j + w) * 2048) = preg[j][0]; *(LAS bf16x8*)(pbuf + (4 * j + w) * 2048 + 1024) = preg[j][1]; }
  flag_set(flags + F_PRDY + w * 4, 2);
  v_tiles(20, 32);
  flag_wait4(flags + F_PRDY, 2);
  float L = 0.f;
#pragma unroll
  for (int i = 0; i < NW; ++i) L += *(const LAS float*)((lds_cptr)lds + L_OFF + i * 128 + r * 4);
  v_tiles(32, 36);
  flag_wait4(flags + F_DONE, NT + 32);
#pragma unroll
  for (int j = 4; j < 8; ++j) { *(LAS bf16x8*)(pbuf + (4 * j + w) * 2048) = preg[j][0]; *(LAS bf16x8*)(pbuf + (4 * j + w) * 2048 + 1024) = preg[j][1]; }
  flag_set(flags + F_PRDY + w * 4, 3);
  v_tiles(36, 48);
  flag_wait4(flags + F_PRDY, 3);
  unsigned zout[4];
#pragma unroll
  for (int j = 0; j < 4; ++j) { zout[j] = (j == split ? 64 : (j * 4 + split) * 4 + w) * MSG + voff; PIN(zout[j]); }
  char* orow = (char*)out + ((size_t)pair * 32 + qrow) * ROWB + split * 128 + w * 32 + h * 16;
  PIN(orow);
  unsigned mlw = __float_as_uint(h ? L : M);
  PIN(mlw); PIN(in0); PIN(out1);
  v_tiles(48, NT);
  STAMP(3);
  PROBE(3);

#pragma unroll
  for (int j = 0; j < 4; ++j) {
    u32x4 x;
#pragma unroll
    for (int e = 0; e < 4; ++e) x[e] = pack2(o[j >> 1][8 * (j & 1) + 2 * e], o[j >> 1][8 * (j & 1) + 2 * e + 1]);
    __builtin_amdgcn_raw_buffer_store_b128(x, sZ, zout[j], 0, 16);
    __builtin_amdgcn_raw_buffer_store_b32(mlw, sZ, zout[j] + 1024 - 12 * lane, 0, 16);
  }
  unsigned g0 = __hip_atomic_load((GAS unsigned*)sig_in, RLX_AGENT), g1 = 0, g2 = 0;
  SBAR();
  float own[8];
#pragma unroll
  for (int e = 0; e < 8; ++e) {
    const float lo = (split & 1) ? o[0][8 + e] : o[0][e], hi = (split & 1) ? o[1][8 + e] : o[1][e];
    own[e] = (split & 2) ? hi : lo;
    PIN(own[e]);
  }
  wait_vm<0>();
  PIN(g0);
  STAMP(5);
  if (lane < 3) __hip_atomic_store(sig_out, ((u64)in0 << 32) | out1, RLX_AGENT);
  if (((unsigned)__ballot(g0 != in0) & 7u) != 7u) {
    unsigned left = 1u << 15, t;
#define TA_POLL(G) "s_waitcnt vmcnt(2)\n\tv_cmp_ne_u32 vcc, %[in0], " G "\n\ts_and_b32 %[t], vcc_lo, 7\n\ts_cmp_eq_u32 %[t], 7\n\t" \
                   "s_cbranch_scc1 2f\n\tglobal_load_dword " G ", %[p], off sc1\n\t"
    asm volatile(
        "global_load_dword %[g0], %[p], off sc1\n\ts_sleep 3\n\t"
        "global_load_dword %[g1], %[p], off sc1\n\ts_sleep 3\n\t"
        "global_load_dword %[g2], %[p], off sc1\n"
        "1:\n\t" TA_POLL("%[g0]") TA_POLL("%[g1]") TA_POLL("%[g2]")
        "s_sub_u32 %[left], %[left], 1\n\ts_cbranch_scc0 1b\n"
        "2:"
        : [g0] "=&v"(g0), [g1] "=&v"(g1), [g2] "=&v"(g2), [left] "+s"(left), [t] "=&s"(t)
        : [p] "v"(sig_in), [in0] "v"(in0)
        : "memory", "vcc", "scc");
#undef TA_POLL
  }
  __builtin_amdgcn_fence(__ATOMIC_ACQUIRE, "wavefront");
  asm volatile("" ::: "memory");
  u32x4 a[3];
  unsigned ml[3];
#pragma unroll
  for (int k = 0; k < 3; ++k) {
    ml[k] = __builtin_amdgcn_raw_buffer_load_b32(sZ, 1024 + lane * 4, zin[k], 16);
    a[k] = __builtin_amdgcn_raw_buffer_load_b128(sZ, voff, zin[k], 16);
  }
  const unsigned peer_view = __hip_atomic_load((GAS unsigned*)sig_in + 1, RLX_AGENT);
  STAMP(4);
  {
    float mk[3], lk[3], wk[3], G = M;
#pragma unroll
    for (int k = 0; k < 3; ++k) {
      auto x = __builtin_amdgcn_permlane32_swap(ml[k], ml[k], false, false);
      mk[k] = __uint_as_float(x[0]); lk[k] = __uint_as_float(x[1]);
      G = fmaxf(G, mk[k]);
    }
    const float w0 = __builtin_amdgcn_exp2f(M - G);
    float den = w0 * L;
#pragma unroll
    for (int k = 0; k < 3; ++k) { wk[k] = __builtin_amdgcn_exp2f(mk[k] - G); den = fmaf(wk[k], lk[k], den); }
    const float inv = 1.0f / den;
#pragma unroll
    for (int e = 0; e < 8; ++e) own[e] *= w0;
#pragma unroll
    for (int k = 0; k < 3; ++k)
#pragma unroll
      for (int e = 0; e < 4; ++e) {
        own[2 * e] = fmaf(wk[k], __uint_as_float(a[k][e] << 16), own[2 * e]);
        own[2 * e + 1] = fmaf(wk[k], __uint_as_float(a[k][e] & 0xffff0000u), own[2 * e + 1]);
      }
#pragma unroll
    for (int e = 0; e < 8; ++e) own[e] *= inv;
  }
  auto x = __builtin_amdgcn_permlane32_swap(pack2(own[0], own[1]), pack2(own[4], own[5]), false, false);
  auto y = __builtin_amdgcn_permlane32_swap(pack2(own[2], own[3]), pack2(own[6], own[7]), false, false);
  *(GAS u32x4*)orow = u32x4{x[0], y[0], x[1], y[1]};
  asm volatile("" ::"v"(g0), "v"(g1), "v"(g2), "v"(peer_view));
  if (lane < 3 && peer_view == out1) __hip_atomic_store(sig_out, ((u64)in0 << 32) | (out1 + 1), RLX_AGENT);
}

}

extern "C" void kernel_launch(void* const* d_in, const int* in_sizes, int n_in, void* d_out, int out_size, void* d_ws,
                              size_t ws_size, hipStream_t stream) {
  using namespace ta;
  (void)in_sizes; (void)n_in; (void)out_size; (void)ws_size;
  (void)hipFuncSetAttribute((const void*)tree_attn, hipFuncAttributeMaxDynamicSharedMemorySize, LDS_BYTES);
  hipLaunchKernelGGL(tree_attn, dim3(NPAIR * NSPLIT), dim3((NW + 1) * 64), LDS_BYTES, stream,
                     (const __hip_bfloat16*)d_in[0], (const __hip_bfloat16*)d_in[1], (const __hip_bfloat16*)d_in[2],
                     (const __hip_bfloat16*)d_in[3], (const __hip_bfloat16*)d_in[4], (const __hip_bfloat16*)d_in[5],
                     (char*)d_ws, (__hip_bfloat16*)d_out);
}
```

```cpp
#include <hip/hip_runtime.h>
#include <hip/hip_bf16.h>

#define TA_WIN 20
#define TA_WSTEP 2
#define TA_TAU 545
#define TA_OFF 1000
#define TA_LSTEP 700
#define TA_BOOST0 0
#define TA_BOOST1 0
#define TA_CODEPF 128
#define TA_KNEE 4
#define TA_KDIV 2
#define TA_POLL2 0
#define TA_SIGLINE 1
#define TA_OUTPOL 0
#define TA_RUNLEN 8
#define TA_PRETOUCH 1
#define TA_PSLEEP 0
#define TA_SIGSTR 128
#define TA_KMAX 7

#define STAMP(k) do {} while (0)

namespace ta {

constexpr int NB = 8, HKV = 8, CTX = 8192, HD = 256;
constexpr int NPAIR = NB * HKV, NSPLIT = 4, NW = 4;
constexpr int ROWB = HD * 2;
constexpr int WG_BYTES = CTX / NSPLIT * ROWB;
constexpr int NT = CTX / NSPLIT / 32;
constexpr int KP = 1040, VP = 1056;
constexpr int SLOT = 16 * VP, NSLOT = 5;
constexpr int P_OFF = NSLOT * SLOT;
constexpr int REF_OFF = P_OFF + NT / 2 * 2048;
constexpr int TV_OFF = REF_OFF + (NT + 1) * 128;
constexpr int PT_OFF = TV_OFF + 4 * ROWB;
constexpr int L_OFF = PT_OFF + 256;
constexpr int FLAG_OFF = L_OFF + NW * 128;
constexpr int F_DONE = 0, F_PRDY = 16, F_LAND = 32, F_REF = 36, F_Q = 40;
constexpr int LDS_BYTES = FLAG_OFF + 48;
constexpr int TK_OFF = REF_OFF - 4 * ROWB, Q_OFF = TK_OFF - 16 * KP, MK_OFF = Q_OFF - 2048;
constexpr float SCALE = 0.0625f, LOG2E = 1.4426950408889634f, C2 = SCALE * LOG2E;
constexpr int WIN = TA_WIN;
constexpr int MSG = 1024 + 256;
constexpr int CTRL_BYTES = NPAIR * 64 * 128;
static_assert(LDS_BYTES <= 160 * 1024 && MK_OFF >= P_OFF + 16 * 2048 && FLAG_OFF % 16 == 0, "LDS map");

static_assert(WIN >= 17 && WIN + 7 * TA_WSTEP <= 48, "publish / drain ladder assumes 1..3 chunks in flight");

typedef __bf16 bf16x8 __attribute__((ext_vector_type(8)));
typedef __bf16 bf16x2 __attribute__((ext_vector_type(2)));
typedef short s16x4 __attribute__((ext_vector_type(4)));
typedef float f32x16 __attribute__((ext_vector_type(16)));
typedef float f32x4 __attribute__((ext_vector_type(4)));
typedef float f32x2 __attribute__((ext_vector_type(2)));
typedef unsigned u32x4 __attribute__((ext_vector_type(4)));
typedef unsigned u32x2 __attribute__((ext_vector_type(2)));
typedef unsigned long long u64;
#define LAS __attribute__((address_space(3)))
#define GAS __attribute__((address_space(1)))
typedef LAS const char* lds_cptr;
typedef LAS char* lds_ptr;
typedef __amdgpu_buffer_rsrc_t srd_t;

#define SBAR() __builtin_amdgcn_sched_barrier(0)
#define PIN(x) asm volatile("" : "+v"(x))
#define MFMA32(a, b, c) __builtin_amdgcn_mfma_f32_32x32x16_bf16((a), (b), (c), 0, 0, 0)
#define RLX_AGENT __ATOMIC_RELAXED, __HIP_MEMORY_SCOPE_AGENT
#define RFL(x) __builtin_amdgcn_readfirstlane(x)

__device__ __forceinline__ srd_t make_srd(const void* p, int bytes) {
  return __builtin_amdgcn_make_buffer_rsrc((void*)p, (short)0, bytes, 0x00020000);
}

template <int PAD, bool NTL>
__device__ __forceinline__ void dma4(srd_t srd, unsigned voff, unsigned soff, unsigned ldsb) {
  unsigned keep;
  if constexpr (NTL) {
  asm volatile(
      "s_mov_b32 %0, m0\n\ts_mov_b32 m0, %4\n\ts_nop 3\n\t"
      "buffer_load_dwordx4 %1, %2, %3 offen nt lds\n\t"
      "s_add_u32 m0, %4, %5\n\ts_nop 0\n\t"
      "buffer_load_dwordx4 %1, %2, %3 offen offset:1024 nt lds\n\t"
      "s_add_u32 m0, %4, %6\n\ts_nop 0\n\t"
      "buffer_load_dwordx4 %1, %2, %3 offen offset:2048 nt lds\n\t"
      "s_add_u32 m0, %4, %7\n\ts_nop 0\n\t"
      "buffer_load_dwordx4 %1, %2, %3 offen offset:3072 nt lds\n\t"
      "s_mov_b32 m0, %0"
      : "=&s"(keep)
      : "v"(voff), "s"(srd), "s"(soff), "s"(ldsb), "n"(PAD), "n"(2 * PAD), "n"(3 * PAD)
      : "memory", "scc");
  } else {
  asm volatile(
      "s_mov_b32 %0, m0\n\ts_mov_b32 m0, %4\n\ts_nop 3\n\t"
      "buffer_load_dwordx4 %1, %2, %3 offen lds\n\t"
      "s_add_u32 m0, %4, %5\n\ts_nop 0\n\t"
      "buffer_load_dwordx4 %1, %2, %3 offen offset:1024 lds\n\t"
      "s_add_u32 m0, %4, %6\n\ts_nop 0\n\t"
      "buffer_load_dwordx4 %1, %2, %3 offen offset:2048 lds\n\t"
      "s_add_u32 m0, %4, %7\n\ts_nop 0\n\t"
      "buffer_load_dwordx4 %1, %2, %3 offen offset:3072 lds\n\t"
      "s_mov_b32 m0, %0"
      : "=&s"(keep)
      : "v"(voff), "s"(srd), "s"(soff), "s"(ldsb), "n"(PAD), "n"(2 * PAD), "n"(3 * PAD)
      : "memory", "scc");
  }
}
#define TA_PIECE(I, OFS, STEP) \
      "s_waitcnt vmcnt(%[n])\n\t" \
      "buffer_load_dwordx4 %[vo], %[srd], %[so] offen " OFS " nt lds\n\t" \
      "s_add_u32 m0, m0, " STEP "\n\t" \
      ".if (%[n] & 15) == " #I "\n\tds_write_b32 %[la], %[lv]\n\t.endif\n\t"
#define TA_GROUP(I0, I1, I2, I3) \
      TA_PIECE(I0, "", "%[pad]") TA_PIECE(I1, "offset:1024", "%[pad]") TA_PIECE(I2, "offset:2048", "%[pad]") \
      TA_PIECE(I3, "offset:3072", "%[padg]") "s_add_u32 %[so], %[so], 0x1000\n\t"
template <int N>
__device__ __forceinline__ void dma_chunk(srd_t srd, unsigned voff, unsigned soff, unsigned ldsb, unsigned pad, unsigned land_addr,
                                          int c, unsigned done_addr, u32x4& done, u64& now) {
  unsigned keep;
  asm volatile(
      "s_mov_b32 %[keep], m0\n\ts_mov_b32 m0, %[lds]\n\ts_nop 2\n\t"
      TA_GROUP(0, 1, 2, 3) TA_GROUP(4, 5, 6, 7) TA_GROUP(8, 9, 10, 11)
      "ds_read_b128 %[done], %[da]\n\ts_memrealtime %[now]\n\t"
      TA_GROUP(12, 13, 14, 15)
      "s_mov_b32 m0, %[keep]\n\ts_waitcnt lgkmcnt(0)"
      : [keep] "=&s"(keep), [so] "+s"(soff), [done] "=&v"(done), [now] "=&s"(now)
      : [vo] "v"(voff), [srd] "s"(srd), [lds] "s"(ldsb), [pad] "s"(pad), [padg] "s"(pad + 4096), [n] "n"(N),
        [la] "v"(land_addr), [lv] "v"(c - N / 16), [da] "v"(done_addr)
      : "memory", "scc");
}
__device__ __forceinline__ void dma2(srd_t srd, unsigned voff, unsigned soff, unsigned ldsb) {
  unsigned keep;
  asm volatile("s_mov_b32 %0, m0\n\ts_mov_b32 m0, %4\n\ts_nop 3\n\t"
               "buffer_load_dwordx4 %1, %2, %3 offen lds\n\t"
               "buffer_load_dwordx4 %1, %2, %3 offen offset:1024 lds\n\t"
               "s_mov_b32 m0, %0"
               : "=&s"(keep) : "v"(voff), "s"(srd), "s"(soff), "s"(ldsb) : "memory");
}
template <int N> __device__ __forceinline__ void wait_vm() { asm volatile("s_waitcnt vmcnt(%0)" ::"n"(N) : "memory"); }

__device__ __forceinline__ bf16x8 ld_frag(lds_cptr p) { return *(const LAS bf16x8*)p; }
__device__ __forceinline__ s16x4 ld_tr(lds_cptr p) { return __builtin_amdgcn_ds_read_tr16_b64_v4i16((LAS s16x4*)p); }
__device__ __forceinline__ bf16x8 join(s16x4 lo, s16x4 hi) {
  return __builtin_bit_cast(bf16x8, __builtin_shufflevector(lo, hi, 0, 1, 2, 3, 4, 5, 6, 7));
}
__device__ __forceinline__ float xhalf_max(float x) {
  auto r = __builtin_amdgcn_permlane32_swap(__float_as_uint(x), __float_as_uint(x), false, false);
  return fmaxf(__uint_as_float(r[0]), __uint_as_float(r[1]));
}
__device__ __forceinline__ float xhalf_sum(float x) {
  auto r = __builtin_amdgcn_permlane32_swap(__float_as_uint(x), __float_as_uint(x), false, false);
  return __uint_as_float(r[0]) + __uint_as_float(r[1]);
}
__device__ __forceinline__ unsigned pack2(float a, float b) {
  const bf16x2 v = {(__bf16)a, (__bf16)b};
  return __builtin_bit_cast(unsigned, v);
}
__device__ __forceinline__ bf16x8 pack8(const f32x16& p, int o) {
  bf16x8 r;
#pragma unroll
  for (int j = 0; j < 8; ++j) r[j] = (__bf16)p[o + j];
  return r;
}
__device__ __forceinline__ void flag_set(unsigned addr, unsigned v) {
  asm volatile("s_waitcnt lgkmcnt(0)\n\tds_write_b32 %0, %1" ::"v"(addr), "v"(v) : "memory");
}
__device__ __forceinline__ void flag_wait(unsigned addr, int need) {
#pragma unroll 1
  for (int spin = 0; spin < (1 << 16); ++spin) {
    unsigned v;
    asm volatile("ds_read_b32 %0, %1\n\ts_waitcnt lgkmcnt(0)" : "=v"(v) : "v"(addr) : "memory");
    if ((int)RFL(v) >= need) break;
    __builtin_amdgcn_s_sleep(1);
  }
}
__device__ __forceinline__ void flag_wait4(unsigned addr, int need) {
#pragma unroll 1
  for (int spin = 0; spin < (1 << 16); ++spin) {
    u32x4 v;
    asm volatile("ds_read_b128 %0, %1\n\ts_waitcnt lgkmcnt(0)" : "=v"(v) : "v"(addr) : "memory");
    if ((int)RFL(min(min(v[0], v[1]), min(v[2], v[3]))) >= need) break;
    __builtin_amdgcn_s_sleep(1);
  }
}

__global__ __launch_bounds__((NW + 1) * 64, 2) void tree_attn(
    const __hip_bfloat16* __restrict__ q, const __hip_bfloat16* __restrict__ kc, const __hip_bfloat16* __restrict__ vc,
    const __hip_bfloat16* __restrict__ kt, const __hip_bfloat16* __restrict__ vt, const __hip_bfloat16* __restrict__ mask,
    char* ws, __hip_bfloat16* __restrict__ out) {
  extern __shared__ __attribute__((aligned(16))) char lds[];
  u64 kbase, base2, now, pc;
  unsigned d0, d1;
  const int wid = RFL(threadIdx.x >> 6);
  asm volatile("s_load_dwordx2 %[kb], %[ka], 0x8\n\t"
               "s_load_dwordx2 %[b2], %[ka], %[sel]\n\t"
               "s_getpc_b64 %[pc]\n\t"
               "s_load_dword %[d0], %[pc], %[line]\n\t"
               "s_load_dword %[d1], %[pc], %[line] offset:64\n\t"
               "s_memrealtime %[now]"
               : [kb] "=&s"(kbase), [b2] "=&s"(base2), [pc] "=&s"(pc), [d0] "=&s"(d0), [d1] "=&s"(d1), [now] "=&s"(now)
               : [ka] "s"(__builtin_amdgcn_kernarg_segment_ptr()), [sel] "s"((wid >> 2) * 8),
                 [line] "s"(((blockIdx.x >> 3) * (NW + 1) + wid) * TA_CODEPF)
               : "memory");
  SBAR();
  const unsigned bid = (blockIdx.x / (8 * TA_RUNLEN)) * (8 * TA_RUNLEN) + (blockIdx.x & 7) * TA_RUNLEN + ((blockIdx.x >> 3) & (TA_RUNLEN - 1));
  const int lane = threadIdx.x & 63;
  const unsigned lds0 = (unsigned)(uintptr_t)lds;
  const unsigned voff = lane * 16;
  const int pair = bid >> 2, split = bid & 3;
  {
    unsigned keep;
#define TA_G4(VO, BASE, POL) \
        "global_load_lds_dwordx4 " VO ", " BASE POL "\n\ts_add_u32 m0, m0, %[pad]\n\ts_nop 0\n\t" \
        "global_load_lds_dwordx4 " VO ", " BASE " offset:1024" POL "\n\ts_add_u32 m0, m0, %[pad]\n\ts_nop 0\n\t" \
        "global_load_lds_dwordx4 " VO ", " BASE " offset:2048" POL "\n\ts_add_u32 m0, m0, %[pad]\n\ts_nop 0\n\t" \
        "global_load_lds_dwordx4 " VO ", " BASE " offset:3072" POL "\n\t"
    asm volatile(
        "s_mov_b32 %[keep], m0\n\ts_mov_b32 m0, %[dst2]\n\t"
        "s_waitcnt lgkmcnt(0)\n\t"
        TA_G4("%[vo2]", "%[b2]", "")
        "s_mov_b32 m0, %[dst]\n\ts_nop 0\n\t"
        TA_G4("%[vo]", "%[kb]", " nt")
        "s_mov_b32 m0, %[keep]"
        : [keep] "=&s"(keep), [kb] "+s"(kbase), [b2] "+s"(base2), [now] "+s"(now), [d0] "+s"(d0), [d1] "+s"(d1)
        : [vo] "v"(bid * WG_BYTES + wid * 4096 + voff),
          [dst] "s"(lds0 + wid * 4 * KP + (wid >> 2) * (SLOT - 16 * KP)),
          [vo2] "v"((wid == NW ? bid * WG_BYTES + 5 * 4096 : pair * 32 * ROWB + wid * 4096) + voff),
          [dst2] "s"(lds0 + (wid == NW ? SLOT + 4 * KP : Q_OFF + wid * 4 * KP)), [pad] "n"(KP - 1024)
        : "memory", "scc");
#undef TA_G4
  }
#define PROBE(k) do {} while (0)
  const unsigned flags = lds0 + FLAG_OFF;
  if (threadIdx.x < 12) flag_set(flags + lane * 4, lane < 4 ? lane : lane == F_LAND / 4 ? 1 : 0);
  asm volatile("s_waitcnt lgkmcnt(0)\n\ts_barrier" ::: "memory");

  if (wid == NW) {
    __builtin_amdgcn_s_setprio(3);
    const srd_t sK = make_srd((const char*)kbase + (size_t)bid * WG_BYTES, WG_BYTES);
#pragma unroll 1
    for (int g = 2; g < 4; ++g) dma4<KP - 1024, true>(sK, voff, 16384 + g * 4096, lds0 + SLOT + g * 4 * KP);
    const srd_t sV = make_srd((const char*)vc + (size_t)bid * WG_BYTES, WG_BYTES);
    const unsigned t0 = (unsigned)now + TA_OFF / 10;
    u32x4 done = {0u, 0u, 0u, 0u};
    unsigned dst = lds0 + 2 * SLOT;
#pragma unroll 1
    for (int c = 2; c < 2 * NT; ++c) {
      if (c >= NSLOT && (int)RFL(min(min(done[0], done[1]), min(done[2], done[3]))) < c - NSLOT + 1)
        flag_wait4(flags + F_DONE, c - NSLOT + 1);
      const int lag = (int)(RFL((unsigned)now) - t0) - (((4 * c - 1) * (TA_TAU * 64 / 10)) >> 8);
      int k = lag <= 0 ? 0 : (lag * (655360 / TA_LSTEP)) >> 16;
      if (k > TA_KNEE) k = min(TA_KNEE + (k - TA_KNEE) / TA_KDIV, TA_KMAX);
      if (c < TA_BOOST0 || c >= 2 * NT - TA_BOOST1) k = 7;
      const srd_t srd = c < NT ? sK : sV;
      const unsigned soff = (c & (NT - 1)) * 16384, pad = c < NT ? KP - 1024 : VP - 1024;
#define TA_CH(K) dma_chunk<WIN - 1 + (K) * TA_WSTEP>(srd, voff, soff, dst, pad, flags + F_LAND, c, flags + F_DONE, done, now)
      if (k < 4) {
        if (k < 2) { if (k == 0) TA_CH(0); else TA_CH(1); } else { if (k == 2) TA_CH(2); else TA_CH(3); }
      } else {
        if (k < 6) { if (k == 4) TA_CH(4); else TA_CH(5); } else { if (k == 6) TA_CH(6); else TA_CH(7); }
      }
#undef TA_CH
      dst = dst == lds0 + (NSLOT - 1) * SLOT ? lds0 : dst + SLOT;
    }
    wait_vm<32>(); flag_set(flags + F_LAND, 2 * NT - 2);
    wait_vm<16>(); flag_set(flags + F_LAND, 2 * NT - 1);
    wait_vm<0>(); flag_set(flags + F_LAND, 2 * NT);
    return;
  }

  const int w = wid;
  const int r = lane & 31, h = lane >> 5;
  if (w == 3) {
    dma2(make_srd((const char*)kt + (size_t)pair * 16 * ROWB, 16 * ROWB), voff, split * 4 * ROWB, lds0 + TK_OFF);
    dma2(make_srd((const char*)mask + (pair >> 3) * 512, 512), voff, 0, lds0 + MK_OFF);
  }
  if (w == 2) dma2(make_srd((const char*)vt + (size_t)pair * 16 * ROWB, 16 * ROWB), voff, split * 4 * ROWB, lds0 + TV_OFF);
  const int qrow = 2 * (r & 15) + (r >> 4);
  const int koff = (r & 15) * KP + (r >> 4) * 512 + h * 16;
  const int trofs = ((lane >> 4) & 1) * 128 + w * 32 + (lane & 3) * 8;
  const int vofs = (4 * h + ((lane & 15) >> 2)) * VP + trofs;
  const srd_t sZ = make_srd(ws + CTRL_BYTES + (size_t)pair * 64 * MSG, 64 * MSG);
  const int peer = (split + 1 + (lane & 3)) & 3;
  GAS u64* sig_in = (GAS u64*)(ws + ((pair * 4 + split) * 4 + w) * TA_SIGSTR) + peer;
  GAS u64* sig_out = (GAS u64*)(ws + ((pair * 4 + peer) * 4 + w) * TA_SIGSTR) + split;
  unsigned zin[3];
#pragma unroll
  for (int k = 0; k < 3; ++k) zin[k] = ((split * 4 + ((split + 1 + k) & 3)) * 4 + w) * MSG;
  if (w == 3) wait_vm<4>(); else if (w == 2) wait_vm<2>(); else wait_vm<0>();
  if (lane == 0) asm volatile("ds_add_u32 %0, %1" ::"v"(flags + F_Q), "v"(1u) : "memory");
  flag_wait(flags + F_Q, NW);
  STAMP(1);
  bf16x8 qf[16];
#pragma unroll
  for (int kk = 0; kk < 16; ++kk) qf[kk] = ld_frag((lds_cptr)lds + Q_OFF + koff + kk * 32);

  const lds_ptr refp = (lds_ptr)lds + REF_OFF + r * 4;
  float m = -INFINITY, l = 0.f;
  if (w == 3) {
    wait_vm<0>();
    const u32x2 mbits = *(const LAS u32x2*)((lds_cptr)lds + MK_OFF + (qrow & 15) * 32 + split * 8);
    const lds_cptr tk = (lds_cptr)lds + TK_OFF + (r & 3) * ROWB + h * 16;
    f32x16 s = f32x16{};
#pragma unroll
    for (int kk = 0; kk < 16; ++kk) s = MFMA32(ld_frag(tk + kk * 32), qf[kk], s);
#pragma unroll
    for (int i = 0; i < 4; ++i) {
      const float mk = __uint_as_float((i & 1) ? (mbits[i >> 1] & 0xffff0000u) : (mbits[i >> 1] << 16));
      s[i] = fmaf(s[i], SCALE, (1.0f - mk) * -1000000000.0f) * LOG2E;
      m = fmaxf(m, s[i]);
    }
#pragma unroll
    for (int i = 0; i < 4; ++i) { s[i] = __builtin_amdgcn_exp2f(s[i] - m); l += s[i]; }
    if (h) l = 0.f;
    *(LAS float*)refp = m;
    *(LAS u32x2*)((lds_ptr)lds + PT_OFF + r * 8) = u32x2{pack2(s[0], s[1]), pack2(s[2], s[3])};
    flag_set(flags + F_REF, 1);
  }
  STAMP(2);

  int slot_off = w * SLOT;
  const lds_ptr pbuf = (lds_ptr)lds + P_OFF + lane * 16;
  bf16x8 preg[NT / 8][2];

  auto k_tile = [&](int c, bf16x8& p0, bf16x8& p1) {
    flag_wait(flags + F_LAND, c + 1);
    const lds_cptr kfrag = (lds_cptr)lds + slot_off + koff;
    f32x16 s = f32x16{};
    bf16x8 kf[8];
#pragma unroll
    for (int kk = 0; kk < 8; ++kk) kf[kk] = ld_frag(kfrag + kk * 32);
    SBAR();
#pragma unroll
    for (int kk = 0; kk < 8; ++kk) { s = MFMA32(kf[kk], qf[kk], s); kf[kk] = ld_frag(kfrag + (kk + 8) * 32); SBAR(); }
    flag_set(flags + F_DONE + w * 4, min(c + NW, NT));
    slot_off = slot_off == 0 ? (NSLOT - 1) * SLOT : slot_off - SLOT;
    SBAR();
#pragma unroll
    for (int kk = 0; kk < 8; ++kk) s = MFMA32(kf[kk], qf[kk + 8], s);
    float tmax = fmaxf(s[0], s[1]);
#pragma unroll
    for (int i = 2; i < 16; ++i) tmax = fmaxf(tmax, s[i]);
    tmax = xhalf_max(tmax) * C2;
    PIN(tmax);
    flag_wait(flags + F_REF, c + 1);
    const float mn = fmaxf(*(const LAS float*)(refp + c * 128), tmax);
    *(LAS float*)(refp + (c + 1) * 128) = mn;
    flag_set(flags + F_REF, c + 2);
    const float alpha = __builtin_amdgcn_exp2f(m - mn);
    m = mn;
    float ps = 0.f;
#pragma unroll
    for (int i = 0; i < 16; ++i) { s[i] = __builtin_amdgcn_exp2f(fmaf(s[i], C2, -mn)); ps += s[i]; }
    l = fmaf(l, alpha, ps);
    p0 = pack8(s, 0); p1 = pack8(s, 8);
    PIN(l); PIN(m);
  };
#pragma unroll 1
  for (int c = w; c < NT / 2; c += NW) {
    bf16x8 p0, p1;
    k_tile(c, p0, p1);
    *(LAS bf16x8*)(pbuf + c * 2048) = p0;
    *(LAS bf16x8*)(pbuf + c * 2048 + 1024) = p1;
  }
  unsigned warm;
  {
    const char* pa = w == 0 ? (const char*)vc + (size_t)bid * WG_BYTES : w == 1 ? ws + CTRL_BYTES + (size_t)pair * 64 * MSG
                   : w == 2 ? (const char*)out + (size_t)pair * 32 * ROWB : ws + CTRL_BYTES + (size_t)(pair + 1) * 64 * MSG - 64;
    warm = __builtin_nontemporal_load((const GAS unsigned*)pa + (lane & 1));
  }
  unsigned in0 = __hip_atomic_load((GAS unsigned*)sig_in, RLX_AGENT);
  unsigned out1 = __hip_atomic_load((GAS unsigned*)sig_out, RLX_AGENT) + 1;
#pragma unroll
  for (int t = 0; t < NT / 8; ++t) { k_tile(NT / 2 + 4 * t + w, preg[t][0], preg[t][1]); PIN(preg[t][0]); PIN(preg[t][1]); }
  wait_vm<0>();
  asm volatile("" ::"v"(warm));
  flag_set(flags + F_PRDY + w * 4, 1);
  flag_wait4(flags + F_PRDY, 1);
  PROBE(2);

  f32x16 o[2];
  float rcur = *(const LAS float*)refp;
  {
    const lds_cptr tv = (lds_cptr)lds + TV_OFF + ((lane & 15) >> 2) * ROWB + trofs;
    u32x2 pt2 = *(const LAS u32x2*)((lds_cptr)lds + PT_OFF + r * 8);
    if (h) pt2 = u32x2{0u, 0u};
    const bf16x8 pt = __builtin_bit_cast(bf16x8, u32x4{pt2[0], pt2[1], 0u, 0u});
#pragma unroll
    for (int cb = 0; cb < 2; ++cb) { const s16x4 v4 = ld_tr(tv + cb * 256); o[cb] = MFMA32(join(v4, v4), pt, f32x16{}); }
  }
  const float M = *(const LAS float*)(refp + NT * 128);
  *(LAS float*)((lds_ptr)lds + L_OFF + w * 128 + r * 4) = xhalf_sum(l) * __builtin_amdgcn_exp2f(m - M);
  slot_off = (NT % NSLOT) * SLOT;
  auto v_tiles = [&](int v0, int v1) {
#pragma unroll 1
    for (int v = v0; v < v1; ++v) {
      const float rnew = *(const LAS float*)(refp + (v + 1) * 128);
      bf16x8 p0 = ld_frag(pbuf + (v & (NT / 2 - 1)) * 2048), p1 = ld_frag(pbuf + (v & (NT / 2 - 1)) * 2048 + 1024);
      if (__any(rnew != rcur)) {
        const float alpha = __builtin_amdgcn_exp2f(rcur - rnew);
        o[0] *= alpha; o[1] *= alpha;
        rcur = rnew;
      }
      PIN(p0); PIN(p1);
      flag_wait(flags + F_LAND, NT + v + 1);
      const lds_cptr vfrag = (lds_cptr)lds + slot_off + vofs;
      bf16x8 vf[4];
#pragma unroll
      for (int i = 0; i < 4; ++i)
        vf[i] = join(ld_tr(vfrag + (i >> 1) * 512 + (i & 1) * 256), ld_tr(vfrag + 8 * VP + (i >> 1) * 512 + (i & 1) * 256));
      SBAR();
      o[0] = MFMA32(vf[0], p0, o[0]);
      o[1] = MFMA32(vf[1], p0, o[1]);
      SBAR();
      flag_set(flags + F_DONE + w * 4, NT + v + 1);
      slot_off = slot_off == (NSLOT - 1) * SLOT ? 0 : slot_off + SLOT;
      o[0] = MFMA32(vf[2], p1, o[0]);
      o[1] = MFMA32(vf[3], p1, o[1]);
    }
  };
  v_tiles(0, 20);
  flag_wait4(flags + F_DONE, NT + 16);
#pragma unroll
  for (int j = 0; j < 4; ++j) { *(LAS bf16x8*)(pbuf + (4 * j + w) * 2048) = preg[j][0]; *(LAS bf16x8*)(pbuf + (4 * j + w) * 2048 + 1024) = preg[j][1]; }
  flag_set(flags + F_PRDY + w * 4, 2);
  v_tiles(20, 32);
  flag_wait4(flags + F_PRDY, 2);
  float L = 0.f;
#pragma unroll
  for (int i = 0; i < NW; ++i) L += *(const LAS float*)((lds_cptr)lds + L_OFF + i * 128 + r * 4);
  v_tiles(32, 36);
  flag_wait4(flags + F_DONE, NT + 32);
#pragma unroll
  for (int j = 4; j < 8; ++j) { *(LAS bf16x8*)(pbuf + (4 * j + w) * 2048) = preg[j][0]; *(LAS bf16x8*)(pbuf + (4 * j + w) * 2048 + 1024) = preg[j][1]; }
  flag_set(flags + F_PRDY + w * 4, 3);
  v_tiles(36, 48);
  flag_wait4(flags + F_PRDY, 3);
  unsigned zout[4];
#pragma unroll
  for (int j = 0; j < 4; ++j) { zout[j] = (j == split ? 64 : (j * 4 + split) * 4 + w) * MSG + voff; PIN(zout[j]); }
  char* orow = (char*)out + ((size_t)pair * 32 + qrow) * ROWB + split * 128 + w * 32 + h * 16;
  PIN(orow);
  unsigned mlw = __float_as_uint(h ? L : M);
  PIN(mlw); PIN(in0); PIN(out1);
  v_tiles(48, NT);
  STAMP(3);
  PROBE(3);

#pragma unroll
  for (int j = 0; j < 4; ++j) {
    u32x4 x;
#pragma unroll
    for (int e = 0; e < 4; ++e) x[e] = pack2(o[j >> 1][8 * (j & 1) + 2 * e], o[j >> 1][8 * (j & 1) + 2 * e + 1]);
    __builtin_amdgcn_raw_buffer_store_b128(x, sZ, zout[j], 0, 16);
    __builtin_amdgcn_raw_buffer_store_b32(mlw, sZ, zout[j] + 1024 - 12 * lane, 0, 16);
  }
  unsigned g0 = __hip_atomic_load((GAS unsigned*)sig_in, RLX_AGENT), g1 = 0, g2 = 0;
  SBAR();
  float own[8];
#pragma unroll
  for (int e = 0; e < 8; ++e) {
    const float lo = (split & 1) ? o[0][8 + e] : o[0][e], hi = (split & 1) ? o[1][8 + e] : o[1][e];
    own[e] = (split & 2) ? hi : lo;
    PIN(own[e]);
  }
  wait_vm<0>();
  PIN(g0);
  STAMP(5);
  if (lane < 3) __hip_atomic_store(sig_out, ((u64)in0 << 32) | out1, RLX_AGENT);
  if (((unsigned)__ballot(g0 != in0) & 7u) != 7u) {
    int spin = 0;
#pragma unroll 1
    do { if (TA_PSLEEP) __builtin_amdgcn_s_sleep(TA_PSLEEP); g0 = __hip_atomic_load((GAS unsigned*)sig_in, RLX_AGENT); } while (((unsigned)__ballot(g0 != in0) & 7u) != 7u && ++spin < (1 << 16));
  }
  __builtin_amdgcn_fence(__ATOMIC_ACQUIRE, "wavefront");
  asm volatile("" ::: "memory");
  u32x4 a[3];
  unsigned ml[3];
#pragma unroll
  for (int k = 0; k < 3; ++k) {
    ml[k] = __builtin_amdgcn_raw_buffer_load_b32(sZ, 1024 + lane * 4, zin[k], 16);
    a[k] = __builtin_amdgcn_raw_buffer_load_b128(sZ, voff, zin[k], 16);
  }
  const unsigned peer_view = __hip_atomic_load((GAS unsigned*)sig_in + 1, RLX_AGENT);
  STAMP(4);
  {
    float mk[3], lk[3], wk[3], G = M;
#pragma unroll
    for (int k = 0; k < 3; ++k) {
      auto x = __builtin_amdgcn_permlane32_swap(ml[k], ml[k], false, false);
      mk[k] = __uint_as_float(x[0]); lk[k] = __uint_as_float(x[1]);
      G = fmaxf(G, mk[k]);
    }
    const float w0 = __builtin_amdgcn_exp2f(M - G);
    float den = w0 * L;
#pragma unroll
    for (int k = 0; k < 3; ++k) { wk[k] = __builtin_amdgcn_exp2f(mk[k] - G); den = fmaf(wk[k], lk[k], den); }
    const float inv = 1.0f / den;
#pragma unroll
    for (int e = 0; e < 8; ++e) own[e] *= w0;
#pragma unroll
    for (int k = 0; k < 3; ++k)
#pragma unroll
      for (int e = 0; e < 4; ++e) {
        own[2 * e] = fmaf(wk[k], __uint_as_float(a[k][e] << 16), own[2 * e]);
        own[2 * e + 1] = fmaf(wk[k], __uint_as_float(a[k][e] & 0xffff0000u), own[2 * e + 1]);
      }
#pragma unroll
    for (int e = 0; e < 8; ++e) own[e] *= inv;
  }
  auto x = __builtin_amdgcn_permlane32_swap(pack2(own[0], own[1]), pack2(own[4], own[5]), false, false);
  auto y = __builtin_amdgcn_permlane32_swap(pack2(own[2], own[3]), pack2(own[6], own[7]), false, false);
  *(GAS u32x4*)orow = u32x4{x[0], y[0], x[1], y[1]};
  asm volatile("" ::"v"(g0), "v"(g1), "v"(g2), "v"(peer_view));
  if (lane < 3 && peer_view == out1) __hip_atomic_store(sig_out, ((u64)in0 << 32) | (out1 + 1), RLX_AGENT);
}

}

extern "C" void kernel_launch(void* const* d_in, const int* in_sizes, int n_in, void* d_out, int out_size, void* d_ws,
                              size_t ws_size, hipStream_t stream) {
  using namespace ta;
  (void)in_sizes; (void)n_in; (void)out_size; (void)ws_size;
  (void)hipFuncSetAttribute((const void*)tree_attn, hipFuncAttributeMaxDynamicSharedMemorySize, LDS_BYTES);
  hipLaunchKernelGGL(tree_attn, dim3(NPAIR * NSPLIT), dim3((NW + 1) * 64), LDS_BYTES, stream,
                     (const __hip_bfloat16*)d_in[0], (const __hip_bfloat16*)d_in[1], (const __hip_bfloat16*)d_in[2],
                     (const __hip_bfloat16*)d_in[3], (const __hip_bfloat16*)d_in[4], (const __hip_bfloat16*)d_in[5],
                     (char*)d_ws, (__hip_bfloat16*)d_out);
}
```

```cpp
#include <hip/hip_runtime.h>
#include <hip/hip_bf16.h>

namespace ta {

constexpr int NB = 8, HKV = 8, CTX = 8192, HD = 256;
constexpr int NPAIR = NB * HKV, NSPLIT = 4, NW = 4;
constexpr int ROWB = HD * 2;
constexpr int WG_BYTES = CTX / NSPLIT * ROWB;
constexpr int NT = CTX / NSPLIT / 32;
constexpr int KP = 1040, VP = 1056;
constexpr int SLOT = 16 * VP, NSLOT = 5;
constexpr int P_OFF = NSLOT * SLOT;
constexpr int REF_OFF = P_OFF + NT / 2 * 2048;
constexpr int TV_OFF = REF_OFF + (NT + 1) * 128;
constexpr int PT_OFF = TV_OFF + 4 * ROWB;
constexpr int L_OFF = PT_OFF + 256;
constexpr int FLAG_OFF = L_OFF + NW * 128;
constexpr int F_DONE = 0, F_PRDY = 16, F_LAND = 32, F_REF = 36, F_Q = 40;
constexpr int LDS_BYTES = FLAG_OFF + 48;
constexpr int TK_OFF = REF_OFF - 4 * ROWB, Q_OFF = TK_OFF - 16 * KP, MK_OFF = Q_OFF - 2048;
constexpr float SCALE = 0.0625f, LOG2E = 1.4426950408889634f, C2 = SCALE * LOG2E;
constexpr int WIN = 20, WSTEP = 2, TAU_NS = 540, OFF_NS = 400, LSTEP_NS = 700, KNEE = 4;
constexpr int MSG = 1024 + 256;
constexpr int CTRL_BYTES = NPAIR * 64 * 128;
constexpr int LDS_SPINS = 1 << 20, MEM_SPINS = 1 << 21;
static_assert(LDS_BYTES <= 160 * 1024 && MK_OFF >= P_OFF + 16 * 2048 && FLAG_OFF % 16 == 0, "LDS map");
static_assert(WIN >= 17 && WIN + 7 * WSTEP <= 48, "publish / drain ladder assumes 1..3 chunks in flight");

typedef __bf16 bf16x8 __attribute__((ext_vector_type(8)));
typedef __bf16 bf16x2 __attribute__((ext_vector_type(2)));
typedef short s16x4 __attribute__((ext_vector_type(4)));
typedef float f32x16 __attribute__((ext_vector_type(16)));
typedef float f32x4 __attribute__((ext_vector_type(4)));
typedef unsigned u32x4 __attribute__((ext_vector_type(4)));
typedef unsigned u32x2 __attribute__((ext_vector_type(2)));
typedef unsigned long long u64;
#define LAS __attribute__((address_space(3)))
#define GAS __attribute__((address_space(1)))
typedef LAS const char* lds_cptr;
typedef LAS char* lds_ptr;
typedef __amdgpu_buffer_rsrc_t srd_t;

#define SBAR() __builtin_amdgcn_sched_barrier(0)
#define PIN(x) asm volatile("" : "+v"(x))
#define MFMA32(a, b, c) __builtin_amdgcn_mfma_f32_32x32x16_bf16((a), (b), (c), 0, 0, 0)
#define RLX_AGENT __ATOMIC_RELAXED, __HIP_MEMORY_SCOPE_AGENT
#define RFL(x) __builtin_amdgcn_readfirstlane(x)
constexpr int SC1 = 16;

__device__ __forceinline__ srd_t make_srd(const void* p, int bytes) {
  return __builtin_amdgcn_make_buffer_rsrc((void*)p, (short)0, bytes, 0x00020000);
}

template <int PAD>
__device__ __forceinline__ void dma4(srd_t srd, unsigned voff, unsigned soff, unsigned ldsb) {
  unsigned keep;
  asm volatile(
      "s_mov_b32 %0, m0\n\ts_mov_b32 m0, %4\n\ts_nop 3\n\t"
      "buffer_load_dwordx4 %1, %2, %3 offen nt lds\n\t"
      "s_add_u32 m0, %4, %5\n\ts_nop 0\n\t"
      "buffer_load_dwordx4 %1, %2, %3 offen offset:1024 nt lds\n\t"
      "s_add_u32 m0, %4, %6\n\ts_nop 0\n\t"
      "buffer_load_dwordx4 %1, %2, %3 offen offset:2048 nt lds\n\t"
      "s_add_u32 m0, %4, %7\n\ts_nop 0\n\t"
      "buffer_load_dwordx4 %1, %2, %3 offen offset:3072 nt lds\n\t"
      "s_mov_b32 m0, %0"
      : "=&s"(keep)
      : "v"(voff), "s"(srd), "s"(soff), "s"(ldsb), "n"(PAD), "n"(2 * PAD), "n"(3 * PAD)
      : "memory", "scc");
}
#define TA_PIECE(I, OFS, STEP) \
      "s_waitcnt vmcnt(%[n])\n\t" \
      "buffer_load_dwordx4 %[vo], %[srd], %[so] offen " OFS " nt lds\n\t" \
      "s_add_u32 m0, m0, " STEP "\n\t" \
      ".if (%[n] & 15) == " #I "\n\tds_write_b32 %[la], %[lv]\n\t.endif\n\t"
#define TA_GROUP(I0, I1, I2, I3) \
      TA_PIECE(I0, "", "%[pad]") TA_PIECE(I1, "offset:1024", "%[pad]") TA_PIECE(I2, "offset:2048", "%[pad]") \
      TA_PIECE(I3, "offset:3072", "%[padg]") "s_add_u32 %[so], %[so], 0x1000\n\t"
template <int N>
__device__ __forceinline__ void dma_chunk(srd_t srd, unsigned voff, unsigned soff, unsigned ldsb, unsigned pad,
                                          unsigned land_addr, int c, unsigned done_addr, u32x4& done, u64& now) {
  unsigned keep;
  asm volatile(
      "s_mov_b32 %[keep], m0\n\ts_mov_b32 m0, %[lds]\n\ts_nop 2\n\t"
      TA_GROUP(0, 1, 2, 3) TA_GROUP(4, 5, 6, 7) TA_GROUP(8, 9, 10, 11)
      "ds_read_b128 %[done], %[da]\n\ts_memrealtime %[now]\n\t"
      TA_GROUP(12, 13, 14, 15)
      "s_mov_b32 m0, %[keep]\n\ts_waitcnt lgkmcnt(0)"
      : [keep] "=&s"(keep), [so] "+&s"(soff), [done] "=&v"(done), [now] "=&s"(now)
      : [vo] "v"(voff), [srd] "s"(srd), [lds] "s"(ldsb), [pad] "s"(pad), [padg] "s"(pad + 4096), [n] "n"(N),
        [la] "v"(land_addr), [lv] "v"(c - N / 16), [da] "v"(done_addr)
      : "memory", "scc");
}
__device__ __forceinline__ void dma2(srd_t srd, unsigned voff, unsigned soff, unsigned ldsb) {
  unsigned keep;
  asm volatile("s_mov_b32 %0, m0\n\ts_mov_b32 m0, %4\n\ts_nop 3\n\t"
               "buffer_load_dwordx4 %1, %2, %3 offen lds\n\t"
               "buffer_load_dwordx4 %1, %2, %3 offen offset:1024 lds\n\t"
               "s_mov_b32 m0, %0"
               : "=&s"(keep) : "v"(voff), "s"(srd), "s"(soff), "s"(ldsb) : "memory");
}
template <int N> __device__ __forceinline__ void wait_vm() { asm volatile("s_waitcnt vmcnt(%0)" ::"n"(N) : "memory"); }

__device__ __forceinline__ bf16x8 ld_frag(lds_cptr p) { return *(const LAS bf16x8*)p; }
__device__ __forceinline__ s16x4 ld_tr(lds_cptr p) { return __builtin_amdgcn_ds_read_tr16_b64_v4i16((LAS s16x4*)p); }
__device__ __forceinline__ bf16x8 join(s16x4 lo, s16x4 hi) {
  return __builtin_bit_cast(bf16x8, __builtin_shufflevector(lo, hi, 0, 1, 2, 3, 4, 5, 6, 7));
}
__device__ __forceinline__ float xhalf_max(float x) {
  auto r = __builtin_amdgcn_permlane32_swap(__float_as_uint(x), __float_as_uint(x), false, false);
  return fmaxf(__uint_as_float(r[0]), __uint_as_float(r[1]));
}
__device__ __forceinline__ float xhalf_sum(float x) {
  auto r = __builtin_amdgcn_permlane32_swap(__float_as_uint(x), __float_as_uint(x), false, false);
  return __uint_as_float(r[0]) + __uint_as_float(r[1]);
}
__device__ __forceinline__ unsigned pack2(float a, float b) {
  const bf16x2 v = {(__bf16)a, (__bf16)b};
  return __builtin_bit_cast(unsigned, v);
}
__device__ __forceinline__ bf16x8 pack8(const f32x16& p, int o) {
  bf16x8 r;
#pragma unroll
  for (int j = 0; j < 8; ++j) r[j] = (__bf16)p[o + j];
  return r;
}
__device__ __forceinline__ void flag_set(unsigned addr, unsigned v) {
  asm volatile("s_waitcnt lgkmcnt(0)\n\tds_write_b32 %0, %1" ::"v"(addr), "v"(v) : "memory");
}
__device__ __forceinline__ void flag_wait(unsigned addr, int need) {
#pragma unroll 1
  for (int spin = 0; spin < LDS_SPINS; ++spin) {
    unsigned v;
    asm volatile("ds_read_b32 %0, %1\n\ts_waitcnt lgkmcnt(0)" : "=v"(v) : "v"(addr) : "memory");
    if ((int)RFL(v) >= need) break;
    __builtin_amdgcn_s_sleep(1);
  }
}
__device__ __forceinline__ void flag_wait4(unsigned addr, int need) {
#pragma unroll 1
  for (int spin = 0; spin < LDS_SPINS; ++spin) {
    u32x4 v;
    asm volatile("ds_read_b128 %0, %1\n\ts_waitcnt lgkmcnt(0)" : "=v"(v) : "v"(addr) : "memory");
    if ((int)RFL(min(min(v[0], v[1]), min(v[2], v[3]))) >= need) break;
    __builtin_amdgcn_s_sleep(1);
  }
}

__global__ __launch_bounds__((NW + 1) * 64, 2) void tree_attn(
    const __hip_bfloat16* __restrict__ q, const __hip_bfloat16* __restrict__ kc, const __hip_bfloat16* __restrict__ vc,
    const __hip_bfloat16* __restrict__ kt, const __hip_bfloat16* __restrict__ vt, const __hip_bfloat16* __restrict__ mask,
    char* ws, __hip_bfloat16* __restrict__ out) {
  extern __shared__ __attribute__((aligned(16))) char lds[];
  u64 kbase, base2, now, pc;
  unsigned d0, d1;
  const int wid = RFL(threadIdx.x >> 6);
  asm volatile("s_load_dwordx2 %[kb], %[ka], 0x8\n\t"
               "s_load_dwordx2 %[b2], %[ka], %[sel]\n\t"
               "s_getpc_b64 %[pc]\n\t"
               "s_load_dword %[d0], %[pc], %[line]\n\t"
               "s_load_dword %[d1], %[pc], %[line] offset:64\n\t"
               "s_memrealtime %[now]"
               : [kb] "=&s"(kbase), [b2] "=&s"(base2), [pc] "=&s"(pc), [d0] "=&s"(d0), [d1] "=&s"(d1), [now] "=&s"(now)
               : [ka] "s"(__builtin_amdgcn_kernarg_segment_ptr()), [sel] "s"((wid >> 2) * 8),
                 [line] "s"(((blockIdx.x >> 3) * (NW + 1) + wid) * 128)
               : "memory");
  SBAR();
  const unsigned bid = (blockIdx.x & 7) * 32 + (blockIdx.x >> 3);
  const int lane = threadIdx.x & 63;
  const unsigned lds0 = (unsigned)(uintptr_t)lds;
  const unsigned voff = lane * 16;
  const int pair = bid >> 2, split = bid & 3;
  {
    unsigned keep;
#define TA_G4(VO, BASE, POL) \
        "global_load_lds_dwordx4 " VO ", " BASE POL "\n\ts_add_u32 m0, m0, %[pad]\n\ts_nop 0\n\t" \
        "global_load_lds_dwordx4 " VO ", " BASE " offset:1024" POL "\n\ts_add_u32 m0, m0, %[pad]\n\ts_nop 0\n\t" \
        "global_load_lds_dwordx4 " VO ", " BASE " offset:2048" POL "\n\ts_add_u32 m0, m0, %[pad]\n\ts_nop 0\n\t" \
        "global_load_lds_dwordx4 " VO ", " BASE " offset:3072" POL "\n\t"
    asm volatile(
        "s_mov_b32 %[keep], m0\n\ts_mov_b32 m0, %[dst2]\n\t"
        "s_waitcnt lgkmcnt(0)\n\t"
        TA_G4("%[vo2]", "%[b2]", "")
        "s_mov_b32 m0, %[dst]\n\ts_nop 0\n\t"
        TA_G4("%[vo]", "%[kb]", " nt")
        "s_mov_b32 m0, %[keep]"
        : [keep] "=&s"(keep), [kb] "+s"(kbase), [b2] "+s"(base2), [now] "+s"(now), [d0] "+s"(d0), [d1] "+s"(d1)
        : [vo] "v"(bid * WG_BYTES + wid * 4096 + voff),
          [dst] "s"(lds0 + wid * 4 * KP + (wid >> 2) * (SLOT - 16 * KP)),
          [vo2] "v"((wid == NW ? bid * WG_BYTES + 5 * 4096 : pair * 32 * ROWB + wid * 4096) + voff),
          [dst2] "s"(lds0 + (wid == NW ? SLOT + 4 * KP : Q_OFF + wid * 4 * KP)), [pad] "n"(KP - 1024)
        : "memory", "scc");
#undef TA_G4
  }
  const unsigned flags = lds0 + FLAG_OFF;
  if (threadIdx.x < 12) flag_set(flags + lane * 4, lane < 4 ? lane : lane == F_LAND / 4 ? 1 : 0);
  asm volatile("s_waitcnt lgkmcnt(0)\n\ts_barrier" ::: "memory");

  if (wid == NW) {
    __builtin_amdgcn_s_setprio(3);
    const srd_t sK = make_srd((const char*)kbase + (size_t)bid * WG_BYTES, WG_BYTES);
#pragma unroll 1
    for (int g = 2; g < 4; ++g) dma4<KP - 1024>(sK, voff, 16384 + g * 4096, lds0 + SLOT + g * 4 * KP);
    const srd_t sV = make_srd((const char*)vc + (size_t)bid * WG_BYTES, WG_BYTES);
    const unsigned t0 = (unsigned)now + OFF_NS / 10;
    u32x4 done = {0u, 0u, 0u, 0u};
    unsigned dst = lds0 + 2 * SLOT;
#pragma unroll 1
    for (int c = 2; c < 2 * NT; ++c) {
      if (c >= NSLOT && (int)RFL(min(min(done[0], done[1]), min(done[2], done[3]))) < c - NSLOT + 1)
        flag_wait4(flags + F_DONE, c - NSLOT + 1);
      const int lag = min((int)(RFL((unsigned)now) - t0) - (((4 * c - 1) * (TAU_NS * 64 / 10)) >> 8), 1 << 16);
      int k = lag <= 0 ? 0 : (lag * (655360 / LSTEP_NS)) >> 16;
      if (k > KNEE) k = KNEE + (k - KNEE) / 2;
      const srd_t srd = c < NT ? sK : sV;
      const unsigned soff = (c & (NT - 1)) * 16384, pad = c < NT ? KP - 1024 : VP - 1024;
#define TA_CH(K) dma_chunk<WIN - 1 + (K) * WSTEP>(srd, voff, soff, dst, pad, flags + F_LAND, c, flags + F_DONE, done, now)
      if (k < 4) {
        if (k < 2) { if (k == 0) TA_CH(0); else TA_CH(1); } else { if (k == 2) TA_CH(2); else TA_CH(3); }
      } else {
        if (k < 6) { if (k == 4) TA_CH(4); else TA_CH(5); } else { if (k == 6) TA_CH(6); else TA_CH(7); }
      }
#undef TA_CH
      dst = dst == lds0 + (NSLOT - 1) * SLOT ? lds0 : dst + SLOT;
    }
    wait_vm<32>(); flag_set(flags + F_LAND, 2 * NT - 2);
    wait_vm<16>(); flag_set(flags + F_LAND, 2 * NT - 1);
    wait_vm<0>(); flag_set(flags + F_LAND, 2 * NT);
    return;
  }

  const int w = wid;
  const int r = lane & 31, h = lane >> 5;
  if (w == 3) {
    dma2(make_srd((const char*)kt + (size_t)pair * 16 * ROWB, 16 * ROWB), voff, split * 4 * ROWB, lds0 + TK_OFF);
    dma2(make_srd((const char*)mask + (pair >> 3) * 512, 512), voff, 0, lds0 + MK_OFF);
  }
  if (w == 2) dma2(make_srd((const char*)vt + (size_t)pair * 16 * ROWB, 16 * ROWB), voff, split * 4 * ROWB, lds0 + TV_OFF);
  const int qrow = 2 * (r & 15) + (r >> 4);
  const int koff = (r & 15) * KP + (r >> 4) * 512 + h * 16;
  const int trofs = ((lane >> 4) & 1) * 128 + w * 32 + (lane & 3) * 8;
  const int vofs = (4 * h + ((lane & 15) >> 2)) * VP + trofs;
  if (w == 3) wait_vm<4>(); else if (w == 2) wait_vm<2>(); else wait_vm<0>();
  if (lane == 0) asm volatile("ds_add_u32 %0, %1" ::"v"(flags + F_Q), "v"(1u) : "memory");
  flag_wait(flags + F_Q, NW);
  bf16x8 qf[16];
#pragma unroll
  for (int kk = 0; kk < 16; ++kk) qf[kk] = ld_frag((lds_cptr)lds + Q_OFF + koff + kk * 32);

  const lds_ptr refp = (lds_ptr)lds + REF_OFF + r * 4;
  float m = -INFINITY, l = 0.f;
  if (w == 3) {
    wait_vm<0>();
    const u32x2 mbits = *(const LAS u32x2*)((lds_cptr)lds + MK_OFF + (qrow & 15) * 32 + split * 8);
    const lds_cptr tk = (lds_cptr)lds + TK_OFF + (r & 3) * ROWB + h * 16;
    f32x16 s = f32x16{};
#pragma unroll
    for (int kk = 0; kk < 16; ++kk) s = MFMA32(ld_frag(tk + kk * 32), qf[kk], s);
#pragma unroll
    for (int i = 0; i < 4; ++i) {
      const float mk = __uint_as_float((i & 1) ? (mbits[i >> 1] & 0xffff0000u) : (mbits[i >> 1] << 16));
      s[i] = fmaf(s[i], SCALE, (1.0f - mk) * -1000000000.0f) * LOG2E;
      m = fmaxf(m, s[i]);
    }
#pragma unroll
    for (int i = 0; i < 4; ++i) { s[i] = __builtin_amdgcn_exp2f(s[i] - m); l += s[i]; }
    if (h) l = 0.f;
    *(LAS float*)refp = m;
    *(LAS u32x2*)((lds_ptr)lds + PT_OFF + r * 8) = u32x2{pack2(s[0], s[1]), pack2(s[2], s[3])};
    flag_set(flags + F_REF, 1);
  }

  int slot_off = w * SLOT;
  const lds_ptr pbuf = (lds_ptr)lds + P_OFF + lane * 16;
  bf16x8 preg[NT / 8][2];

  auto k_tile = [&](int c, bf16x8& p0, bf16x8& p1) {
    flag_wait(flags + F_LAND, c + 1);
    const lds_cptr kfrag = (lds_cptr)lds + slot_off + koff;
    f32x16 s = f32x16{};
    bf16x8 kf[8];
#pragma unroll
    for (int kk = 0; kk < 8; ++kk) kf[kk] = ld_frag(kfrag + kk * 32);
    SBAR();
#pragma unroll
    for (int kk = 0; kk < 8; ++kk) { s = MFMA32(kf[kk], qf[kk], s); kf[kk] = ld_frag(kfrag + (kk + 8) * 32); SBAR(); }
    flag_set(flags + F_DONE + w * 4, min(c + NW, NT));
    slot_off = slot_off == 0 ? (NSLOT - 1) * SLOT : slot_off - SLOT;
    SBAR();
#pragma unroll
    for (int kk = 0; kk < 8; ++kk) s = MFMA32(kf[kk], qf[kk + 8], s);
    float tmax = fmaxf(s[0], s[1]);
#pragma unroll
    for (int i = 2; i < 16; ++i) tmax = fmaxf(tmax, s[i]);
    tmax = xhalf_max(tmax) * C2;
    PIN(tmax);
    flag_wait(flags + F_REF, c + 1);
    const float mn = fmaxf(*(const LAS float*)(refp + c * 128), tmax);
    *(LAS float*)(refp + (c + 1) * 128) = mn;
    flag_set(flags + F_REF, c + 2);
    const float alpha = __builtin_amdgcn_exp2f(m - mn);
    m = mn;
    float ps = 0.f;
#pragma unroll
    for (int i = 0; i < 16; ++i) { s[i] = __builtin_amdgcn_exp2f(fmaf(s[i], C2, -mn)); ps += s[i]; }
    l = fmaf(l, alpha, ps);
    p0 = pack8(s, 0); p1 = pack8(s, 8);
    PIN(l); PIN(m);
  };
#pragma unroll 1
  for (int c = w; c < NT / 2; c += NW) {
    bf16x8 p0, p1;
    k_tile(c, p0, p1);
    *(LAS bf16x8*)(pbuf + c * 2048) = p0;
    *(LAS bf16x8*)(pbuf + c * 2048 + 1024) = p1;
  }

  char* msgs = ws + CTRL_BYTES + (size_t)pair * 64 * MSG;
  const srd_t sZ = make_srd(msgs, 64 * MSG);
  const int peer = (split + 1 + (lane & 3) % 3) & 3;
  GAS u64* sig_in = (GAS u64*)(ws + (((pair * 4 + peer) * 4 + split) * 4 + w) * 128);
  GAS u64* sig_out = (GAS u64*)(ws + (((pair * 4 + split) * 4 + peer) * 4 + w) * 128);
  const char* ahead = w == 0 ? (const char*)vc + (size_t)bid * WG_BYTES : w == 1 ? msgs
                    : w == 2 ? (const char*)out + (size_t)pair * 32 * ROWB : msgs + 64 * MSG - 64;
  const unsigned warm = __builtin_nontemporal_load((const GAS unsigned*)ahead + (lane & 1));
  unsigned in0 = __hip_atomic_load((GAS unsigned*)sig_in, RLX_AGENT);
  unsigned out1 = __hip_atomic_load((GAS unsigned*)sig_out, RLX_AGENT) + 1;

#pragma unroll
  for (int t = 0; t < NT / 8; ++t) { k_tile(NT / 2 + 4 * t + w, preg[t][0], preg[t][1]); PIN(preg[t][0]); PIN(preg[t][1]); }
  wait_vm<0>();
  asm volatile("" ::"v"(warm));
  flag_set(flags + F_PRDY + w * 4, 1);
  flag_wait4(flags + F_PRDY, 1);

  f32x16 o[2];
  float rcur = *(const LAS float*)refp;
  {
    const lds_cptr tv = (lds_cptr)lds + TV_OFF + ((lane & 15) >> 2) * ROWB + trofs;
    u32x2 pt2 = *(const LAS u32x2*)((lds_cptr)lds + PT_OFF + r * 8);
    if (h) pt2 = u32x2{0u, 0u};
    const bf16x8 pt = __builtin_bit_cast(bf16x8, u32x4{pt2[0], pt2[1], 0u, 0u});
#pragma unroll
    for (int cb = 0; cb < 2; ++cb) { const s16x4 v4 = ld_tr(tv + cb * 256); o[cb] = MFMA32(join(v4, v4), pt, f32x16{}); }
  }
  const float M = *(const LAS float*)(refp + NT * 128);
  *(LAS float*)((lds_ptr)lds + L_OFF + w * 128 + r * 4) = xhalf_sum(l) * __builtin_amdgcn_exp2f(m - M);
  slot_off = (NT % NSLOT) * SLOT;
  auto v_tiles = [&](int v0, int v1) {
#pragma unroll 1
    for (int v = v0; v < v1; ++v) {
      if (v == 32 || v == 48) flag_wait4(flags + F_PRDY, v >> 4);
      const float rnew = *(const LAS float*)(refp + (v + 1) * 128);
      bf16x8 p0 = ld_frag(pbuf + (v & (NT / 2 - 1)) * 2048), p1 = ld_frag(pbuf + (v & (NT / 2 - 1)) * 2048 + 1024);
      if (__any(rnew != rcur)) {
        const float alpha = __builtin_amdgcn_exp2f(rcur - rnew);
        o[0] *= alpha; o[1] *= alpha;
        rcur = rnew;
      }
      PIN(p0); PIN(p1);
      flag_wait(flags + F_LAND, NT + v + 1);
      const lds_cptr vfrag = (lds_cptr)lds + slot_off + vofs;
      bf16x8 vf[4];
#pragma unroll
      for (int i = 0; i < 4; ++i)
        vf[i] = join(ld_tr(vfrag + (i >> 1) * 512 + (i & 1) * 256), ld_tr(vfrag + 8 * VP + (i >> 1) * 512 + (i & 1) * 256));
      SBAR();
      o[0] = MFMA32(vf[0], p0, o[0]);
      o[1] = MFMA32(vf[1], p0, o[1]);
      SBAR();
      flag_set(flags + F_DONE + w * 4, NT + v + 1);
      slot_off = slot_off == (NSLOT - 1) * SLOT ? 0 : slot_off + SLOT;
      o[0] = MFMA32(vf[2], p1, o[0]);
      o[1] = MFMA32(vf[3], p1, o[1]);
    }
  };
  auto refill = [&](int j0) {
    flag_wait4(flags + F_DONE, NT + 16 + 4 * j0);
#pragma unroll
    for (int j = j0; j < j0 + 4; ++j) {
      *(LAS bf16x8*)(pbuf + (4 * j + w) * 2048) = preg[j][0];
      *(LAS bf16x8*)(pbuf + (4 * j + w) * 2048 + 1024) = preg[j][1];
    }
    flag_set(flags + F_PRDY + w * 4, 2 + j0 / 4);
  };
  v_tiles(0, 20);
  refill(0);
  v_tiles(20, 36);
  refill(4);

  float L = 0.f;
#pragma unroll
  for (int i = 0; i < NW; ++i) L += *(const LAS float*)((lds_cptr)lds + L_OFF + i * 128 + r * 4);
  unsigned zout[4];
#pragma unroll
  for (int j = 0; j < 4; ++j) { zout[j] = (j == split ? 64 : (j * 4 + split) * 4 + w) * MSG + voff; PIN(zout[j]); }
  char* orow = (char*)out + ((size_t)pair * 32 + qrow) * ROWB + split * 128 + w * 32 + h * 16;
  f32x4 mlw = *(const LAS f32x4*)((lds_cptr)lds + ((lane & 8) ? L_OFF : REF_OFF + NT * 128) + (lane & 7) * 16);
  if (lane & 8) {
#pragma unroll
    for (int i = 1; i < NW; ++i) mlw += *(const LAS f32x4*)((lds_cptr)lds + L_OFF + i * 128 + (lane & 7) * 16);
  }
  unsigned mlout = (lane < 48 ? (((split + 1 + (lane >> 4)) & 3) * 4 + split) * 4 + w : 64) * MSG + 1024 + (lane & 15) * 16;
  PIN(L); PIN(orow); PIN(mlw); PIN(mlout); PIN(in0); PIN(out1);
  v_tiles(36, NT);

  __builtin_amdgcn_raw_buffer_store_b128(__builtin_bit_cast(u32x4, mlw), sZ, mlout, 0, SC1);
#pragma unroll
  for (int j = 0; j < 4; ++j) {
    u32x4 x;
#pragma unroll
    for (int e = 0; e < 4; ++e) x[e] = pack2(o[j >> 1][8 * (j & 1) + 2 * e], o[j >> 1][8 * (j & 1) + 2 * e + 1]);
    __builtin_amdgcn_raw_buffer_store_b128(x, sZ, zout[j], 0, SC1);
  }
  unsigned got = __hip_atomic_load((GAS unsigned*)sig_in, RLX_AGENT);
  SBAR();
  float own[8];
#pragma unroll
  for (int e = 0; e < 8; ++e) {
    const float lo = (split & 1) ? o[0][8 + e] : o[0][e], hi = (split & 1) ? o[1][8 + e] : o[1][e];
    own[e] = (split & 2) ? hi : lo;
    PIN(own[e]);
  }
  wait_vm<0>();
  PIN(got);
  if (lane < 3) __hip_atomic_store(sig_out, ((u64)in0 << 32) | out1, RLX_AGENT);
  if (((unsigned)__ballot(got != in0) & 7u) != 7u) {
    int spin = 0;
#pragma unroll 1
    do got = __hip_atomic_load((GAS unsigned*)sig_in, RLX_AGENT);
    while (((unsigned)__ballot(got != in0) & 7u) != 7u && ++spin < MEM_SPINS);
  }
  __builtin_amdgcn_fence(__ATOMIC_ACQUIRE, "wavefront");
  asm volatile("" ::: "memory");
  u32x4 a[3];
  unsigned ml[3];
#pragma unroll
  for (int k = 0; k < 3; ++k) {
    const unsigned zin = ((split * 4 + ((split + 1 + k) & 3)) * 4 + w) * MSG;
    ml[k] = __builtin_amdgcn_raw_buffer_load_b32(sZ, 1024 + lane * 4, zin, SC1);
    a[k] = __builtin_amdgcn_raw_buffer_load_b128(sZ, voff, zin, SC1);
  }
  const unsigned peer_view = __hip_atomic_load((GAS unsigned*)sig_in + 1, RLX_AGENT);
  {
    float mk[3], lk[3], wk[3], G = M;
#pragma unroll
    for (int k = 0; k < 3; ++k) {
      auto x = __builtin_amdgcn_permlane32_swap(ml[k], ml[k], false, false);
      mk[k] = __uint_as_float(x[0]); lk[k] = __uint_as_float(x[1]);
      G = fmaxf(G, mk[k]);
    }
    const float w0 = __builtin_amdgcn_exp2f(M - G);
    float den = w0 * L;
#pragma unroll
    for (int k = 0; k < 3; ++k) { wk[k] = __builtin_amdgcn_exp2f(mk[k] - G); den = fmaf(wk[k], lk[k], den); }
    const float inv = 1.0f / den;
#pragma unroll
    for (int e = 0; e < 8; ++e) own[e] *= w0;
#pragma unroll
    for (int k = 0; k < 3; ++k)
#pragma unroll
      for (int e = 0; e < 4; ++e) {
        own[2 * e] = fmaf(wk[k], __uint_as_float(a[k][e] << 16), own[2 * e]);
        own[2 * e + 1] = fmaf(wk[k], __uint_as_float(a[k][e] & 0xffff0000u), own[2 * e + 1]);
      }
#pragma unroll
    for (int e = 0; e < 8; ++e) own[e] *= inv;
  }
  auto x = __builtin_amdgcn_permlane32_swap(pack2(own[0], own[1]), pack2(own[4], own[5]), false, false);
  auto y = __builtin_amdgcn_permlane32_swap(pack2(own[2], own[3]), pack2(own[6], own[7]), false, false);
  *(GAS u32x4*)orow = u32x4{x[0], y[0], x[1], y[1]};
  if (lane < 3 && peer_view == out1) __hip_atomic_store(sig_out, ((u64)in0 << 32) | (out1 + 1), RLX_AGENT);
}

}

extern "C" void kernel_launch(void* const* d_in, const int* in_sizes, int n_in, void* d_out, int out_size, void* d_ws,
                              size_t ws_size, hipStream_t stream) {
  using namespace ta;
  (void)in_sizes; (void)n_in; (void)out_size; (void)ws_size;
  (void)hipFuncSetAttribute((const void*)tree_attn, hipFuncAttributeMaxDynamicSharedMemorySize, LDS_BYTES);
  hipLaunchKernelGGL(tree_attn, dim3(NPAIR * NSPLIT), dim3((NW + 1) * 64), LDS_BYTES, stream,
                     (const __hip_bfloat16*)d_in[0], (const __hip_bfloat16*)d_in[1], (const __hip_bfloat16*)d_in[2],
                     (const __hip_bfloat16*)d_in[3], (const __hip_bfloat16*)d_in[4], (const __hip_bfloat16*)d_in[5],
                     (char*)d_ws, (__hip_bfloat16*)d_out);
}
```

```cpp
#include <hip/hip_runtime.h>
#include <hip/hip_bf16.h>

namespace ta {

constexpr int NB = 8, HKV = 8, CTX = 8192, HD = 256;
constexpr int NPAIR = NB * HKV, NSPLIT = 4, NW = 4;
constexpr int ROWB = HD * 2;
constexpr int WG_BYTES = CTX / NSPLIT * ROWB;
constexpr int NT = CTX / NSPLIT / 32;
constexpr int KP = 1040, VP = 1056;
constexpr int SLOT = 16 * VP, NSLOT = 5;
constexpr int P_OFF = NSLOT * SLOT;
constexpr int REF_OFF = P_OFF + NT / 2 * 2048;
constexpr int TV_OFF = REF_OFF + (NT + 1) * 128;
constexpr int PT_OFF = TV_OFF + 4 * ROWB;
constexpr int L_OFF = PT_OFF + 256;
constexpr int FLAG_OFF = L_OFF + NW * 128;
constexpr int F_DONE = 0, F_PRDY = 16, F_LAND = 32, F_REF = 36, F_Q = 40;
constexpr int LDS_BYTES = FLAG_OFF + 48;
constexpr int TK_OFF = REF_OFF - 4 * ROWB, Q_OFF = TK_OFF - 16 * KP, MK_OFF = Q_OFF - 2048;
constexpr float SCALE = 0.0625f, LOG2E = 1.4426950408889634f, C2 = SCALE * LOG2E;
constexpr int WIN = 20, WSTEP = 2, TAU_NS = 540, OFF_NS = 400, LSTEP_NS = 700, KNEE = 4, START_C = 5, START_NS = 2900;
constexpr int MSG = 1024 + 256;
constexpr int CTRL_BYTES = NPAIR * 64 * 128;
constexpr int LDS_SPINS = 1 << 20, MEM_SPINS = 1 << 21;
static_assert(LDS_BYTES <= 160 * 1024 && MK_OFF >= P_OFF + 16 * 2048 && FLAG_OFF % 16 == 0, "LDS map");
static_assert(WIN >= 17 && WIN + 7 * WSTEP <= 48, "publish / drain ladder assumes 1..3 chunks in flight");

typedef __bf16 bf16x8 __attribute__((ext_vector_type(8)));
typedef __bf16 bf16x2 __attribute__((ext_vector_type(2)));
typedef short s16x4 __attribute__((ext_vector_type(4)));
typedef float f32x16 __attribute__((ext_vector_type(16)));
typedef float f32x4 __attribute__((ext_vector_type(4)));
typedef unsigned u32x4 __attribute__((ext_vector_type(4)));
typedef unsigned u32x2 __attribute__((ext_vector_type(2)));
typedef unsigned long long u64;
#define LAS __attribute__((address_space(3)))
#define GAS __attribute__((address_space(1)))
typedef LAS const char* lds_cptr;
typedef LAS char* lds_ptr;
typedef __amdgpu_buffer_rsrc_t srd_t;

#define SBAR() __builtin_amdgcn_sched_barrier(0)
#define PIN(x) asm volatile("" : "+v"(x))
#define MFMA32(a, b, c) __builtin_amdgcn_mfma_f32_32x32x16_bf16((a), (b), (c), 0, 0, 0)
#define RLX_AGENT __ATOMIC_RELAXED, __HIP_MEMORY_SCOPE_AGENT
#define RFL(x) __builtin_amdgcn_readfirstlane(x)
constexpr int SC1 = 16;

__device__ __forceinline__ srd_t make_srd(const void* p, int bytes) {
  return __builtin_amdgcn_make_buffer_rsrc((void*)p, (short)0, bytes, 0x00020000);
}

template <int PAD>
__device__ __forceinline__ void dma4(srd_t srd, unsigned voff, unsigned soff, unsigned ldsb) {
  unsigned keep;
  asm volatile(
      "s_mov_b32 %0, m0\n\ts_mov_b32 m0, %4\n\ts_nop 3\n\t"
      "buffer_load_dwordx4 %1, %2, %3 offen nt lds\n\t"
      "s_add_u32 m0, %4, %5\n\ts_nop 0\n\t"
      "buffer_load_dwordx4 %1, %2, %3 offen offset:1024 nt lds\n\t"
      "s_add_u32 m0, %4, %6\n\ts_nop 0\n\t"
      "buffer_load_dwordx4 %1, %2, %3 offen offset:2048 nt lds\n\t"
      "s_add_u32 m0, %4, %7\n\ts_nop 0\n\t"
      "buffer_load_dwordx4 %1, %2, %3 offen offset:3072 nt lds\n\t"
      "s_mov_b32 m0, %0"
      : "=&s"(keep)
      : "v"(voff), "s"(srd), "s"(soff), "s"(ldsb), "n"(PAD), "n"(2 * PAD), "n"(3 * PAD)
      : "memory", "scc");
}
#define TA_PIECE(I, OFS, STEP) \
      "s_waitcnt vmcnt(%[n])\n\t" \
      "buffer_load_dwordx4 %[vo], %[srd], %[so] offen " OFS " nt lds\n\t" \
      "s_add_u32 m0, m0, " STEP "\n\t" \
      ".if (%[n] & 15) == " #I "\n\tds_write_b32 %[la], %[lv]\n\t.endif\n\t"
#define TA_GROUP(I0, I1, I2, I3) \
      TA_PIECE(I0, "", "%[pad]") TA_PIECE(I1, "offset:1024", "%[pad]") TA_PIECE(I2, "offset:2048", "%[pad]") \
      TA_PIECE(I3, "offset:3072", "%[padg]") "s_add_u32 %[so], %[so], 0x1000\n\t"
template <int N>
__device__ __forceinline__ void dma_chunk(srd_t srd, unsigned voff, unsigned soff, unsigned ldsb, unsigned pad,
                                          unsigned land_addr, int c, unsigned done_addr, u32x4& done, u64& now) {
  unsigned keep;
  asm volatile(
      "s_mov_b32 %[keep], m0\n\ts_mov_b32 m0, %[lds]\n\ts_nop 2\n\t"
      TA_GROUP(0, 1, 2, 3) TA_GROUP(4, 5, 6, 7) TA_GROUP(8, 9, 10, 11)
      "ds_read_b128 %[done], %[da]\n\ts_memrealtime %[now]\n\t"
      TA_GROUP(12, 13, 14, 15)
      "s_mov_b32 m0, %[keep]\n\ts_waitcnt lgkmcnt(0)"
      : [keep] "=&s"(keep), [so] "+&s"(soff), [done] "=&v"(done), [now] "=&s"(now)
      : [vo] "v"(voff), [srd] "s"(srd), [lds] "s"(ldsb), [pad] "s"(pad), [padg] "s"(pad + 4096), [n] "n"(N),
        [la] "v"(land_addr), [lv] "v"(c - N / 16), [da] "v"(done_addr)
      : "memory", "scc");
}
__device__ __forceinline__ void dma2(srd_t srd, unsigned voff, unsigned soff, unsigned ldsb) {
  unsigned keep;
  asm volatile("s_mov_b32 %0, m0\n\ts_mov_b32 m0, %4\n\ts_nop 3\n\t"
               "buffer_load_dwordx4 %1, %2, %3 offen nt lds\n\t"
               "buffer_load_dwordx4 %1, %2, %3 offen offset:1024 nt lds\n\t"
               "s_mov_b32 m0, %0"
               : "=&s"(keep) : "v"(voff), "s"(srd), "s"(soff), "s"(ldsb) : "memory");
}
template <int N> __device__ __forceinline__ void wait_vm() { asm volatile("s_waitcnt vmcnt(%0)" ::"n"(N) : "memory"); }

__device__ __forceinline__ bf16x8 ld_frag(lds_cptr p) { return *(const LAS bf16x8*)p; }
__device__ __forceinline__ s16x4 ld_tr(lds_cptr p) { return __builtin_amdgcn_ds_read_tr16_b64_v4i16((LAS s16x4*)p); }
__device__ __forceinline__ bf16x8 join(s16x4 lo, s16x4 hi) {
  return __builtin_bit_cast(bf16x8, __builtin_shufflevector(lo, hi, 0, 1, 2, 3, 4, 5, 6, 7));
}
__device__ __forceinline__ float xhalf_max(float x) {
  auto r = __builtin_amdgcn_permlane32_swap(__float_as_uint(x), __float_as_uint(x), false, false);
  return fmaxf(__uint_as_float(r[0]), __uint_as_float(r[1]));
}
__device__ __forceinline__ float xhalf_sum(float x) {
  auto r = __builtin_amdgcn_permlane32_swap(__float_as_uint(x), __float_as_uint(x), false, false);
  return __uint_as_float(r[0]) + __uint_as_float(r[1]);
}
__device__ __forceinline__ unsigned pack2(float a, float b) {
  const bf16x2 v = {(__bf16)a, (__bf16)b};
  return __builtin_bit_cast(unsigned, v);
}
__device__ __forceinline__ bf16x8 pack8(const f32x16& p, int o) {
  bf16x8 r;
#pragma unroll
  for (int j = 0; j < 8; ++j) r[j] = (__bf16)p[o + j];
  return r;
}
__device__ __forceinline__ void flag_set(unsigned addr, unsigned v) {
  asm volatile("s_waitcnt lgkmcnt(0)\n\tds_write_b32 %0, %1" ::"v"(addr), "v"(v) : "memory");
}
__device__ __forceinline__ void flag_wait(unsigned addr, int need) {
#pragma unroll 1
  for (int spin = 0; spin < LDS_SPINS; ++spin) {
    unsigned v;
    asm volatile("ds_read_b32 %0, %1\n\ts_waitcnt lgkmcnt(0)" : "=v"(v) : "v"(addr) : "memory");
    if ((int)RFL(v) >= need) break;
    __builtin_amdgcn_s_sleep(1);
  }
}
__device__ __forceinline__ void flag_wait4(unsigned addr, int need) {
#pragma unroll 1
  for (int spin = 0; spin < LDS_SPINS; ++spin) {
    u32x4 v;
    asm volatile("ds_read_b128 %0, %1\n\ts_waitcnt lgkmcnt(0)" : "=v"(v) : "v"(addr) : "memory");
    if ((int)RFL(min(min(v[0], v[1]), min(v[2], v[3]))) >= need) break;
    __builtin_amdgcn_s_sleep(1);
  }
}

__global__ __launch_bounds__((NW + 1) * 64, 2) void tree_attn(
    const __hip_bfloat16* __restrict__ q, const __hip_bfloat16* __restrict__ kc, const __hip_bfloat16* __restrict__ vc,
    const __hip_bfloat16* __restrict__ kt, const __hip_bfloat16* __restrict__ vt, const __hip_bfloat16* __restrict__ mask,
    char* ws, __hip_bfloat16* __restrict__ out) {
  extern __shared__ __attribute__((aligned(16))) char lds[];
  u64 kbase, base2, now, pc;
  unsigned d0, d1;
  const int wid = RFL(threadIdx.x >> 6);
  asm volatile("s_load_dwordx2 %[kb], %[ka], 0x8\n\t"
               "s_load_dwordx2 %[b2], %[ka], %[sel]\n\t"
               "s_getpc_b64 %[pc]\n\t"
               "s_load_dword %[d0], %[pc], %[line]\n\t"
               "s_load_dword %[d1], %[pc], %[line] offset:64\n\t"
               "s_memrealtime %[now]"
               : [kb] "=&s"(kbase), [b2] "=&s"(base2), [pc] "=&s"(pc), [d0] "=&s"(d0), [d1] "=&s"(d1), [now] "=&s"(now)
               : [ka] "s"(__builtin_amdgcn_kernarg_segment_ptr()), [sel] "s"((wid >> 2) * 8),
                 [line] "s"(((blockIdx.x >> 3) * (NW + 1) + wid) * 128)
               : "memory");
  SBAR();
  const unsigned bid = (blockIdx.x & 7) * 32 + (blockIdx.x >> 3);
  const int lane = threadIdx.x & 63;
  const unsigned lds0 = (unsigned)(uintptr_t)lds;
  const unsigned voff = lane * 16;
  const int pair = bid >> 2, split = bid & 3;
  {
    unsigned keep;
#define TA_G4(VO, BASE) \
        "global_load_lds_dwordx4 " VO ", " BASE " nt\n\ts_add_u32 m0, m0, %[pad]\n\ts_nop 0\n\t" \
        "global_load_lds_dwordx4 " VO ", " BASE " offset:1024 nt\n\ts_add_u32 m0, m0, %[pad]\n\ts_nop 0\n\t" \
        "global_load_lds_dwordx4 " VO ", " BASE " offset:2048 nt\n\ts_add_u32 m0, m0, %[pad]\n\ts_nop 0\n\t" \
        "global_load_lds_dwordx4 " VO ", " BASE " offset:3072 nt\n\t"
    asm volatile(
        "s_mov_b32 %[keep], m0\n\ts_mov_b32 m0, %[dst2]\n\t"
        "s_waitcnt lgkmcnt(0)\n\t"
        TA_G4("%[vo2]", "%[b2]")
        "s_mov_b32 m0, %[dst]\n\ts_nop 0\n\t"
        TA_G4("%[vo]", "%[kb]")
        "s_mov_b32 m0, %[keep]"
        : [keep] "=&s"(keep), [kb] "+s"(kbase), [b2] "+s"(base2), [now] "+s"(now), [d0] "+s"(d0), [d1] "+s"(d1)
        : [vo] "v"(bid * WG_BYTES + wid * 4096 + voff),
          [dst] "s"(lds0 + wid * 4 * KP + (wid >> 2) * (SLOT - 16 * KP)),
          [vo2] "v"((wid == NW ? bid * WG_BYTES + 5 * 4096 : pair * 32 * ROWB + wid * 4096) + voff),
          [dst2] "s"(lds0 + (wid == NW ? SLOT + 4 * KP : Q_OFF + wid * 4 * KP)), [pad] "n"(KP - 1024)
        : "memory", "scc");
#undef TA_G4
  }
  const unsigned flags = lds0 + FLAG_OFF;
  if (threadIdx.x < 12) flag_set(flags + lane * 4, lane < 4 ? lane : lane == F_LAND / 4 ? 1 : 0);
  asm volatile("s_waitcnt lgkmcnt(0)\n\ts_barrier" ::: "memory");

  if (wid == NW) {
    __builtin_amdgcn_s_setprio(3);
    const srd_t sK = make_srd((const char*)kbase + (size_t)bid * WG_BYTES, WG_BYTES);
#pragma unroll 1
    for (int g = 2; g < 4; ++g) dma4<KP - 1024>(sK, voff, 16384 + g * 4096, lds0 + SLOT + g * 4 * KP);
    const srd_t sV = make_srd((const char*)vc + (size_t)bid * WG_BYTES, WG_BYTES);
    unsigned t0 = (unsigned)now + OFF_NS / 10;
    u32x4 done = {0u, 0u, 0u, 0u};
    unsigned dst = lds0 + 2 * SLOT;
#pragma unroll 1
    for (int c = 2; c < 2 * NT; ++c) {
      if (c >= NSLOT && (int)RFL(min(min(done[0], done[1]), min(done[2], done[3]))) < c - NSLOT + 1)
        flag_wait4(flags + F_DONE, c - NSLOT + 1);
      if (c == START_C) t0 += max((int)(RFL((unsigned)now) - t0) - (START_NS - OFF_NS) / 10, 0) * 3 / 4;
      const int lag = min((int)(RFL((unsigned)now) - t0) - (((4 * c - 1) * (TAU_NS * 64 / 10)) >> 8), 1 << 16);
      int k = lag <= 0 ? 0 : (lag * (655360 / LSTEP_NS)) >> 16;
      if (k > KNEE) k = KNEE + (k - KNEE) / 2;
      const srd_t srd = c < NT ? sK : sV;
      const unsigned soff = (c & (NT - 1)) * 16384, pad = c < NT ? KP - 1024 : VP - 1024;
#define TA_CH(K) dma_chunk<WIN - 1 + (K) * WSTEP>(srd, voff, soff, dst, pad, flags + F_LAND, c, flags + F_DONE, done, now)
      if (k < 4) {
        if (k < 2) { if (k == 0) TA_CH(0); else TA_CH(1); } else { if (k == 2) TA_CH(2); else TA_CH(3); }
      } else {
        if (k < 6) { if (k == 4) TA_CH(4); else TA_CH(5); } else { if (k == 6) TA_CH(6); else TA_CH(7); }
      }
#undef TA_CH
      dst = dst == lds0 + (NSLOT - 1) * SLOT ? lds0 : dst + SLOT;
    }
    wait_vm<32>(); flag_set(flags + F_LAND, 2 * NT - 2);
    wait_vm<16>(); flag_set(flags + F_LAND, 2 * NT - 1);
    wait_vm<0>(); flag_set(flags + F_LAND, 2 * NT);
    return;
  }

  const int w = wid;
  const int r = lane & 31, h = lane >> 5;
  if (w == 3) {
    dma2(make_srd((const char*)kt + (size_t)pair * 16 * ROWB, 16 * ROWB), voff, split * 4 * ROWB, lds0 + TK_OFF);
    dma2(make_srd((const char*)mask + (pair >> 3) * 512, 512), voff, 0, lds0 + MK_OFF);
  }
  if (w == 2) dma2(make_srd((const char*)vt + (size_t)pair * 16 * ROWB, 16 * ROWB), voff, split * 4 * ROWB, lds0 + TV_OFF);
  const int qrow = 2 * (r & 15) + (r >> 4);
  const int koff = (r & 15) * KP + (r >> 4) * 512 + h * 16;
  const int trofs = ((lane >> 4) & 1) * 128 + w * 32 + (lane & 3) * 8;
  const int vofs = (4 * h + ((lane & 15) >> 2)) * VP + trofs;
  if (w == 3) wait_vm<4>(); else if (w == 2) wait_vm<2>(); else wait_vm<0>();
  if (lane == 0) asm volatile("ds_add_u32 %0, %1" ::"v"(flags + F_Q), "v"(1u) : "memory");
  flag_wait(flags + F_Q, NW);
  bf16x8 qf[16];
#pragma unroll
  for (int kk = 0; kk < 16; ++kk) qf[kk] = ld_frag((lds_cptr)lds + Q_OFF + koff + kk * 32);

  const lds_ptr refp = (lds_ptr)lds + REF_OFF + r * 4;
  float m = -INFINITY, l = 0.f;
  if (w == 3) {
    wait_vm<0>();
    const u32x2 mbits = *(const LAS u32x2*)((lds_cptr)lds + MK_OFF + (qrow & 15) * 32 + split * 8);
    const lds_cptr tk = (lds_cptr)lds + TK_OFF + (r & 3) * ROWB + h * 16;
    f32x16 s = f32x16{};
#pragma unroll
    for (int kk = 0; kk < 16; ++kk) s = MFMA32(ld_frag(tk + kk * 32), qf[kk], s);
#pragma unroll
    for (int i = 0; i < 4; ++i) {
      const float mk = __uint_as_float((i & 1) ? (mbits[i >> 1] & 0xffff0000u) : (mbits[i >> 1] << 16));
      s[i] = fmaf(s[i], SCALE, (1.0f - mk) * -1000000000.0f) * LOG2E;
      m = fmaxf(m, s[i]);
    }
#pragma unroll
    for (int i = 0; i < 4; ++i) { s[i] = __builtin_amdgcn_exp2f(s[i] - m); l += s[i]; }
    if (h) l = 0.f;
    *(LAS float*)refp = m;
    *(LAS u32x2*)((lds_ptr)lds + PT_OFF + r * 8) = u32x2{pack2(s[0], s[1]), pack2(s[2], s[3])};
    flag_set(flags + F_REF, 1);
  }

  int slot_off = w * SLOT;
  const lds_ptr pbuf = (lds_ptr)lds + P_OFF + lane * 16;
  bf16x8 preg[NT / 8][2];

  auto k_tile = [&](int c, bf16x8& p0, bf16x8& p1) {
    flag_wait(flags + F_LAND, c + 1);
    const lds_cptr kfrag = (lds_cptr)lds + slot_off + koff;
    f32x16 s = f32x16{};
    bf16x8 kf[8];
#pragma unroll
    for (int kk = 0; kk < 8; ++kk) kf[kk] = ld_frag(kfrag + kk * 32);
    SBAR();
#pragma unroll
    for (int kk = 0; kk < 8; ++kk) { s = MFMA32(kf[kk], qf[kk], s); kf[kk] = ld_frag(kfrag + (kk + 8) * 32); SBAR(); }
    flag_set(flags + F_DONE + w * 4, min(c + NW, NT));
    slot_off = slot_off == 0 ? (NSLOT - 1) * SLOT : slot_off - SLOT;
    SBAR();
#pragma unroll
    for (int kk = 0; kk < 8; ++kk) s = MFMA32(kf[kk], qf[kk + 8], s);
    float tmax = fmaxf(s[0], s[1]);
#pragma unroll
    for (int i = 2; i < 16; ++i) tmax = fmaxf(tmax, s[i]);
    tmax = xhalf_max(tmax) * C2;
    PIN(tmax);
    flag_wait(flags + F_REF, c + 1);
    const float mn = fmaxf(*(const LAS float*)(refp + c * 128), tmax);
    *(LAS float*)(refp + (c + 1) * 128) = mn;
    flag_set(flags + F_REF, c + 2);
    const float alpha = __builtin_amdgcn_exp2f(m - mn);
    m = mn;
    float ps = 0.f;
#pragma unroll
    for (int i = 0; i < 16; ++i) { s[i] = __builtin_amdgcn_exp2f(fmaf(s[i], C2, -mn)); ps += s[i]; }
    l = fmaf(l, alpha, ps);
    p0 = pack8(s, 0); p1 = pack8(s, 8);
    PIN(l); PIN(m);
  };
#pragma unroll 1
  for (int c = w; c < NT / 2; c += NW) {
    bf16x8 p0, p1;
    k_tile(c, p0, p1);
    *(LAS bf16x8*)(pbuf + c * 2048) = p0;
    *(LAS bf16x8*)(pbuf + c * 2048 + 1024) = p1;
  }

  char* msgs = ws + CTRL_BYTES + (size_t)pair * 64 * MSG;
  const srd_t sZ = make_srd(msgs, 64 * MSG);
  const int peer = (split + 1 + (lane & 3) % 3) & 3;
  GAS u64* sig_in = (GAS u64*)(ws + (((pair * 4 + peer) * 4 + split) * 4 + w) * 128);
  GAS u64* sig_out = (GAS u64*)(ws + (((pair * 4 + split) * 4 + peer) * 4 + w) * 128);
  const char* ahead = w == 0 ? (const char*)vc + (size_t)bid * WG_BYTES : w == 1 ? msgs
                    : w == 2 ? (const char*)out + (size_t)pair * 32 * ROWB : msgs + 64 * MSG - 64;
  const unsigned warm = __builtin_nontemporal_load((const GAS unsigned*)ahead + (lane & 1));
  unsigned in0 = __hip_atomic_load((GAS unsigned*)sig_in, RLX_AGENT);
  unsigned out1 = __hip_atomic_load((GAS unsigned*)sig_out, RLX_AGENT) + 1;

#pragma unroll
  for (int t = 0; t < NT / 8; ++t) { k_tile(NT / 2 + 4 * t + w, preg[t][0], preg[t][1]); PIN(preg[t][0]); PIN(preg[t][1]); }
  wait_vm<0>();
  asm volatile("" ::"v"(warm));
  flag_set(flags + F_PRDY + w * 4, 1);
  flag_wait4(flags + F_PRDY, 1);

  f32x16 o[2];
  float rcur = *(const LAS float*)refp;
  {
    const lds_cptr tv = (lds_cptr)lds + TV_OFF + ((lane & 15) >> 2) * ROWB + trofs;
    u32x2 pt2 = *(const LAS u32x2*)((lds_cptr)lds + PT_OFF + r * 8);
    if (h) pt2 = u32x2{0u, 0u};
    const bf16x8 pt = __builtin_bit_cast(bf16x8, u32x4{pt2[0], pt2[1], 0u, 0u});
#pragma unroll
    for (int cb = 0; cb < 2; ++cb) { const s16x4 v4 = ld_tr(tv + cb * 256); o[cb] = MFMA32(join(v4, v4), pt, f32x16{}); }
  }
  const float M = *(const LAS float*)(refp + NT * 128);
  *(LAS float*)((lds_ptr)lds + L_OFF + w * 128 + r * 4) = xhalf_sum(l) * __builtin_amdgcn_exp2f(m - M);
  slot_off = (NT % NSLOT) * SLOT;
  auto v_tiles = [&](int v0, int v1) {
#pragma unroll 1
    for (int v = v0; v < v1; ++v) {
      if (v == 32 || v == 48) flag_wait4(flags + F_PRDY, v >> 4);
      const float rnew = *(const LAS float*)(refp + (v + 1) * 128);
      bf16x8 p0 = ld_frag(pbuf + (v & (NT / 2 - 1)) * 2048), p1 = ld_frag(pbuf + (v & (NT / 2 - 1)) * 2048 + 1024);
      if (__any(rnew != rcur)) {
        const float alpha = __builtin_amdgcn_exp2f(rcur - rnew);
        o[0] *= alpha; o[1] *= alpha;
        rcur = rnew;
      }
      PIN(p0); PIN(p1);
      flag_wait(flags + F_LAND, NT + v + 1);
      const lds_cptr vfrag = (lds_cptr)lds + slot_off + vofs;
      bf16x8 vf[4];
#pragma unroll
      for (int i = 0; i < 4; ++i)
        vf[i] = join(ld_tr(vfrag + (i >> 1) * 512 + (i & 1) * 256), ld_tr(vfrag + 8 * VP + (i >> 1) * 512 + (i & 1) * 256));
      SBAR();
      o[0] = MFMA32(vf[0], p0, o[0]);
      o[1] = MFMA32(vf[1], p0, o[1]);
      SBAR();
      flag_set(flags + F_DONE + w * 4, NT + v + 1);
      slot_off = slot_off == (NSLOT - 1) * SLOT ? 0 : slot_off + SLOT;
      o[0] = MFMA32(vf[2], p1, o[0]);
      o[1] = MFMA32(vf[3], p1, o[1]);
    }
  };
  auto refill = [&](int j0) {
    flag_wait4(flags + F_DONE, NT + 16 + 4 * j0);
#pragma unroll
    for (int j = j0; j < j0 + 4; ++j) {
      *(LAS bf16x8*)(pbuf + (4 * j + w) * 2048) = preg[j][0];
      *(LAS bf16x8*)(pbuf + (4 * j + w) * 2048 + 1024) = preg[j][1];
    }
    flag_set(flags + F_PRDY + w * 4, 2 + j0 / 4);
  };
  v_tiles(0, 20);
  refill(0);
  v_tiles(20, 36);
  refill(4);

  float L = 0.f;
#pragma unroll
  for (int i = 0; i < NW; ++i) L += *(const LAS float*)((lds_cptr)lds + L_OFF + i * 128 + r * 4);
  unsigned zout[4];
#pragma unroll
  for (int j = 0; j < 4; ++j) { zout[j] = (j == split ? 64 : (j * 4 + split) * 4 + w) * MSG + voff; PIN(zout[j]); }
  char* orow = (char*)out + ((size_t)pair * 32 + qrow) * ROWB + split * 128 + w * 32 + h * 16;
  f32x4 mlw = *(const LAS f32x4*)((lds_cptr)lds + ((lane & 8) ? L_OFF : REF_OFF + NT * 128) + (lane & 7) * 16);
  if (lane & 8) {
#pragma unroll
    for (int i = 1; i < NW; ++i) mlw += *(const LAS f32x4*)((lds_cptr)lds + L_OFF + i * 128 + (lane & 7) * 16);
  }
  unsigned mlout = (lane < 48 ? (((split + 1 + (lane >> 4)) & 3) * 4 + split) * 4 + w : 64) * MSG + 1024 + (lane & 15) * 16;
  PIN(L); PIN(orow); PIN(mlw); PIN(mlout); PIN(in0); PIN(out1);
  v_tiles(36, NT);

  __builtin_amdgcn_raw_buffer_store_b128(__builtin_bit_cast(u32x4, mlw), sZ, mlout, 0, SC1);
#pragma unroll
  for (int j = 0; j < 4; ++j) {
    u32x4 x;
#pragma unroll
    for (int e = 0; e < 4; ++e) x[e] = pack2(o[j >> 1][8 * (j & 1) + 2 * e], o[j >> 1][8 * (j & 1) + 2 * e + 1]);
    __builtin_amdgcn_raw_buffer_store_b128(x, sZ, zout[j], 0, SC1);
  }
  unsigned got = __hip_atomic_load((GAS unsigned*)sig_in, RLX_AGENT);
  SBAR();
  float own[8];
#pragma unroll
  for (int e = 0; e < 8; ++e) {
    const float lo = (split & 1) ? o[0][8 + e] : o[0][e], hi = (split & 1) ? o[1][8 + e] : o[1][e];
    own[e] = (split & 2) ? hi : lo;
    PIN(own[e]);
  }
  wait_vm<0>();
  PIN(got);
  if (lane < 3) __hip_atomic_store(sig_out, ((u64)in0 << 32) | out1, RLX_AGENT);
  if (((unsigned)__ballot(got != in0) & 7u) != 7u) {
    int spin = 0;
#pragma unroll 1
    do got = __hip_atomic_load((GAS unsigned*)sig_in, RLX_AGENT);
    while (((unsigned)__ballot(got != in0) & 7u) != 7u && ++spin < MEM_SPINS);
  }
  __builtin_amdgcn_fence(__ATOMIC_ACQUIRE, "wavefront");
  asm volatile("" ::: "memory");
  u32x4 a[3];
  unsigned ml[3];
#pragma unroll
  for (int k = 0; k < 3; ++k) {
    const unsigned zin = ((split * 4 + ((split + 1 + k) & 3)) * 4 + w) * MSG;
    ml[k] = __builtin_amdgcn_raw_buffer_load_b32(sZ, 1024 + lane * 4, zin, SC1);
    a[k] = __builtin_amdgcn_raw_buffer_load_b128(sZ, voff, zin, SC1);
  }
  const unsigned peer_view = __hip_atomic_load((GAS unsigned*)sig_in + 1, RLX_AGENT);
  {
    float mk[3], lk[3], wk[3], G = M;
#pragma unroll
    for (int k = 0; k < 3; ++k) {
      auto x = __builtin_amdgcn_permlane32_swap(ml[k], ml[k], false, false);
      mk[k] = __uint_as_float(x[0]); lk[k] = __uint_as_float(x[1]);
      G = fmaxf(G, mk[k]);
    }
    const float w0 = __builtin_amdgcn_exp2f(M - G);
    float den = w0 * L;
#pragma unroll
    for (int k = 0; k < 3; ++k) { wk[k] = __builtin_amdgcn_exp2f(mk[k] - G); den = fmaf(wk[k], lk[k], den); }
    const float inv = 1.0f / den;
#pragma unroll
    for (int e = 0; e < 8; ++e) own[e] *= w0;
#pragma unroll
    for (int k = 0; k < 3; ++k)
#pragma unroll
      for (int e = 0; e < 4; ++e) {
        own[2 * e] = fmaf(wk[k], __uint_as_float(a[k][e] << 16), own[2 * e]);
        own[2 * e + 1] = fmaf(wk[k], __uint_as_float(a[k][e] & 0xffff0000u), own[2 * e + 1]);
      }
#pragma unroll
    for (int e = 0; e < 8; ++e) own[e] *= inv;
  }
  auto x = __builtin_amdgcn_permlane32_swap(pack2(own[0], own[1]), pack2(own[4], own[5]), false, false);
  auto y = __builtin_amdgcn_permlane32_swap(pack2(own[2], own[3]), pack2(own[6], own[7]), false, false);
  *(GAS u32x4*)orow = u32x4{x[0], y[0], x[1], y[1]};
  if (lane < 3 && peer_view == out1) __hip_atomic_store(sig_out, ((u64)in0 << 32) | (out1 + 1), RLX_AGENT);
}

}

extern "C" void kernel_launch(void* const* d_in, const int* in_sizes, int n_in, void* d_out, int out_size, void* d_ws,
                              size_t ws_size, hipStream_t stream) {
  using namespace ta;
  (void)in_sizes; (void)n_in; (void)out_size; (void)ws_size;
  (void)hipFuncSetAttribute((const void*)tree_attn, hipFuncAttributeMaxDynamicSharedMemorySize, LDS_BYTES);
  hipLaunchKernelGGL(tree_attn, dim3(NPAIR * NSPLIT), dim3((NW + 1) * 64), LDS_BYTES, stream,
                     (const __hip_bfloat16*)d_in[0], (const __hip_bfloat16*)d_in[1], (const __hip_bfloat16*)d_in[2],
                     (const __hip_bfloat16*)d_in[3], (const __hip_bfloat16*)d_in[4], (const __hip_bfloat16*)d_in[5],
                     (char*)d_ws, (__hip_bfloat16*)d_out);
}
```

```cpp
#include <hip/hip_runtime.h>
#include <hip/hip_bf16.h>

#define TA_NOWN 27
#define TA_SIGAT 1
#define TA_ET 12
#define TA_TABA 0x6666666666666654ull
#define TA_TAB0 0x1111111111111110ull
#define TA_POLLAT 2
#define TA_SET 1
#define TA_TAU 540
#define TA_OFF 400
#define TA_START 2750
#define TA_TAB 0x6666666655443210ull
namespace ta {

constexpr int NB = 8, HKV = 8, CTX = 8192, HD = 256;
constexpr int NPAIR = NB * HKV, NSPLIT = 4, NW = 4;
constexpr int ROWB = HD * 2;
constexpr int WG_BYTES = CTX / NSPLIT * ROWB;
constexpr int NT = CTX / NSPLIT / 32;
constexpr int KP = 1040, VP = 1056;
constexpr int SLOT = 16 * VP, NSLOT = 5;
constexpr int P_OFF = NSLOT * SLOT;
constexpr int REF_OFF = P_OFF + NT / 2 * 2048;
constexpr int TV_OFF = REF_OFF + (NT + 1) * 128;
constexpr int PT_OFF = TV_OFF + 4 * ROWB;
constexpr int L_OFF = PT_OFF + 256;
constexpr int FLAG_OFF = L_OFF + NW * 128;
constexpr int F_DONE = 0, F_PRDY = 16, F_LAND = 32, F_REF = 36, F_Q = 40;
constexpr int LDS_BYTES = FLAG_OFF + 48;
constexpr int TK_OFF = REF_OFF - 4 * ROWB, Q_OFF = TK_OFF - 16 * KP, MK_OFF = Q_OFF - 2048;
constexpr float SCALE = 0.0625f, LOG2E = 1.4426950408889634f, C2 = SCALE * LOG2E;
constexpr int TAU_NS = TA_TAU, OFF_NS = TA_OFF, START_C = 5, START_NS = TA_START;
constexpr unsigned long long CLASS_TAB = TA_TAB;
constexpr int ET = TA_ET, NOWN = TA_NOWN;
constexpr int MSG = 1024 + 256;
constexpr int CTRL_BYTES = NPAIR * 64 * 128;
constexpr unsigned CODE_BYTES = 23 * 1024;
constexpr int LDS_SPINS = 1 << 20, MEM_SPINS = 1 << 21;
static_assert(ET % 4 == 0 && NT - ET >= 36 && NOWN % 4 == 3 && NOWN >= 19 && NOWN <= 31 && TA_SIGAT < TA_POLLAT && TA_POLLAT <= ET / 4 && ET / 4 <= NSLOT, "second pass");
static_assert(LDS_BYTES <= 160 * 1024 && MK_OFF >= P_OFF + 16 * 2048 && FLAG_OFF % 16 == 0, "LDS map");

typedef __bf16 bf16x8 __attribute__((ext_vector_type(8)));
typedef __bf16 bf16x2 __attribute__((ext_vector_type(2)));
typedef short s16x4 __attribute__((ext_vector_type(4)));
typedef float f32x16 __attribute__((ext_vector_type(16)));
typedef float f32x4 __attribute__((ext_vector_type(4)));
typedef unsigned u32x4 __attribute__((ext_vector_type(4)));
typedef unsigned u32x2 __attribute__((ext_vector_type(2)));
typedef unsigned long long u64;
#define LAS __attribute__((address_space(3)))
#define GAS __attribute__((address_space(1)))
typedef LAS const char* lds_cptr;
typedef LAS char* lds_ptr;
typedef __amdgpu_buffer_rsrc_t srd_t;

#define SBAR() __builtin_amdgcn_sched_barrier(0)
#define PIN(x) asm volatile("" : "+v"(x))
#define MFMA32(a, b, c) __builtin_amdgcn_mfma_f32_32x32x16_bf16((a), (b), (c), 0, 0, 0)
#define RLX_AGENT __ATOMIC_RELAXED, __HIP_MEMORY_SCOPE_AGENT
#define RFL(x) __builtin_amdgcn_readfirstlane(x)
constexpr int SC1 = 16;

__device__ __forceinline__ srd_t make_srd(const void* p, int bytes) {
  return __builtin_amdgcn_make_buffer_rsrc((void*)p, (short)0, bytes, 0x00020000);
}

template <int PAD>
__device__ __forceinline__ void dma4(srd_t srd, unsigned voff, unsigned soff, unsigned ldsb) {
  unsigned keep;
  asm volatile(
      "s_mov_b32 %0, m0\n\ts_mov_b32 m0, %4\n\ts_nop 3\n\t"
      "buffer_load_dwordx4 %1, %2, %3 offen nt lds\n\t"
      "s_add_u32 m0, %4, %5\n\ts_nop 0\n\t"
      "buffer_load_dwordx4 %1, %2, %3 offen offset:1024 nt lds\n\t"
      "s_add_u32 m0, %4, %6\n\ts_nop 0\n\t"
      "buffer_load_dwordx4 %1, %2, %3 offen offset:2048 nt lds\n\t"
      "s_add_u32 m0, %4, %7\n\ts_nop 0\n\t"
      "buffer_load_dwordx4 %1, %2, %3 offen offset:3072 nt lds\n\t"
      "s_mov_b32 m0, %0"
      : "=&s"(keep)
      : "v"(voff), "s"(srd), "s"(soff), "s"(ldsb), "n"(PAD), "n"(2 * PAD), "n"(3 * PAD)
      : "memory", "scc");
}
#define TA_PIECE(N, I, OFS, STEP) \
      "s_waitcnt vmcnt(" #N ")\n\t" \
      "buffer_load_dwordx4 %[vo], %[srd], %[so] offen " OFS " nt lds\n\t" \
      "s_add_u32 m0, m0, " STEP "\n\t" \
      ".if " #N " - 16 == " #I "\n\tds_write_b32 %[la], %[lv]\n\t.endif\n\t"
#define TA_GROUP(N, I0, I1, I2, I3) \
      TA_PIECE(N, I0, "", "%[pad]") TA_PIECE(N, I1, "offset:1024", "%[pad]") TA_PIECE(N, I2, "offset:2048", "%[pad]") \
      TA_PIECE(N, I3, "offset:3072", "%[padg]") "s_add_u32 %[so], %[so], 0x1000\n\t"
#define TA_BODY(N) \
      ".p2align 9\n\t" \
      TA_GROUP(N, 0, 1, 2, 3) TA_GROUP(N, 4, 5, 6, 7) \
      "ds_read_b32 %[done], %[da]\n\ts_memrealtime vcc\n\t" \
      TA_GROUP(N, 8, 9, 10, 11) \
      "s_waitcnt lgkmcnt(0)\n\t" \
      "s_mov_b32 %[now], vcc_lo\n\t" \
      "s_sub_i32 %[t], vcc_lo, %[target]\n\ts_max_i32 %[t], %[t], 0\n\ts_min_i32 %[t], %[t], 0x3ff\n\t" \
      TA_PIECE(N, 12, "", "%[pad]") \
      "s_lshr_b32 %[t], %[t], 6\n\ts_lshl_b32 %[t], %[t], 2\n\ts_lshr_b64 vcc, %[tab], %[t]\n\t" \
      "s_and_b32 %[k], vcc_lo, 7\n\ts_add_i32 %[t], %[c], %[back]\n\t" \
      TA_PIECE(N, 13, "offset:1024", "%[pad]") \
      "v_cmp_gt_i32 vcc, %[t], %[done]\n\t" \
      "s_add_i32 %[c], %[c], 1\n\ts_add_i32 %[target], %[target], %[step]\n\t" \
      "s_add_i32 %[dst], %[dst], %[slot]\n\ts_cmp_eq_u32 %[dst], %[top]\n\ts_cselect_b32 %[dst], %[bot], %[dst]\n\t" \
      TA_PIECE(N, 14, "offset:2048", "%[pad]") \
      "s_cmp_ge_i32 %[c], %[cend]\n\ts_cselect_b32 %[t], 1, vcc_lo\n\t" \
      "s_lshl_b32 vcc_lo, %[k], 9\n\ts_add_u32 vcc_lo, vcc_lo, %[b0]\n\ts_addc_u32 vcc_hi, %[b1], 0\n\t" \
      TA_PIECE(N, 15, "offset:3072", "%[padg]") \
      "s_add_u32 %[so], %[so], 0x1000\n\ts_mov_b32 m0, %[dst]\n\tv_add_u32 %[lv], 1, %[lv]\n\t" \
      "s_cmp_lg_u32 %[t], 0\n\ts_cbranch_scc1 9f\n\ts_setpc_b64 vcc\n\t"
struct Stream { int c; unsigned soff, dst, target, now, k; };
__device__ __forceinline__ void stream_chunks(Stream& st, int cend, srd_t srd, unsigned voff, unsigned pad, unsigned step,
                                              u64 tab, unsigned ring, unsigned land_addr, unsigned done_addr) {
  unsigned keep, t, b0, b1, done, lv = st.c - 1;
  asm volatile(
      "s_mov_b32 %[keep], m0\n\ts_mov_b32 m0, %[dst]\n\t"
      "s_getpc_b64 vcc\n"
      "0:\n\t"
      "s_add_u32 %[b0], vcc_lo, 1f-0b\n\ts_addc_u32 %[b1], vcc_hi, 0\n\t"
      "s_lshl_b32 vcc_lo, %[k], 9\n\ts_add_u32 vcc_lo, vcc_lo, %[b0]\n\ts_addc_u32 vcc_hi, %[b1], 0\n\t"
      "s_setpc_b64 vcc\n\t"
      ".p2align 9\n"
      "1:\n\t"
      TA_BODY(18) TA_BODY(20) TA_BODY(22) TA_BODY(24) TA_BODY(26) TA_BODY(28) TA_BODY(31)
      "\n9:\n\t"
      "s_mov_b32 m0, %[keep]"
      : [keep] "=&s"(keep), [t] "=&s"(t), [b0] "=&s"(b0), [b1] "=&s"(b1), [done] "=&v"(done), [lv] "+&v"(lv),
        [c] "+&s"(st.c), [so] "+&s"(st.soff), [dst] "+&s"(st.dst), [target] "+&s"(st.target), [now] "=&s"(st.now), [k] "+&s"(st.k)
      : [vo] "v"(voff), [srd] "s"(srd), [pad] "s"(pad), [padg] "s"(pad + 4096), [step] "s"(step), [tab] "s"(tab),
        [cend] "s"(cend), [bot] "s"(ring), [top] "s"(ring + NSLOT * SLOT), [slot] "n"(SLOT), [back] "n"(2 - NSLOT),
        [la] "v"(land_addr), [da] "v"(done_addr)
      : "memory", "scc", "vcc");
  st.c = RFL(st.c); st.soff = RFL(st.soff); st.dst = RFL(st.dst); st.target = RFL(st.target); st.now = RFL(st.now); st.k = RFL(st.k);
}
__device__ __forceinline__ void dma4_rows(srd_t srd, unsigned voff, unsigned soff, unsigned ldsb, unsigned land_addr, unsigned lv) {
  unsigned keep;
#define TA_ROWS "s_waitcnt vmcnt(%[n])\n\tbuffer_load_dwordx4 %[vo], %[srd], %[so] offen nt lds\n\t" \
                "s_add_u32 m0, m0, %[vp]\n\ts_add_u32 %[so], %[so], 0x1000\n\t"
  asm volatile("s_mov_b32 %[keep], m0\n\ts_mov_b32 m0, %[lds]\n\ts_nop 2\n\t" TA_ROWS TA_ROWS TA_ROWS TA_ROWS
               "ds_write_b32 %[la], %[lv]\n\ts_mov_b32 m0, %[keep]"
               : [keep] "=&s"(keep), [so] "+&s"(soff)
               : [vo] "v"(voff), [srd] "s"(srd), [lds] "s"(ldsb), [vp] "n"(VP), [n] "n"(NOWN), [la] "v"(land_addr), [lv] "v"(lv)
               : "memory", "scc");
#undef TA_ROWS
}
__device__ __forceinline__ void dma2(srd_t srd, unsigned voff, unsigned soff, unsigned ldsb) {
  unsigned keep;
  asm volatile("s_mov_b32 %0, m0\n\ts_mov_b32 m0, %4\n\ts_nop 3\n\t"
               "buffer_load_dwordx4 %1, %2, %3 offen nt lds\n\t"
               "buffer_load_dwordx4 %1, %2, %3 offen offset:1024 nt lds\n\t"
               "s_mov_b32 m0, %0"
               : "=&s"(keep) : "v"(voff), "s"(srd), "s"(soff), "s"(ldsb) : "memory");
}
template <int N> __device__ __forceinline__ void wait_vm() { asm volatile("s_waitcnt vmcnt(%0)" ::"n"(N) : "memory"); }

__device__ __forceinline__ bf16x8 ld_frag(lds_cptr p) { return *(const LAS bf16x8*)p; }
__device__ __forceinline__ s16x4 ld_tr(lds_cptr p) { return __builtin_amdgcn_ds_read_tr16_b64_v4i16((LAS s16x4*)p); }
__device__ __forceinline__ bf16x8 join(s16x4 lo, s16x4 hi) {
  return __builtin_bit_cast(bf16x8, __builtin_shufflevector(lo, hi, 0, 1, 2, 3, 4, 5, 6, 7));
}
__device__ __forceinline__ float xhalf_max(float x) {
  auto r = __builtin_amdgcn_permlane32_swap(__float_as_uint(x), __float_as_uint(x), false, false);
  return fmaxf(__uint_as_float(r[0]), __uint_as_float(r[1]));
}
__device__ __forceinline__ float xhalf_sum(float x) {
  auto r = __builtin_amdgcn_permlane32_swap(__float_as_uint(x), __float_as_uint(x), false, false);
  return __uint_as_float(r[0]) + __uint_as_float(r[1]);
}
__device__ __forceinline__ unsigned pack2(float a, float b) {
  const bf16x2 v = {(__bf16)a, (__bf16)b};
  return __builtin_bit_cast(unsigned, v);
}
__device__ __forceinline__ bf16x8 pack8(const f32x16& p, int o) {
  bf16x8 r;
#pragma unroll
  for (int j = 0; j < 8; ++j) r[j] = (__bf16)p[o + j];
  return r;
}
__device__ __forceinline__ void flag_set(unsigned addr, unsigned v) {
  asm volatile("s_waitcnt lgkmcnt(0)\n\tds_write_b32 %0, %1" ::"v"(addr), "v"(v) : "memory");
}
__device__ __forceinline__ void flag_wait(unsigned addr, int need) {
#pragma unroll 1
  for (int spin = 0; spin < LDS_SPINS; ++spin) {
    unsigned v;
    asm volatile("ds_read_b32 %0, %1\n\ts_waitcnt lgkmcnt(0)" : "=v"(v) : "v"(addr) : "memory");
    if ((int)RFL(v) >= need) break;
    __builtin_amdgcn_s_sleep(1);
  }
}
__device__ __forceinline__ void flag_wait4(unsigned addr, int need) {
#pragma unroll 1
  for (int spin = 0; spin < LDS_SPINS; ++spin) {
    u32x4 v;
    asm volatile("ds_read_b128 %0, %1\n\ts_waitcnt lgkmcnt(0)" : "=v"(v) : "v"(addr) : "memory");
    if ((int)RFL(min(min(v[0], v[1]), min(v[2], v[3]))) >= need) break;
    __builtin_amdgcn_s_sleep(1);
  }
}

__global__ __launch_bounds__((NW + 1) * 64, 2) void tree_attn(
    const __hip_bfloat16* __restrict__ q, const __hip_bfloat16* __restrict__ kc, const __hip_bfloat16* __restrict__ vc,
    const __hip_bfloat16* __restrict__ kt, const __hip_bfloat16* __restrict__ vt, const __hip_bfloat16* __restrict__ mask,
    char* ws, __hip_bfloat16* __restrict__ out) {
  extern __shared__ __attribute__((aligned(16))) char lds[];
  u64 kbase, base2, now, pc;
  unsigned d0, d1, d2;
  const int wid = RFL(threadIdx.x >> 6);
  asm volatile("s_load_dwordx2 %[kb], %[ka], 0x8\n\t"
               "s_load_dwordx2 %[b2], %[ka], %[sel]\n\t"
               "s_getpc_b64 %[pc]\n\t"
               "s_load_dword %[d0], %[pc], %[line]\n\t"
               "s_load_dword %[d1], %[pc], %[line] offset:64\n\t"
               "s_load_dword %[d2], %[pc], %[line] offset:128\n\t"
               "s_memrealtime %[now]"
               : [kb] "=&s"(kbase), [b2] "=&s"(base2), [pc] "=&s"(pc), [d0] "=&s"(d0), [d1] "=&s"(d1), [d2] "=&s"(d2), [now] "=&s"(now)
               : [ka] "s"(__builtin_amdgcn_kernarg_segment_ptr()), [sel] "s"((wid >> 2) * 8),
                 [line] "s"(min(((blockIdx.x >> 3) * (NW + 1) + wid) * 192, CODE_BYTES - 256))
               : "memory");
  SBAR();
  const unsigned bid = (blockIdx.x & 7) * 32 + (blockIdx.x >> 3);
  const int lane = threadIdx.x & 63;
  const unsigned lds0 = (unsigned)(uintptr_t)lds;
  const unsigned voff = lane * 16;
  const int pair = bid >> 2, split = bid & 3;
  {
    unsigned keep;
#define TA_G4(VO, BASE) \
        "global_load_lds_dwordx4 " VO ", " BASE " nt\n\ts_add_u32 m0, m0, %[pad]\n\ts_nop 0\n\t" \
        "global_load_lds_dwordx4 " VO ", " BASE " offset:1024 nt\n\ts_add_u32 m0, m0, %[pad]\n\ts_nop 0\n\t" \
        "global_load_lds_dwordx4 " VO ", " BASE " offset:2048 nt\n\ts_add_u32 m0, m0, %[pad]\n\ts_nop 0\n\t" \
        "global_load_lds_dwordx4 " VO ", " BASE " offset:3072 nt\n\t"
    asm volatile(
        "s_mov_b32 %[keep], m0\n\ts_mov_b32 m0, %[dst2]\n\t"
        "s_waitcnt lgkmcnt(0)\n\t"
        TA_G4("%[vo2]", "%[b2]")
        "s_mov_b32 m0, %[dst]\n\ts_nop 0\n\t"
        TA_G4("%[vo]", "%[kb]")
        "s_mov_b32 m0, %[keep]"
        : [keep] "=&s"(keep), [kb] "+s"(kbase), [b2] "+s"(base2), [now] "+s"(now), [d0] "+s"(d0), [d1] "+s"(d1), [d2] "+s"(d2)
        : [vo] "v"(bid * WG_BYTES + wid * 4096 + voff),
          [dst] "s"(lds0 + wid * 4 * KP + (wid >> 2) * (SLOT - 16 * KP)),
          [vo2] "v"((wid == NW ? bid * WG_BYTES + 5 * 4096 : pair * 32 * ROWB + wid * 4096) + voff),
          [dst2] "s"(lds0 + (wid == NW ? SLOT + 4 * KP : Q_OFF + wid * 4 * KP)), [pad] "n"(KP - 1024)
        : "memory", "scc");
#undef TA_G4
  }
  const unsigned flags = lds0 + FLAG_OFF;
  if (threadIdx.x < 12) flag_set(flags + lane * 4, lane < 4 ? lane : lane == F_LAND / 4 ? 1 : 0);
  asm volatile("s_waitcnt lgkmcnt(0)\n\ts_barrier" ::: "memory");

  if (wid == NW) {
    __builtin_amdgcn_s_setprio(3);
    const srd_t sK = make_srd((const char*)kbase + (size_t)bid * WG_BYTES, WG_BYTES);
#pragma unroll 1
    for (int g = 2; g < 4; ++g) dma4<KP - 1024>(sK, voff, 16384 + g * 4096, lds0 + SLOT + g * 4 * KP);
    const srd_t sV = make_srd((const char*)vc + (size_t)bid * WG_BYTES, WG_BYTES);
    const unsigned t_in = (unsigned)now;
    Stream st = {2, 2 * 16384, lds0 + 2 * SLOT, t_in + (OFF_NS + 5 * TAU_NS / 2) / 10, 0, 0};
    const unsigned vfor = ((lane >> 3) & 3) == split ? WG_BYTES : voff;
    const unsigned vown = (2 * ((lane >> 3) & 3) + (lane >> 5)) * ROWB + split * 128 + (((lane & 7) ^ ((lane >> 2) & 4)) << 4);
#pragma unroll 1
    while (st.c < 2 * NT) {
      flag_wait4(flags + F_DONE, st.c - NSLOT + 1);
      if (st.c == NT) st.soff = 0;
      const bool isk = st.c < NT, part = st.c >= 2 * NT - ET;
      if (part) st.k = max(st.k, 4u);
      stream_chunks(st, st.c < START_C ? START_C : isk ? NT : part ? 2 * NT : 2 * NT - ET, isk ? sK : sV, part ? vfor : voff,
                    isk ? KP - 1024 : VP - 1024, part ? TAU_NS * 3 / 40 : TAU_NS / 10,
                    st.c < START_C ? TA_TAB0 : part ? TA_TABA : CLASS_TAB, lds0, flags + F_LAND, flags + F_DONE + (lane & 3) * 4);
      if (st.c == START_C) st.target += max((int)(st.now - t_in) - START_NS / 10, 0) * 3 / 4;
    }
    constexpr int BEHIND = (NOWN - 3) / 4;
#pragma unroll 1
    for (int u = 0; u < ET; ++u) {
      if ((u & 3) == 0) flag_wait4(flags + F_DONE, 2 * NT + u / 4 - NSLOT + 1);
      dma4_rows(sV, vown, (NT - ET + u) * 16384, st.dst + (u & 3) * 4 * VP, flags + F_LAND,
                2 * NT + (u >= BEHIND ? u - BEHIND : (u - BEHIND) >> 2));
      if ((u & 3) == 3) st.dst = st.dst == lds0 + (NSLOT - 1) * SLOT ? lds0 : st.dst + SLOT;
    }
    if constexpr (NOWN > 23) { wait_vm<24>(); flag_set(flags + F_LAND, 2 * NT + ET - 6); }
    wait_vm<20>(); flag_set(flags + F_LAND, 2 * NT + ET - 5);
    wait_vm<16>(); flag_set(flags + F_LAND, 2 * NT + ET - 4);
    wait_vm<12>(); flag_set(flags + F_LAND, 2 * NT + ET - 3);
    wait_vm<8>(); flag_set(flags + F_LAND, 2 * NT + ET - 2);
    wait_vm<4>(); flag_set(flags + F_LAND, 2 * NT + ET - 1);
    wait_vm<0>(); flag_set(flags + F_LAND, 2 * NT + ET);
    return;
  }

  const int w = wid;
  const int r = lane & 31, h = lane >> 5;
  if (w == 3) {
    dma2(make_srd((const char*)kt + (size_t)pair * 16 * ROWB, 16 * ROWB), voff, split * 4 * ROWB, lds0 + TK_OFF);
    dma2(make_srd((const char*)mask + (pair >> 3) * 512, 512), voff, 0, lds0 + MK_OFF);
  }
  if (w == 2) dma2(make_srd((const char*)vt + (size_t)pair * 16 * ROWB, 16 * ROWB), voff, split * 4 * ROWB, lds0 + TV_OFF);
  const int qrow = 2 * (r & 15) + (r >> 4);
  const int koff = (r & 15) * KP + (r >> 4) * 512 + h * 16;
  const int trofs = ((lane >> 4) & 1) * 128 + w * 32 + (lane & 3) * 8;
  const int vofs = (4 * h + ((lane & 15) >> 2)) * VP + trofs;
  if (w == 3) wait_vm<4>(); else if (w == 2) wait_vm<2>(); else wait_vm<0>();
  if (lane == 0) asm volatile("ds_add_u32 %0, %1" ::"v"(flags + F_Q), "v"(1u) : "memory");
  flag_wait(flags + F_Q, NW);
  bf16x8 qf[16];
#pragma unroll
  for (int kk = 0; kk < 16; ++kk) qf[kk] = ld_frag((lds_cptr)lds + Q_OFF + koff + kk * 32);

  const lds_ptr refp = (lds_ptr)lds + REF_OFF + r * 4;
  float m = -INFINITY, l = 0.f;
  if (w == 3) {
    wait_vm<0>();
    const u32x2 mbits = *(const LAS u32x2*)((lds_cptr)lds + MK_OFF + (qrow & 15) * 32 + split * 8);
    const lds_cptr tk = (lds_cptr)lds + TK_OFF + (r & 3) * ROWB + h * 16;
    f32x16 s = f32x16{};
#pragma unroll
    for (int kk = 0; kk < 16; ++kk) s = MFMA32(ld_frag(tk + kk * 32), qf[kk], s);
#pragma unroll
    for (int i = 0; i < 4; ++i) {
      const float mk = __uint_as_float((i & 1) ? (mbits[i >> 1] & 0xffff0000u) : (mbits[i >> 1] << 16));
      s[i] = fmaf(s[i], SCALE, (1.0f - mk) * -1000000000.0f) * LOG2E;
      m = fmaxf(m, s[i]);
    }
#pragma unroll
    for (int i = 0; i < 4; ++i) { s[i] = __builtin_amdgcn_exp2f(s[i] - m); l += s[i]; }
    if (h) l = 0.f;
    *(LAS float*)refp = m;
    *(LAS u32x2*)((lds_ptr)lds + PT_OFF + r * 8) = u32x2{pack2(s[0], s[1]), pack2(s[2], s[3])};
    flag_set(flags + F_REF, 1);
  }

  int slot_off = w * SLOT;
  const lds_ptr pbuf = (lds_ptr)lds + P_OFF + lane * 16;
  bf16x8 preg[NT / 8][2];

  auto k_tile = [&](int c, bf16x8& p0, bf16x8& p1) {
    flag_wait(flags + F_LAND, c + 1);
    const lds_cptr kfrag = (lds_cptr)lds + slot_off + koff;
    f32x16 s = f32x16{};
    bf16x8 kf[8];
#pragma unroll
    for (int kk = 0; kk < 8; ++kk) kf[kk] = ld_frag(kfrag + kk * 32);
    SBAR();
#pragma unroll
    for (int kk = 0; kk < 8; ++kk) { s = MFMA32(kf[kk], qf[kk], s); kf[kk] = ld_frag(kfrag + (kk + 8) * 32); SBAR(); }
    flag_set(flags + F_DONE + w * 4, min(c + NW, NT));
    slot_off = slot_off == 0 ? (NSLOT - 1) * SLOT : slot_off - SLOT;
    SBAR();
#pragma unroll
    for (int kk = 0; kk < 8; ++kk) s = MFMA32(kf[kk], qf[kk + 8], s);
    float tmax = fmaxf(s[0], s[1]);
#pragma unroll
    for (int i = 2; i < 16; ++i) tmax = fmaxf(tmax, s[i]);
    tmax = xhalf_max(tmax) * C2;
    PIN(tmax);
    flag_wait(flags + F_REF, c + 1);
    const float mn = fmaxf(*(const LAS float*)(refp + c * 128), tmax);
    *(LAS float*)(refp + (c + 1) * 128) = mn;
    flag_set(flags + F_REF, c + 2);
    const float alpha = __builtin_amdgcn_exp2f(m - mn);
    m = mn;
    float ps = 0.f;
#pragma unroll
    for (int i = 0; i < 16; ++i) { s[i] = __builtin_amdgcn_exp2f(fmaf(s[i], C2, -mn)); ps += s[i]; }
    l = fmaf(l, alpha, ps);
    p0 = pack8(s, 0); p1 = pack8(s, 8);
    PIN(l); PIN(m);
  };
#pragma unroll 1
  for (int c = w; c < NT / 2; c += NW) {
    bf16x8 p0, p1;
    k_tile(c, p0, p1);
    *(LAS bf16x8*)(pbuf + c * 2048) = p0;
    *(LAS bf16x8*)(pbuf + c * 2048 + 1024) = p1;
  }

  char* msgs = ws + CTRL_BYTES + (size_t)pair * 64 * MSG;
  const srd_t sZ = make_srd(msgs, 64 * MSG);
  const int peer = (split + 1 + (lane & 3) % 3) & 3;
  GAS u64* sig_in = (GAS u64*)(ws + (((pair * 4 + peer) * 4 + split) * 4 + w) * 128);
  GAS u64* sig_out = (GAS u64*)(ws + (((pair * 4 + split) * 4 + peer) * 4 + w) * 128);
  const char* ahead = w == 0 ? (const char*)vc + (size_t)bid * WG_BYTES : w == 1 ? msgs
                    : w == 2 ? (const char*)out + (size_t)pair * 32 * ROWB + split * 128 : msgs + 64 * MSG - 64;
  const unsigned warm = __builtin_nontemporal_load((const GAS unsigned*)ahead + (lane & 1));
  unsigned in0 = __hip_atomic_load((GAS unsigned*)sig_in, RLX_AGENT);
  unsigned out1 = __hip_atomic_load((GAS unsigned*)sig_out, RLX_AGENT) + 1;

#pragma unroll
  for (int t = 0; t < NT / 8; ++t) { k_tile(NT / 2 + 4 * t + w, preg[t][0], preg[t][1]); PIN(preg[t][0]); PIN(preg[t][1]); }
  wait_vm<0>();
  asm volatile("" ::"v"(warm));
  flag_set(flags + F_PRDY + w * 4, 1);
  flag_wait4(flags + F_PRDY, 1);

  f32x16 o[2];
  float rcur = *(const LAS float*)refp;
  {
    const lds_cptr tv = (lds_cptr)lds + TV_OFF + ((lane & 15) >> 2) * ROWB + trofs;
    u32x2 pt2 = *(const LAS u32x2*)((lds_cptr)lds + PT_OFF + r * 8);
    if (h) pt2 = u32x2{0u, 0u};
    const bf16x8 pt = __builtin_bit_cast(bf16x8, u32x4{pt2[0], pt2[1], 0u, 0u});
#pragma unroll
    for (int cb = 0; cb < 2; ++cb) { const s16x4 v4 = ld_tr(tv + cb * 256); o[cb] = MFMA32(join(v4, v4), pt, f32x16{}); }
  }
  const float M = *(const LAS float*)(refp + NT * 128);
  *(LAS float*)((lds_ptr)lds + L_OFF + w * 128 + r * 4) = xhalf_sum(l) * __builtin_amdgcn_exp2f(m - M);
  slot_off = (NT % NSLOT) * SLOT;
  auto v_tiles = [&](int v0, int v1) {
#pragma unroll 1
    for (int v = v0; v < v1; ++v) {
      if (v == 32 || v == 48) flag_wait4(flags + F_PRDY, v >> 4);
      const float rnew = *(const LAS float*)(refp + (v + 1) * 128);
      bf16x8 p0 = ld_frag(pbuf + (v & (NT / 2 - 1)) * 2048), p1 = ld_frag(pbuf + (v & (NT / 2 - 1)) * 2048 + 1024);
      if (__any(rnew != rcur)) {
        const float alpha = __builtin_amdgcn_exp2f(rcur - rnew);
        o[0] *= alpha; o[1] *= alpha;
        rcur = rnew;
      }
      PIN(p0); PIN(p1);
      flag_wait(flags + F_LAND, NT + v + 1);
      const lds_cptr vfrag = (lds_cptr)lds + slot_off + vofs;
      bf16x8 vf[4];
#pragma unroll
      for (int i = 0; i < 4; ++i)
        vf[i] = join(ld_tr(vfrag + (i >> 1) * 512 + (i & 1) * 256), ld_tr(vfrag + 8 * VP + (i >> 1) * 512 + (i & 1) * 256));
      SBAR();
      o[0] = MFMA32(vf[0], p0, o[0]);
      o[1] = MFMA32(vf[1], p0, o[1]);
      SBAR();
      flag_set(flags + F_DONE + w * 4, NT + v + 1);
      slot_off = slot_off == (NSLOT - 1) * SLOT ? 0 : slot_off + SLOT;
      o[0] = MFMA32(vf[2], p1, o[0]);
      o[1] = MFMA32(vf[3], p1, o[1]);
    }
  };
  auto refill = [&](int j0) {
    flag_wait4(flags + F_DONE, NT + 16 + 4 * j0);
#pragma unroll
    for (int j = j0; j < j0 + 4; ++j) {
      *(LAS bf16x8*)(pbuf + (4 * j + w) * 2048) = preg[j][0];
      *(LAS bf16x8*)(pbuf + (4 * j + w) * 2048 + 1024) = preg[j][1];
    }
    flag_set(flags + F_PRDY + w * 4, 2 + j0 / 4);
  };
  v_tiles(0, 20);
  refill(0);
  v_tiles(20, 36);
  refill(4);

  float L = 0.f;
#pragma unroll
  for (int i = 0; i < NW; ++i) L += *(const LAS float*)((lds_cptr)lds + L_OFF + i * 128 + r * 4);
  unsigned zout[4];
#pragma unroll
  for (int j = 0; j < 4; ++j) { zout[j] = (j == split ? 64 : (j * 4 + split) * 4 + w) * MSG + voff; PIN(zout[j]); }
  char* orow = (char*)out + ((size_t)pair * 32 + qrow) * ROWB + split * 128 + w * 32 + h * 16;
  f32x4 mlw = *(const LAS f32x4*)((lds_cptr)lds + ((lane & 8) ? L_OFF : REF_OFF + NT * 128) + (lane & 7) * 16);
  if (lane & 8) {
#pragma unroll
    for (int i = 1; i < NW; ++i) mlw += *(const LAS f32x4*)((lds_cptr)lds + L_OFF + i * 128 + (lane & 7) * 16);
  }
  unsigned mlout = (lane < 48 ? (((split + 1 + (lane >> 4)) & 3) * 4 + split) * 4 + w : 64) * MSG + 1024 + (lane & 15) * 16;
  PIN(L); PIN(orow); PIN(mlw); PIN(mlout); PIN(in0); PIN(out1);
  v_tiles(36, NT - ET);
  float own[8];
  {
    const float f = __builtin_amdgcn_exp2f(rcur - M);
#pragma unroll
    for (int e = 0; e < 8; ++e) {
      const float lo = (split & 1) ? o[0][8 + e] : o[0][e], hi = (split & 1) ? o[1][8 + e] : o[1][e];
      own[e] = f * ((split & 2) ? hi : lo);
      PIN(own[e]);
    }
  }
  v_tiles(NT - ET, NT);

  __builtin_amdgcn_raw_buffer_store_b128(__builtin_bit_cast(u32x4, mlw), sZ, mlout, 0, SC1);
#pragma unroll
  for (int j = 0; j < 4; ++j) {
    u32x4 x;
#pragma unroll
    for (int e = 0; e < 4; ++e) x[e] = pack2(o[j >> 1][8 * (j & 1) + 2 * e], o[j >> 1][8 * (j & 1) + 2 * e + 1]);
    __builtin_amdgcn_raw_buffer_store_b128(x, sZ, zout[j], 0, SC1);
  }

  const int oofs = h * VP + ((lane & 15) >> 2) * 128 + ((w * 32 + (lane & 3) * 8) ^ ((lane & 8) << 3));
  auto own_chunk = [&](int i) {
    float f[4];
    bf16x8 p[4][2], vf[4][2];
    f32x16 t[4];
#pragma unroll
    for (int u = 0; u < 4; ++u) {
      const int v = NT - ET + 4 * i + u;
      f[u] = *(const LAS float*)(refp + (v + 1) * 128);
      p[u][0] = ld_frag(pbuf + (v & (NT / 2 - 1)) * 2048); p[u][1] = ld_frag(pbuf + (v & (NT / 2 - 1)) * 2048 + 1024);
    }
    flag_wait(flags + F_LAND, 2 * NT + 4 * i + 4);
    const lds_cptr vfrag = (lds_cptr)lds + slot_off + oofs;
#pragma unroll
    for (int u = 0; u < 4; ++u) {
      vf[u][0] = join(ld_tr(vfrag + u * 4 * VP), ld_tr(vfrag + u * 4 * VP + 2 * VP));
      vf[u][1] = join(ld_tr(vfrag + u * 4 * VP + 512), ld_tr(vfrag + u * 4 * VP + 2 * VP + 512));
    }
#pragma unroll
    for (int u = 0; u < 4; ++u) { f[u] = __builtin_amdgcn_exp2f(f[u] - M); PIN(f[u]); }
    SBAR();
#pragma unroll
    for (int u = 0; u < 4; ++u) t[u] = MFMA32(vf[u][1], p[u][1], MFMA32(vf[u][0], p[u][0], f32x16{}));
#pragma unroll
    for (int u = 0; u < 4; ++u)
#pragma unroll
      for (int e = 0; e < 8; ++e) own[e] = fmaf(f[u], t[u][e], own[e]);
#pragma unroll
    for (int e = 0; e < 8; ++e) PIN(own[e]);
    slot_off = slot_off == (NSLOT - 1) * SLOT ? 0 : slot_off + SLOT;
  };
#pragma unroll 1
  for (int i = 0; i < TA_POLLAT; ++i) {
    if (i == TA_SIGAT) {
      wait_vm<0>();
      if (lane < 3) __hip_atomic_store(sig_out, ((u64)in0 << 32) | out1, RLX_AGENT);
    }
    own_chunk(i);
  }
  {
    unsigned got;
    int spin = 0;
#pragma unroll 1
    do got = __hip_atomic_load((GAS unsigned*)sig_in, RLX_AGENT);
    while (((unsigned)__ballot(got != in0) & 7u) != 7u && ++spin < MEM_SPINS);
  }
  __builtin_amdgcn_fence(__ATOMIC_ACQUIRE, "wavefront");
  asm volatile("" ::: "memory");
  u32x4 a[3];
  unsigned ml[3];
#pragma unroll
  for (int k = 0; k < 3; ++k) {
    const unsigned zin = ((split * 4 + ((split + 1 + k) & 3)) * 4 + w) * MSG;
    ml[k] = __builtin_amdgcn_raw_buffer_load_b32(sZ, 1024 + lane * 4, zin, SC1);
    a[k] = __builtin_amdgcn_raw_buffer_load_b128(sZ, voff, zin, SC1);
  }
  const unsigned peer_view = __hip_atomic_load((GAS unsigned*)sig_in + 1, RLX_AGENT);
#pragma unroll 1
  for (int i = TA_POLLAT; i < ET / 4; ++i) own_chunk(i);
  float w0, rest[8];
  {
    float mk[3], lk[3], wk[3], G = M;
#pragma unroll
    for (int k = 0; k < 3; ++k) {
      auto x = __builtin_amdgcn_permlane32_swap(ml[k], ml[k], false, false);
      mk[k] = __uint_as_float(x[0]); lk[k] = __uint_as_float(x[1]);
      G = fmaxf(G, mk[k]);
    }
    w0 = __builtin_amdgcn_exp2f(M - G);
    float den = w0 * L;
#pragma unroll
    for (int k = 0; k < 3; ++k) { wk[k] = __builtin_amdgcn_exp2f(mk[k] - G); den = fmaf(wk[k], lk[k], den); }
    const float inv = 1.0f / den;
    w0 *= inv;
#pragma unroll
    for (int e = 0; e < 8; ++e) rest[e] = 0.f;
#pragma unroll
    for (int k = 0; k < 3; ++k)
#pragma unroll
      for (int e = 0; e < 4; ++e) {
        rest[2 * e] = fmaf(wk[k], __uint_as_float(a[k][e] << 16), rest[2 * e]);
        rest[2 * e + 1] = fmaf(wk[k], __uint_as_float(a[k][e] & 0xffff0000u), rest[2 * e + 1]);
      }
#pragma unroll
    for (int e = 0; e < 8; ++e) { rest[e] *= inv; PIN(rest[e]); }
    PIN(w0);
  }
#pragma unroll
  for (int e = 0; e < 8; ++e) own[e] = fmaf(own[e], w0, rest[e]);
  auto x = __builtin_amdgcn_permlane32_swap(pack2(own[0], own[1]), pack2(own[4], own[5]), false, false);
  auto y = __builtin_amdgcn_permlane32_swap(pack2(own[2], own[3]), pack2(own[6], own[7]), false, false);
  *(GAS u32x4*)orow = u32x4{x[0], y[0], x[1], y[1]};
  if (lane < 3 && peer_view == out1) __hip_atomic_store(sig_out, ((u64)in0 << 32) | (out1 + 1), RLX_AGENT);
}

}

extern "C" void kernel_launch(void* const* d_in, const int* in_sizes, int n_in, void* d_out, int out_size, void* d_ws,
                              size_t ws_size, hipStream_t stream) {
  using namespace ta;
  (void)in_sizes; (void)n_in; (void)out_size; (void)ws_size;
  (void)hipFuncSetAttribute((const void*)tree_attn, hipFuncAttributeMaxDynamicSharedMemorySize, LDS_BYTES);
  hipLaunchKernelGGL(tree_attn, dim3(NPAIR * NSPLIT), dim3((NW + 1) * 64), LDS_BYTES, stream,
                     (const __hip_bfloat16*)d_in[0], (const __hip_bfloat16*)d_in[1], (const __hip_bfloat16*)d_in[2],
                     (const __hip_bfloat16*)d_in[3], (const __hip_bfloat16*)d_in[4], (const __hip_bfloat16*)d_in[5],
                     (char*)d_ws, (__hip_bfloat16*)d_out);
}
```

```cpp
#include <hip/hip_runtime.h>
#include <hip/hip_bf16.h>

namespace ta {

constexpr int NB = 8, HKV = 8, CTX = 8192, HD = 256;
constexpr int NPAIR = NB * HKV, NSPLIT = 4, NW = 4;
constexpr int ROWB = HD * 2;
constexpr int WG_BYTES = CTX / NSPLIT * ROWB;
constexpr int NT = CTX / NSPLIT / 32;
constexpr int KP = 1040, VP = 1056;
constexpr int SLOT = 16 * VP, NSLOT = 5;
constexpr int P_OFF = NSLOT * SLOT;
constexpr int REF_OFF = P_OFF + NT / 2 * 2048;
constexpr int TV_OFF = REF_OFF + (NT + 1) * 128;
constexpr int PT_OFF = TV_OFF + 4 * ROWB;
constexpr int L_OFF = PT_OFF + 256;
constexpr int FLAG_OFF = L_OFF + NW * 128;
constexpr int F_DONE = 0, F_PRDY = 16, F_LAND = 32, F_REF = 36, F_Q = 40;
constexpr int LDS_BYTES = FLAG_OFF + 48;
constexpr int TK_OFF = REF_OFF - 4 * ROWB, Q_OFF = TK_OFF - 16 * KP, MK_OFF = Q_OFF - 2048;
constexpr float SCALE = 0.0625f, LOG2E = 1.4426950408889634f, C2 = SCALE * LOG2E;
constexpr int TAU_NS = 540, OFF_NS = 400, START_C = 5, START_NS = 2750;
constexpr unsigned long long TAB_START = 0x1111111111111110ull, TAB_FULL = 0x6666666655443210ull, TAB_PART = 0x6666666666666654ull;
constexpr int ET = 12, NOWN = 27;
constexpr int MSG = 1024 + 256;
constexpr int CTRL_BYTES = NPAIR * 64 * 128;
constexpr unsigned CODE_BYTES = 23 * 1024;
constexpr int LDS_SPINS = 1 << 20, MEM_SPINS = 1 << 21;
constexpr bool classes_ok(unsigned long long t) { return t == 0 || ((t & 15) <= 6 && classes_ok(t >> 4)); }
static_assert(classes_ok(TAB_START) && classes_ok(TAB_FULL) && classes_ok(TAB_PART), "seven loader bodies");
static_assert(ET == 12 && NOWN == 27, "second pass: three chunks that are never released, drain ladder");
static_assert(LDS_BYTES <= 160 * 1024 && MK_OFF >= P_OFF + 16 * 2048 && FLAG_OFF % 16 == 0, "LDS map");

typedef __bf16 bf16x8 __attribute__((ext_vector_type(8)));
typedef __bf16 bf16x2 __attribute__((ext_vector_type(2)));
typedef short s16x4 __attribute__((ext_vector_type(4)));
typedef float f32x16 __attribute__((ext_vector_type(16)));
typedef float f32x4 __attribute__((ext_vector_type(4)));
typedef unsigned u32x4 __attribute__((ext_vector_type(4)));
typedef unsigned u32x2 __attribute__((ext_vector_type(2)));
typedef unsigned long long u64;
#define LAS __attribute__((address_space(3)))
#define GAS __attribute__((address_space(1)))
typedef LAS const char* lds_cptr;
typedef LAS char* lds_ptr;
typedef __amdgpu_buffer_rsrc_t srd_t;

#define SBAR() __builtin_amdgcn_sched_barrier(0)
#define PIN(x) asm volatile("" : "+v"(x))
#define MFMA32(a, b, c) __builtin_amdgcn_mfma_f32_32x32x16_bf16((a), (b), (c), 0, 0, 0)
#define RLX_AGENT __ATOMIC_RELAXED, __HIP_MEMORY_SCOPE_AGENT
#define RFL(x) __builtin_amdgcn_readfirstlane(x)
constexpr int SC1 = 16;

__device__ __forceinline__ srd_t make_srd(const void* p, int bytes) {
  return __builtin_amdgcn_make_buffer_rsrc((void*)p, (short)0, bytes, 0x00020000);
}

template <int PAD>
__device__ __forceinline__ void dma4(srd_t srd, unsigned voff, unsigned soff, unsigned ldsb) {
  unsigned keep;
  asm volatile(
      "s_mov_b32 %0, m0\n\ts_mov_b32 m0, %4\n\ts_nop 3\n\t"
      "buffer_load_dwordx4 %1, %2, %3 offen nt lds\n\t"
      "s_add_u32 m0, %4, %5\n\ts_nop 0\n\t"
      "buffer_load_dwordx4 %1, %2, %3 offen offset:1024 nt lds\n\t"
      "s_add_u32 m0, %4, %6\n\ts_nop 0\n\t"
      "buffer_load_dwordx4 %1, %2, %3 offen offset:2048 nt lds\n\t"
      "s_add_u32 m0, %4, %7\n\ts_nop 0\n\t"
      "buffer_load_dwordx4 %1, %2, %3 offen offset:3072 nt lds\n\t"
      "s_mov_b32 m0, %0"
      : "=&s"(keep)
      : "v"(voff), "s"(srd), "s"(soff), "s"(ldsb), "n"(PAD), "n"(2 * PAD), "n"(3 * PAD)
      : "memory", "scc");
}
#define TA_PIECE(N, I, OFS, STEP) \
      "s_waitcnt vmcnt(" #N ")\n\t" \
      "buffer_load_dwordx4 %[vo], %[srd], %[so] offen " OFS " nt lds\n\t" \
      "s_add_u32 m0, m0, " STEP "\n\t" \
      ".if " #N " - 16 == " #I "\n\tds_write_b32 %[la], %[lv]\n\t.endif\n\t"
#define TA_GROUP(N, I0, I1, I2, I3) \
      TA_PIECE(N, I0, "", "%[pad]") TA_PIECE(N, I1, "offset:1024", "%[pad]") TA_PIECE(N, I2, "offset:2048", "%[pad]") \
      TA_PIECE(N, I3, "offset:3072", "%[padg]") "s_add_u32 %[so], %[so], 0x1000\n\t"
#define TA_BODY(N) \
      ".p2align 9\n\t" \
      TA_GROUP(N, 0, 1, 2, 3) TA_GROUP(N, 4, 5, 6, 7) \
      "ds_read_b32 %[done], %[da]\n\ts_memrealtime vcc\n\t" \
      TA_GROUP(N, 8, 9, 10, 11) \
      "s_waitcnt lgkmcnt(0)\n\t" \
      "s_mov_b32 %[now], vcc_lo\n\t" \
      "s_sub_i32 %[t], vcc_lo, %[target]\n\ts_max_i32 %[t], %[t], 0\n\ts_min_i32 %[t], %[t], 0x3ff\n\t" \
      TA_PIECE(N, 12, "", "%[pad]") \
      "s_lshr_b32 %[t], %[t], 6\n\ts_lshl_b32 %[t], %[t], 2\n\ts_lshr_b64 vcc, %[tab], %[t]\n\t" \
      "s_and_b32 %[k], vcc_lo, 7\n\ts_add_i32 %[t], %[c], %[back]\n\t" \
      TA_PIECE(N, 13, "offset:1024", "%[pad]") \
      "v_cmp_gt_i32 vcc, %[t], %[done]\n\t" \
      "s_add_i32 %[c], %[c], 1\n\ts_add_i32 %[target], %[target], %[step]\n\t" \
      "s_add_i32 %[dst], %[dst], %[slot]\n\ts_cmp_eq_u32 %[dst], %[top]\n\ts_cselect_b32 %[dst], %[bot], %[dst]\n\t" \
      TA_PIECE(N, 14, "offset:2048", "%[pad]") \
      "s_cmp_ge_i32 %[c], %[cend]\n\ts_cselect_b32 %[t], 1, vcc_lo\n\t" \
      "s_lshl_b32 vcc_lo, %[k], 9\n\ts_add_u32 vcc_lo, vcc_lo, %[b0]\n\ts_addc_u32 vcc_hi, %[b1], 0\n\t" \
      TA_PIECE(N, 15, "offset:3072", "%[padg]") \
      "s_add_u32 %[so], %[so], 0x1000\n\ts_mov_b32 m0, %[dst]\n\tv_add_u32 %[lv], 1, %[lv]\n\t" \
      "s_cmp_lg_u32 %[t], 0\n\ts_cbranch_scc1 9f\n\ts_setpc_b64 vcc\n\t"
struct Stream { int c; unsigned soff, dst, target, now, k; };
__device__ __forceinline__ void stream_chunks(Stream& st, int cend, srd_t srd, unsigned voff, unsigned pad, unsigned step,
                                              u64 tab, unsigned ring, unsigned land_addr, unsigned done_addr) {
  unsigned keep, t, b0, b1, done, lv = st.c - 1;
  asm volatile(
      "s_mov_b32 %[keep], m0\n\ts_mov_b32 m0, %[dst]\n\t"
      "s_getpc_b64 vcc\n"
      "0:\n\t"
      "s_add_u32 %[b0], vcc_lo, 1f-0b\n\ts_addc_u32 %[b1], vcc_hi, 0\n\t"
      "s_lshl_b32 vcc_lo, %[k], 9\n\ts_add_u32 vcc_lo, vcc_lo, %[b0]\n\ts_addc_u32 vcc_hi, %[b1], 0\n\t"
      "s_setpc_b64 vcc\n\t"
      ".p2align 9\n"
      "1:\n\t"
      TA_BODY(18) TA_BODY(20) TA_BODY(22) TA_BODY(24) TA_BODY(26) TA_BODY(28) TA_BODY(31)
      "\n9:\n\t"
      "s_mov_b32 m0, %[keep]"
      : [keep] "=&s"(keep), [t] "=&s"(t), [b0] "=&s"(b0), [b1] "=&s"(b1), [done] "=&v"(done), [lv] "+&v"(lv),
        [c] "+&s"(st.c), [so] "+&s"(st.soff), [dst] "+&s"(st.dst), [target] "+&s"(st.target), [now] "=&s"(st.now), [k] "+&s"(st.k)
      : [vo] "v"(voff), [srd] "s"(srd), [pad] "s"(pad), [padg] "s"(pad + 4096), [step] "s"(step), [tab] "s"(tab),
        [cend] "s"(cend), [bot] "s"(ring), [top] "s"(ring + NSLOT * SLOT), [slot] "n"(SLOT), [back] "n"(2 - NSLOT),
        [la] "v"(land_addr), [da] "v"(done_addr)
      : "memory", "scc", "vcc");
  st.c = RFL(st.c); st.soff = RFL(st.soff); st.dst = RFL(st.dst); st.target = RFL(st.target); st.now = RFL(st.now); st.k = RFL(st.k);
}
__device__ __forceinline__ void dma4_rows(srd_t srd, unsigned voff, unsigned soff, unsigned ldsb, unsigned land_addr, unsigned lv) {
  unsigned keep;
#define TA_ROWS "s_waitcnt vmcnt(%[n])\n\tbuffer_load_dwordx4 %[vo], %[srd], %[so] offen nt lds\n\t" \
                "s_add_u32 m0, m0, %[vp]\n\ts_add_u32 %[so], %[so], 0x1000\n\t"
  asm volatile("s_mov_b32 %[keep], m0\n\ts_mov_b32 m0, %[lds]\n\ts_nop 2\n\t" TA_ROWS TA_ROWS TA_ROWS TA_ROWS
               "ds_write_b32 %[la], %[lv]\n\ts_mov_b32 m0, %[keep]"
               : [keep] "=&s"(keep), [so] "+&s"(soff)
               : [vo] "v"(voff), [srd] "s"(srd), [lds] "s"(ldsb), [vp] "n"(VP), [n] "n"(NOWN), [la] "v"(land_addr), [lv] "v"(lv)
               : "memory", "scc");
#undef TA_ROWS
}
__device__ __forceinline__ void dma2(srd_t srd, unsigned voff, unsigned soff, unsigned ldsb) {
  unsigned keep;
  asm volatile("s_mov_b32 %0, m0\n\ts_mov_b32 m0, %4\n\ts_nop 3\n\t"
               "buffer_load_dwordx4 %1, %2, %3 offen nt lds\n\t"
               "buffer_load_dwordx4 %1, %2, %3 offen offset:1024 nt lds\n\t"
               "s_mov_b32 m0, %0"
               : "=&s"(keep) : "v"(voff), "s"(srd), "s"(soff), "s"(ldsb) : "memory");
}
template <int N> __device__ __forceinline__ void wait_vm() { asm volatile("s_waitcnt vmcnt(%0)" ::"n"(N) : "memory"); }

__device__ __forceinline__ bf16x8 ld_frag(lds_cptr p) { return *(const LAS bf16x8*)p; }
__device__ __forceinline__ s16x4 ld_tr(lds_cptr p) { return __builtin_amdgcn_ds_read_tr16_b64_v4i16((LAS s16x4*)p); }
__device__ __forceinline__ bf16x8 join(s16x4 lo, s16x4 hi) {
  return __builtin_bit_cast(bf16x8, __builtin_shufflevector(lo, hi, 0, 1, 2, 3, 4, 5, 6, 7));
}
__device__ __forceinline__ float xhalf_max(float x) {
  auto r = __builtin_amdgcn_permlane32_swap(__float_as_uint(x), __float_as_uint(x), false, false);
  return fmaxf(__uint_as_float(r[0]), __uint_as_float(r[1]));
}
__device__ __forceinline__ float xhalf_sum(float x) {
  auto r = __builtin_amdgcn_permlane32_swap(__float_as_uint(x), __float_as_uint(x), false, false);
  return __uint_as_float(r[0]) + __uint_as_float(r[1]);
}
__device__ __forceinline__ unsigned pack2(float a, float b) {
  const bf16x2 v = {(__bf16)a, (__bf16)b};
  return __builtin_bit_cast(unsigned, v);
}
__device__ __forceinline__ bf16x8 pack8(const f32x16& p, int o) {
  bf16x8 r;
#pragma unroll
  for (int j = 0; j < 8; ++j) r[j] = (__bf16)p[o + j];
  return r;
}
__device__ __forceinline__ void flag_set(unsigned addr, unsigned v) {
  asm volatile("s_waitcnt lgkmcnt(0)\n\tds_write_b32 %0, %1" ::"v"(addr), "v"(v) : "memory");
}
__device__ __forceinline__ void flag_wait(unsigned addr, int need) {
#pragma unroll 1
  for (int spin = 0; spin < LDS_SPINS; ++spin) {
    unsigned v;
    asm volatile("ds_read_b32 %0, %1\n\ts_waitcnt lgkmcnt(0)" : "=v"(v) : "v"(addr) : "memory");
    if ((int)RFL(v) >= need) break;
    __builtin_amdgcn_s_sleep(1);
  }
}
__device__ __forceinline__ void flag_wait4(unsigned addr, int need) {
#pragma unroll 1
  for (int spin = 0; spin < LDS_SPINS; ++spin) {
    u32x4 v;
    asm volatile("ds_read_b128 %0, %1\n\ts_waitcnt lgkmcnt(0)" : "=v"(v) : "v"(addr) : "memory");
    if ((int)RFL(min(min(v[0], v[1]), min(v[2], v[3]))) >= need) break;
    __builtin_amdgcn_s_sleep(1);
  }
}

__global__ __launch_bounds__((NW + 1) * 64, 2) void tree_attn(
    const __hip_bfloat16* __restrict__ q, const __hip_bfloat16* __restrict__ kc, const __hip_bfloat16* __restrict__ vc,
    const __hip_bfloat16* __restrict__ kt, const __hip_bfloat16* __restrict__ vt, const __hip_bfloat16* __restrict__ mask,
    char* ws, __hip_bfloat16* __restrict__ out) {
  extern __shared__ __attribute__((aligned(16))) char lds[];
  u64 kbase, base2, now, pc;
  unsigned d0, d1, d2;
  const int wid = RFL(threadIdx.x >> 6);
  asm volatile("s_load_dwordx2 %[kb], %[ka], 0x8\n\t"
               "s_load_dwordx2 %[b2], %[ka], %[sel]\n\t"
               "s_getpc_b64 %[pc]\n\t"
               "s_load_dword %[d0], %[pc], %[line]\n\t"
               "s_load_dword %[d1], %[pc], %[line] offset:64\n\t"
               "s_load_dword %[d2], %[pc], %[line] offset:128\n\t"
               "s_memrealtime %[now]"
               : [kb] "=&s"(kbase), [b2] "=&s"(base2), [pc] "=&s"(pc), [d0] "=&s"(d0), [d1] "=&s"(d1), [d2] "=&s"(d2), [now] "=&s"(now)
               : [ka] "s"(__builtin_amdgcn_kernarg_segment_ptr()), [sel] "s"((wid >> 2) * 8),
                 [line] "s"(min(((blockIdx.x >> 3) * (NW + 1) + wid) * 192, CODE_BYTES - 256))
               : "memory");
  SBAR();
  const unsigned bid = (blockIdx.x & 7) * 32 + (blockIdx.x >> 3);
  const int lane = threadIdx.x & 63;
  const unsigned lds0 = (unsigned)(uintptr_t)lds;
  const unsigned voff = lane * 16;
  const int pair = bid >> 2, split = bid & 3;
  {
    unsigned keep;
#define TA_G4(VO, BASE) \
        "global_load_lds_dwordx4 " VO ", " BASE " nt\n\ts_add_u32 m0, m0, %[pad]\n\ts_nop 0\n\t" \
        "global_load_lds_dwordx4 " VO ", " BASE " offset:1024 nt\n\ts_add_u32 m0, m0, %[pad]\n\ts_nop 0\n\t" \
        "global_load_lds_dwordx4 " VO ", " BASE " offset:2048 nt\n\ts_add_u32 m0, m0, %[pad]\n\ts_nop 0\n\t" \
        "global_load_lds_dwordx4 " VO ", " BASE " offset:3072 nt\n\t"
    asm volatile(
        "s_mov_b32 %[keep], m0\n\ts_mov_b32 m0, %[dst]\n\t"
        "s_waitcnt lgkmcnt(0)\n\t"
        TA_G4("%[vo]", "%[kb]")
        "s_mov_b32 m0, %[dst2]\n\ts_nop 0\n\t"
        TA_G4("%[vo2]", "%[b2]")
        "s_mov_b32 m0, %[keep]"
        : [keep] "=&s"(keep), [kb] "+s"(kbase), [b2] "+s"(base2), [now] "+s"(now), [d0] "+s"(d0), [d1] "+s"(d1), [d2] "+s"(d2)
        : [vo] "v"(bid * WG_BYTES + wid * 4096 + voff),
          [dst] "s"(lds0 + wid * 4 * KP + (wid >> 2) * (SLOT - 16 * KP)),
          [vo2] "v"((wid == NW ? bid * WG_BYTES + 5 * 4096 : pair * 32 * ROWB + wid * 4096) + voff),
          [dst2] "s"(lds0 + (wid == NW ? SLOT + 4 * KP : Q_OFF + wid * 4 * KP)), [pad] "n"(KP - 1024)
        : "memory", "scc");
#undef TA_G4
  }
  const unsigned flags = lds0 + FLAG_OFF;
  if (threadIdx.x < 12) flag_set(flags + lane * 4, lane < 4 ? lane : lane == F_LAND / 4 ? 1 : 0);
  asm volatile("s_waitcnt lgkmcnt(0)\n\ts_barrier" ::: "memory");

  if (wid == NW) {
    __builtin_amdgcn_s_setprio(3);
    const srd_t sK = make_srd((const char*)kbase + (size_t)bid * WG_BYTES, WG_BYTES);
#pragma unroll 1
    for (int g = 2; g < 4; ++g) dma4<KP - 1024>(sK, voff, 16384 + g * 4096, lds0 + SLOT + g * 4 * KP);
    const srd_t sV = make_srd((const char*)vc + (size_t)bid * WG_BYTES, WG_BYTES);
    const unsigned t_in = (unsigned)now;
    Stream st = {2, 2 * 16384, lds0 + 2 * SLOT, t_in + (OFF_NS + 5 * TAU_NS / 2) / 10, 0, 0};
    const unsigned vfor = ((lane >> 3) & 3) == split ? WG_BYTES : voff;
    const unsigned vown = (2 * ((lane >> 3) & 3) + (lane >> 5)) * ROWB + split * 128 + (((lane & 7) ^ ((lane >> 2) & 4)) << 4);
#pragma unroll 1
    while (st.c < 2 * NT) {
      flag_wait4(flags + F_DONE, st.c - NSLOT + 1);
      if (st.c == NT) st.soff = 0;
      const bool isk = st.c < NT, part = st.c >= 2 * NT - ET;
      if (part) st.k = max(st.k, 4u);
      stream_chunks(st, st.c < START_C ? START_C : isk ? NT : part ? 2 * NT : 2 * NT - ET, isk ? sK : sV, part ? vfor : voff,
                    isk ? KP - 1024 : VP - 1024, part ? TAU_NS * 3 / 40 : TAU_NS / 10,
                    st.c < START_C ? TAB_START : part ? TAB_PART : TAB_FULL, lds0, flags + F_LAND, flags + F_DONE + (lane & 3) * 4);
      if (st.c == START_C) st.target += max((int)(st.now - t_in) - START_NS / 10, 0) * 3 / 4;
    }
    constexpr int BEHIND = (NOWN - 3) / 4;
#pragma unroll 1
    for (int u = 0; u < ET; ++u) {
      if ((u & 3) == 0) flag_wait4(flags + F_DONE, 2 * NT + u / 4 - NSLOT + 1);
      dma4_rows(sV, vown, (NT - ET + u) * 16384, st.dst + (u & 3) * 4 * VP, flags + F_LAND,
                2 * NT + (u >= BEHIND ? u - BEHIND : (u - BEHIND) >> 2));
      if ((u & 3) == 3) st.dst = st.dst == lds0 + (NSLOT - 1) * SLOT ? lds0 : st.dst + SLOT;
    }
    wait_vm<16>(); flag_set(flags + F_LAND, 2 * NT + ET - 4);
    wait_vm<0>(); flag_set(flags + F_LAND, 2 * NT + ET);
    return;
  }

  const int w = wid;
  const int r = lane & 31, h = lane >> 5;
  if (w == 3) {
    dma2(make_srd((const char*)kt + (size_t)pair * 16 * ROWB, 16 * ROWB), voff, split * 4 * ROWB, lds0 + TK_OFF);
    dma2(make_srd((const char*)mask + (pair >> 3) * 512, 512), voff, 0, lds0 + MK_OFF);
  }
  if (w == 2) dma2(make_srd((const char*)vt + (size_t)pair * 16 * ROWB, 16 * ROWB), voff, split * 4 * ROWB, lds0 + TV_OFF);
  const int qrow = 2 * (r & 15) + (r >> 4);
  const int koff = (r & 15) * KP + (r >> 4) * 512 + h * 16;
  const int trofs = ((lane >> 4) & 1) * 128 + w * 32 + (lane & 3) * 8;
  const int vofs = (4 * h + ((lane & 15) >> 2)) * VP + trofs;
  if (w == 3) wait_vm<4>(); else if (w == 2) wait_vm<2>(); else wait_vm<0>();
  if (lane == 0) asm volatile("ds_add_u32 %0, %1" ::"v"(flags + F_Q), "v"(1u) : "memory");
  flag_wait(flags + F_Q, NW);
  bf16x8 qf[16];
#pragma unroll
  for (int kk = 0; kk < 16; ++kk) qf[kk] = ld_frag((lds_cptr)lds + Q_OFF + koff + kk * 32);

  const lds_ptr refp = (lds_ptr)lds + REF_OFF + r * 4;
  float m = -INFINITY, l = 0.f;
  if (w == 3) {
    wait_vm<0>();
    const u32x2 mbits = *(const LAS u32x2*)((lds_cptr)lds + MK_OFF + (qrow & 15) * 32 + split * 8);
    const lds_cptr tk = (lds_cptr)lds + TK_OFF + (r & 3) * ROWB + h * 16;
    f32x16 s = f32x16{};
#pragma unroll
    for (int kk = 0; kk < 16; ++kk) s = MFMA32(ld_frag(tk + kk * 32), qf[kk], s);
#pragma unroll
    for (int i = 0; i < 4; ++i) {
      const float mk = __uint_as_float((i & 1) ? (mbits[i >> 1] & 0xffff0000u) : (mbits[i >> 1] << 16));
      s[i] = fmaf(s[i], SCALE, (1.0f - mk) * -1000000000.0f) * LOG2E;
      m = fmaxf(m, s[i]);
    }
#pragma unroll
    for (int i = 0; i < 4; ++i) { s[i] = __builtin_amdgcn_exp2f(s[i] - m); l += s[i]; }
    if (h) l = 0.f;
    *(LAS float*)refp = m;
    *(LAS u32x2*)((lds_ptr)lds + PT_OFF + r * 8) = u32x2{pack2(s[0], s[1]), pack2(s[2], s[3])};
    flag_set(flags + F_REF, 1);
  }

  int slot_off = w * SLOT;
  const lds_ptr pbuf = (lds_ptr)lds + P_OFF + lane * 16;
  bf16x8 preg[NT / 8][2];

  auto k_tile = [&](int c, bf16x8& p0, bf16x8& p1) {
    flag_wait(flags + F_LAND, c + 1);
    const lds_cptr kfrag = (lds_cptr)lds + slot_off + koff;
    f32x16 s = f32x16{};
    bf16x8 kf[8];
#pragma unroll
    for (int kk = 0; kk < 8; ++kk) kf[kk] = ld_frag(kfrag + kk * 32);
    SBAR();
#pragma unroll
    for (int kk = 0; kk < 8; ++kk) { s = MFMA32(kf[kk], qf[kk], s); kf[kk] = ld_frag(kfrag + (kk + 8) * 32); SBAR(); }
    flag_set(flags + F_DONE + w * 4, min(c + NW, NT));
    slot_off = slot_off == 0 ? (NSLOT - 1) * SLOT : slot_off - SLOT;
    SBAR();
#pragma unroll
    for (int kk = 0; kk < 8; ++kk) s = MFMA32(kf[kk], qf[kk + 8], s);
    float tmax = fmaxf(s[0], s[1]);
#pragma unroll
    for (int i = 2; i < 16; ++i) tmax = fmaxf(tmax, s[i]);
    tmax = xhalf_max(tmax) * C2;
    PIN(tmax);
    flag_wait(flags + F_REF, c + 1);
    const float mn = fmaxf(*(const LAS float*)(refp + c * 128), tmax);
    *(LAS float*)(refp + (c + 1) * 128) = mn;
    flag_set(flags + F_REF, c + 2);
    const float alpha = __builtin_amdgcn_exp2f(m - mn);
    m = mn;
    float ps = 0.f;
#pragma unroll
    for (int i = 0; i < 16; ++i) { s[i] = __builtin_amdgcn_exp2f(fmaf(s[i], C2, -mn)); ps += s[i]; }
    l = fmaf(l, alpha, ps);
    p0 = pack8(s, 0); p1 = pack8(s, 8);
    PIN(l); PIN(m);
  };
#pragma unroll 1
  for (int c = w; c < NT / 2; c += NW) {
    bf16x8 p0, p1;
    k_tile(c, p0, p1);
    *(LAS bf16x8*)(pbuf + c * 2048) = p0;
    *(LAS bf16x8*)(pbuf + c * 2048 + 1024) = p1;
  }

  char* msgs = ws + CTRL_BYTES + (size_t)pair * 64 * MSG;
  const srd_t sZ = make_srd(msgs, 64 * MSG);
  const int peer = (split + 1 + (lane & 3) % 3) & 3;
  GAS u64* sig_in = (GAS u64*)(ws + (((pair * 4 + peer) * 4 + split) * 4 + w) * 128);
  GAS u64* sig_out = (GAS u64*)(ws + (((pair * 4 + split) * 4 + peer) * 4 + w) * 128);
  const char* ahead = w == 0 ? (const char*)vc + (size_t)bid * WG_BYTES : w == 1 ? msgs
                    : w == 2 ? (const char*)out + (size_t)pair * 32 * ROWB + split * 128 : msgs + 64 * MSG - 64;
  const unsigned warm = __builtin_nontemporal_load((const GAS unsigned*)ahead + (lane & 1));
  unsigned in0 = __hip_atomic_load((GAS unsigned*)sig_in, RLX_AGENT);
  unsigned out1 = __hip_atomic_load((GAS unsigned*)sig_out, RLX_AGENT) + 1;

#pragma unroll
  for (int t = 0; t < NT / 8; ++t) { k_tile(NT / 2 + 4 * t + w, preg[t][0], preg[t][1]); PIN(preg[t][0]); PIN(preg[t][1]); }
  wait_vm<0>();
  asm volatile("" ::"v"(warm));
  flag_set(flags + F_PRDY + w * 4, 1);
  flag_wait4(flags + F_PRDY, 1);

  f32x16 o[2];
  float rcur = *(const LAS float*)refp;
  {
    const lds_cptr tv = (lds_cptr)lds + TV_OFF + ((lane & 15) >> 2) * ROWB + trofs;
    u32x2 pt2 = *(const LAS u32x2*)((lds_cptr)lds + PT_OFF + r * 8);
    if (h) pt2 = u32x2{0u, 0u};
    const bf16x8 pt = __builtin_bit_cast(bf16x8, u32x4{pt2[0], pt2[1], 0u, 0u});
#pragma unroll
    for (int cb = 0; cb < 2; ++cb) { const s16x4 v4 = ld_tr(tv + cb * 256); o[cb] = MFMA32(join(v4, v4), pt, f32x16{}); }
  }
  const float M = *(const LAS float*)(refp + NT * 128);
  *(LAS float*)((lds_ptr)lds + L_OFF + w * 128 + r * 4) = xhalf_sum(l) * __builtin_amdgcn_exp2f(m - M);
  slot_off = (NT % NSLOT) * SLOT;
  auto v_tiles = [&](int v0, int v1) {
#pragma unroll 1
    for (int v = v0; v < v1; ++v) {
      if (v == 32 || v == 48) flag_wait4(flags + F_PRDY, v >> 4);
      const float rnew = *(const LAS float*)(refp + (v + 1) * 128);
      bf16x8 p0 = ld_frag(pbuf + (v & (NT / 2 - 1)) * 2048), p1 = ld_frag(pbuf + (v & (NT / 2 - 1)) * 2048 + 1024);
      if (__any(rnew != rcur)) {
        const float alpha = __builtin_amdgcn_exp2f(rcur - rnew);
        o[0] *= alpha; o[1] *= alpha;
        rcur = rnew;
      }
      PIN(p0); PIN(p1);
      flag_wait(flags + F_LAND, NT + v + 1);
      const lds_cptr vfrag = (lds_cptr)lds + slot_off + vofs;
      bf16x8 vf[4];
#pragma unroll
      for (int i = 0; i < 4; ++i)
        vf[i] = join(ld_tr(vfrag + (i >> 1) * 512 + (i & 1) * 256), ld_tr(vfrag + 8 * VP + (i >> 1) * 512 + (i & 1) * 256));
      SBAR();
      o[0] = MFMA32(vf[0], p0, o[0]);
      o[1] = MFMA32(vf[1], p0, o[1]);
      SBAR();
      flag_set(flags + F_DONE + w * 4, NT + v + 1);
      slot_off = slot_off == (NSLOT - 1) * SLOT ? 0 : slot_off + SLOT;
      o[0] = MFMA32(vf[2], p1, o[0]);
      o[1] = MFMA32(vf[3], p1, o[1]);
    }
  };
  auto refill = [&](int j0) {
    flag_wait4(flags + F_DONE, NT + 16 + 4 * j0);
#pragma unroll
    for (int j = j0; j < j0 + 4; ++j) {
      *(LAS bf16x8*)(pbuf + (4 * j + w) * 2048) = preg[j][0];
      *(LAS bf16x8*)(pbuf + (4 * j + w) * 2048 + 1024) = preg[j][1];
    }
    flag_set(flags + F_PRDY + w * 4, 2 + j0 / 4);
  };
  v_tiles(0, 20);
  refill(0);
  v_tiles(20, 36);
  refill(4);

  float L = 0.f;
#pragma unroll
  for (int i = 0; i < NW; ++i) L += *(const LAS float*)((lds_cptr)lds + L_OFF + i * 128 + r * 4);
  unsigned zout[4];
#pragma unroll
  for (int j = 0; j < 4; ++j) { zout[j] = (j == split ? 64 : (j * 4 + split) * 4 + w) * MSG + voff; PIN(zout[j]); }
  char* orow = (char*)out + ((size_t)pair * 32 + qrow) * ROWB + split * 128 + w * 32 + h * 16;
  f32x4 mlw = *(const LAS f32x4*)((lds_cptr)lds + ((lane & 8) ? L_OFF : REF_OFF + NT * 128) + (lane & 7) * 16);
  if (lane & 8) {
#pragma unroll
    for (int i = 1; i < NW; ++i) mlw += *(const LAS f32x4*)((lds_cptr)lds + L_OFF + i * 128 + (lane & 7) * 16);
  }
  unsigned mlout = (lane < 48 ? (((split + 1 + (lane >> 4)) & 3) * 4 + split) * 4 + w : 64) * MSG + 1024 + (lane & 15) * 16;
  PIN(L); PIN(orow); PIN(mlw); PIN(mlout); PIN(in0); PIN(out1);
  v_tiles(36, NT - ET);
  float own[8];
  {
    const float f = __builtin_amdgcn_exp2f(rcur - M);
#pragma unroll
    for (int e = 0; e < 8; ++e) {
      const float lo = (split & 1) ? o[0][8 + e] : o[0][e], hi = (split & 1) ? o[1][8 + e] : o[1][e];
      own[e] = f * ((split & 2) ? hi : lo);
      PIN(own[e]);
    }
  }
  v_tiles(NT - ET, NT);

  __builtin_amdgcn_raw_buffer_store_b128(__builtin_bit_cast(u32x4, mlw), sZ, mlout, 0, SC1);
#pragma unroll
  for (int j = 0; j < 4; ++j) {
    u32x4 x;
#pragma unroll
    for (int e = 0; e < 4; ++e) x[e] = pack2(o[j >> 1][8 * (j & 1) + 2 * e], o[j >> 1][8 * (j & 1) + 2 * e + 1]);
    __builtin_amdgcn_raw_buffer_store_b128(x, sZ, zout[j], 0, SC1);
  }

  const int oofs = h * VP + ((lane & 15) >> 2) * 128 + ((w * 32 + (lane & 3) * 8) ^ ((lane & 8) << 3));
  auto own_chunk = [&](int i) {
    float f[4];
    bf16x8 p[4][2], vf[4][2];
    f32x16 t[4];
#pragma unroll
    for (int u = 0; u < 4; ++u) {
      const int v = NT - ET + 4 * i + u;
      f[u] = *(const LAS float*)(refp + (v + 1) * 128);
      p[u][0] = ld_frag(pbuf + (v & (NT / 2 - 1)) * 2048); p[u][1] = ld_frag(pbuf + (v & (NT / 2 - 1)) * 2048 + 1024);
    }
    flag_wait(flags + F_LAND, 2 * NT + 4 * i + 4);
    const lds_cptr vfrag = (lds_cptr)lds + slot_off + oofs;
#pragma unroll
    for (int u = 0; u < 4; ++u) {
      vf[u][0] = join(ld_tr(vfrag + u * 4 * VP), ld_tr(vfrag + u * 4 * VP + 2 * VP));
      vf[u][1] = join(ld_tr(vfrag + u * 4 * VP + 512), ld_tr(vfrag + u * 4 * VP + 2 * VP + 512));
    }
#pragma unroll
    for (int u = 0; u < 4; ++u) { f[u] = __builtin_amdgcn_exp2f(f[u] - M); PIN(f[u]); }
    SBAR();
#pragma unroll
    for (int u = 0; u < 4; ++u) t[u] = MFMA32(vf[u][1], p[u][1], MFMA32(vf[u][0], p[u][0], f32x16{}));
#pragma unroll
    for (int u = 0; u < 4; ++u)
#pragma unroll
      for (int e = 0; e < 8; ++e) own[e] = fmaf(f[u], t[u][e], own[e]);
#pragma unroll
    for (int e = 0; e < 8; ++e) PIN(own[e]);
    slot_off = slot_off == (NSLOT - 1) * SLOT ? 0 : slot_off + SLOT;
  };
#pragma unroll 1
  for (int i = 0; i < 2; ++i) {
    if (i == 1) {
      wait_vm<0>();
      if (lane < 3) __hip_atomic_store(sig_out, ((u64)in0 << 32) | out1, RLX_AGENT);
    }
    own_chunk(i);
  }
  {
    unsigned got;
    int spin = 0;
#pragma unroll 1
    do got = __hip_atomic_load((GAS unsigned*)sig_in, RLX_AGENT);
    while (((unsigned)__ballot(got != in0) & 7u) != 7u && ++spin < MEM_SPINS);
  }
  __builtin_amdgcn_fence(__ATOMIC_ACQUIRE, "wavefront");
  asm volatile("" ::: "memory");
  u32x4 a[3];
  unsigned ml[3];
#pragma unroll
  for (int k = 0; k < 3; ++k) {
    const unsigned zin = ((split * 4 + ((split + 1 + k) & 3)) * 4 + w) * MSG;
    ml[k] = __builtin_amdgcn_raw_buffer_load_b32(sZ, 1024 + lane * 4, zin, SC1);
    a[k] = __builtin_amdgcn_raw_buffer_load_b128(sZ, voff, zin, SC1);
  }
  const unsigned peer_view = __hip_atomic_load((GAS unsigned*)sig_in + 1, RLX_AGENT);
  own_chunk(2);
  float w0, rest[8];
  {
    float mk[3], lk[3], wk[3], G = M;
#pragma unroll
    for (int k = 0; k < 3; ++k) {
      auto x = __builtin_amdgcn_permlane32_swap(ml[k], ml[k], false, false);
      mk[k] = __uint_as_float(x[0]); lk[k] = __uint_as_float(x[1]);
      G = fmaxf(G, mk[k]);
    }
    w0 = __builtin_amdgcn_exp2f(M - G);
    float den = w0 * L;
#pragma unroll
    for (int k = 0; k < 3; ++k) { wk[k] = __builtin_amdgcn_exp2f(mk[k] - G); den = fmaf(wk[k], lk[k], den); }
    const float inv = 1.0f / den;
    w0 *= inv;
#pragma unroll
    for (int e = 0; e < 8; ++e) rest[e] = 0.f;
#pragma unroll
    for (int k = 0; k < 3; ++k)
#pragma unroll
      for (int e = 0; e < 4; ++e) {
        rest[2 * e] = fmaf(wk[k], __uint_as_float(a[k][e] << 16), rest[2 * e]);
        rest[2 * e + 1] = fmaf(wk[k], __uint_as_float(a[k][e] & 0xffff0000u), rest[2 * e + 1]);
      }
#pragma unroll
    for (int e = 0; e < 8; ++e) { rest[e] *= inv; PIN(rest[e]); }
    PIN(w0);
  }
#pragma unroll
  for (int e = 0; e < 8; ++e) own[e] = fmaf(own[e], w0, rest[e]);
  auto x = __builtin_amdgcn_permlane32_swap(pack2(own[0], own[1]), pack2(own[4], own[5]), false, false);
  auto y = __builtin_amdgcn_permlane32_swap(pack2(own[2], own[3]), pack2(own[6], own[7]), false, false);
  *(GAS u32x4*)orow = u32x4{x[0], y[0], x[1], y[1]};
  if (lane < 3 && peer_view == out1) __hip_atomic_store(sig_out, ((u64)in0 << 32) | (out1 + 1), RLX_AGENT);
}

}

extern "C" void kernel_launch(void* const* d_in, const int* in_sizes, int n_in, void* d_out, int out_size, void* d_ws,
                              size_t ws_size, hipStream_t stream) {
  using namespace ta;
  (void)in_sizes; (void)n_in; (void)out_size; (void)ws_size;
  (void)hipFuncSetAttribute((const void*)tree_attn, hipFuncAttributeMaxDynamicSharedMemorySize, LDS_BYTES);
  hipLaunchKernelGGL(tree_attn, dim3(NPAIR * NSPLIT), dim3((NW + 1) * 64), LDS_BYTES, stream,
                     (const __hip_bfloat16*)d_in[0], (const __hip_bfloat16*)d_in[1], (const __hip_bfloat16*)d_in[2],
                     (const __hip_bfloat16*)d_in[3], (const __hip_bfloat16*)d_in[4], (const __hip_bfloat16*)d_in[5],
                     (char*)d_ws, (__hip_bfloat16*)d_out);
}
```
